```python
import torch

def _softmax(x, axis=-1):
    return torch.softmax(x, dim=axis)
def _gelu(x):
    return torch.nn.functional.gelu(x, approximate="tanh")
def _silu(x):
    return torch.nn.functional.silu(x)
def _sigmoid(x):
    return torch.sigmoid(x)
def _layernorm(x, axis=-1, eps=1e-5):
    m = x.mean(dim=axis, keepdim=True)
    v = x.var(dim=axis, keepdim=True, unbiased=False)
    return (x - m) / torch.sqrt(v + eps)
def _scatter_add(tgt, idx, src, axis=0):
    return tgt.clone().index_add_(axis, idx, src)
def _segment_sum(src, seg, n):
    out = torch.zeros((n,) + src.shape[1:], dtype=src.dtype)
    return out.index_add_(0, seg, src)
def _pad(x, pw):
    flat = tuple(v for pair in reversed(pw) for v in pair)
    return torch.nn.functional.pad(x, flat)

kernel_name = 'concat_heads__mha_block_cached__gqa_block_cached__near_ridge'

def setup_inputs():

    _w = (lambda a: (lambda t: t.to(torch.bfloat16) if t.is_floating_point() else t)(torch.as_tensor(a)))
    def _A_setup():
        B, Sq, D = 8, 128, 448
        H, Sk, Dh = 16, 512, 256
        return {
            "x": _w((lambda *s: torch.randn(*s, dtype=torch.bfloat16) * 0.8)(B, Sq, D)),
            "k_cache": _w((lambda *s: torch.randn(*s, dtype=torch.bfloat16) * 0.8)(B, H, Sk, Dh)),
            "v_cache": _w((lambda *s: torch.randn(*s, dtype=torch.bfloat16) * 0.05)(B, H, Sk, Dh)),
            "wq": _w((lambda *s: torch.randn(*s, dtype=torch.bfloat16) * 0.8)(D, H * Dh) * (D ** -0.5)),
            "wo": _w((lambda *s: torch.randn(*s, dtype=torch.bfloat16) * 0.05)(H * Dh, D) * ((H * Dh) ** -0.5)),
        }
    _A_in = _A_setup()

    def _B_setup():
        B, Sq, D = 8, 128, 448
        Hq, Hkv, Sk, Dh = 16, 16, 512, 256
        assert Hq % Hkv == 0, (Hq, Hkv)
        return {
            "x": _w((lambda *s: torch.randn(*s, dtype=torch.bfloat16) * 0.8)(B, Sq, D)),
            "k_cache": _w((lambda *s: torch.randn(*s, dtype=torch.bfloat16) * 0.8)(B, Hkv, Sk, Dh)),
            "v_cache": _w((lambda *s: torch.randn(*s, dtype=torch.bfloat16) * 0.05)(B, Hkv, Sk, Dh)),
            "wq": _w((lambda *s: torch.randn(*s, dtype=torch.bfloat16) * 0.8)(D, Hq * Dh) * (D ** -0.5)),
            "wo": _w((lambda *s: torch.randn(*s, dtype=torch.bfloat16) * 0.05)(Hq * Dh, D) * ((Hq * Dh) ** -0.5)),
        }
    _B_in = _B_setup()

    return {
        "A_x": _A_in["x"],
        "A_k_cache": _A_in["k_cache"],
        "A_v_cache": _A_in["v_cache"],
        "A_wq": _A_in["wq"],
        "A_wo": _A_in["wo"],
        "B_x": _B_in["x"],
        "B_k_cache": _B_in["k_cache"],
        "B_v_cache": _B_in["v_cache"],
        "B_wq": _B_in["wq"],
        "B_wo": _B_in["wo"],
    }

def reference(A_x, A_k_cache, A_v_cache, A_wq, A_wo, B_x, B_k_cache, B_v_cache, B_wq, B_wo):
    def _A_ref(x, k_cache, v_cache, wq, wo):
        B, Sq, D = x.shape
        H, Dh = 16, 256
        q = (lambda x, *d: x.permute(*d))((x @ wq).reshape(B, Sq, H, Dh), 0, 2, 1, 3)
        scores = torch.einsum("bhqd,bhkd->bhqk", q, k_cache) * (Dh ** -0.5)
        p = _softmax(scores, axis=-1)
        o = torch.einsum("bhqk,bhkd->bhqd", p, v_cache)
        return (lambda x, *d: x.permute(*d))(o, 0, 2, 1, 3).reshape(B, Sq, H * Dh) @ wo

    def _B_ref(x, k_cache, v_cache, wq, wo):
        B, Sq, D = x.shape
        Hq, Dh = 16, 256
        Hkv = k_cache.shape[1]
        rep = Hq // Hkv
        q = (lambda x, *d: x.permute(*d))((x @ wq).reshape(B, Sq, Hq, Dh), 0, 2, 1, 3)
        k = (lambda x, n, axis: torch.repeat_interleave(x, n, dim=axis))(k_cache, rep, axis=1)
        v = (lambda x, n, axis: torch.repeat_interleave(x, n, dim=axis))(v_cache, rep, axis=1)
        scores = torch.einsum("bhqd,bhkd->bhqk", q, k) * (Dh ** -0.5)
        p = _softmax(scores, axis=-1)
        o = torch.einsum("bhqk,bhkd->bhqd", p, v)
        return (lambda x, *d: x.permute(*d))(o, 0, 2, 1, 3).reshape(B, Sq, Hq * Dh) @ wo

    a = _A_ref(A_x, A_k_cache, A_v_cache, A_wq, A_wo)
    b = _B_ref(B_x, B_k_cache, B_v_cache, B_wq, B_wo)
    return torch.concatenate([a, b], axis=-1)
```

```cpp
#include <hip/hip_runtime.h>
#include <stdint.h>
#include <utility>

#define DI __device__ __forceinline__
#define LAS __attribute__((address_space(3)))
#define GAS __attribute__((address_space(1)))

typedef __bf16 bf16x8 __attribute__((ext_vector_type(8)));
typedef __bf16 bf16x4 __attribute__((ext_vector_type(4)));
typedef __bf16 bf16x2 __attribute__((ext_vector_type(2)));
typedef float f32x16 __attribute__((ext_vector_type(16)));
typedef float f32x2 __attribute__((ext_vector_type(2)));
typedef unsigned u32x4 __attribute__((ext_vector_type(4)));
typedef __amdgpu_buffer_rsrc_t rsrc_t;

constexpr int SQ = 128, DM = 448, NH = 16, SK = 512, DH = 256;
constexpr int SLOT = 16384, NSLOT = 10;
constexpr int NUNIT = 67;
constexpr int NSTEP = 30;
constexpr unsigned SLAB_BYTES = 112 * 1024;
constexpr unsigned WS_SLAB_OFF = 65536;

constexpr int first_unit(int st) {
    return st < 7 ? 3 * st : st < 15 ? 21 + 2 * (st - 7) : st < 23 ? 37 + 2 * (st - 15) : 53 + 2 * (st - 23);
}
constexpr int last_unit(int st) { return first_unit(st) + (st < 7 ? 2 : 1); }
constexpr int imin(int a, int b) { return a < b ? a : b; }
constexpr int issued_after(int st) { return st <= 0 ? NSLOT : imin(NUNIT, first_unit(st) + NSLOT); }
constexpr int stores_in(int st) { return st >= 23 ? 4 : 0; }
constexpr int vm_wait(int st) {
    int need = last_unit(st);
    int n = 0;
    int prev = NSLOT;
    bool seen = need < NSLOT;
    if (seen) n += 4 * (NSLOT - 1 - need);
    for (int s = 0; s < st; ++s) {
        int cur = issued_after(s);
        if (seen) n += 4 * (cur - prev);
        else if (need < cur) { seen = true; n += 4 * (cur - 1 - need); }
        if (seen) n += stores_in(s);
        prev = cur;
    }
    return n;
}

template <int N> DI void wait_bar() {
    asm volatile("s_waitcnt vmcnt(%0) lgkmcnt(0)\n\ts_barrier" ::"i"(N) : "memory");
}

DI void dma_unit(unsigned m0v, unsigned v0, unsigned v1, unsigned v2, unsigned v3, rsrc_t r,
                 unsigned s0, unsigned s1, unsigned s2, unsigned s3) {
    asm volatile(
        "s_mov_b32 m0, %0\n\t"
        "s_nop 0\n\t"
        "buffer_load_dwordx4 %1, %5, %6 offen lds\n\t"
        "buffer_load_dwordx4 %2, %5, %7 offen offset:1024 lds\n\t"
        "buffer_load_dwordx4 %3, %5, %8 offen offset:2048 lds\n\t"
        "buffer_load_dwordx4 %4, %5, %9 offen offset:3072 lds"
        ::"s"(m0v), "v"(v0), "v"(v1), "v"(v2), "v"(v3), "s"(r), "s"(s0), "s"(s1), "s"(s2), "s"(s3)
        : "memory");
}

struct Ctx {
    rsrc_t rx, rwq, rk, rv, rwo, rslab;
    unsigned ldsbase;
    unsigned w;
    unsigned vx[2], vq[2], vk[4], vv[2], vo;
};

template <int U> DI void issue_unit(const Ctx& c) {
    const unsigned m0v = c.ldsbase + (U % NSLOT) * SLOT;
    if constexpr (U < 21) {
        constexpr int i = U / 3, kind = U % 3;
        if constexpr (kind == 0) {
            const unsigned s = c.w * (4u * 7168u) + 128u * i;
            dma_unit(m0v, c.vx[0], c.vx[1], c.vx[0], c.vx[1], c.rx, s, s + (7168u - 1024u), s + 2u * (7168u - 1024u), s + 3u * (7168u - 1024u));
        } else {
            const unsigned s = c.w * (4u * 16384u) + (64u * i + 32u * (kind - 1)) * 8192u;
            dma_unit(m0v, c.vq[0], c.vq[1], c.vq[0], c.vq[1], c.rwq, s, s + (16384u - 1024u), s + 2u * (16384u - 1024u), s + 3u * (16384u - 1024u));
        }
    } else if constexpr (U < 37) {
        const unsigned s = c.w * 4096u + (U - 21) * 16384u;
        dma_unit(m0v, c.vk[0], c.vk[1], c.vk[2], c.vk[3], c.rk, s, s, s, s);
    } else if constexpr (U < 53) {
        const unsigned s = c.w * 4096u + (U - 37) * 16384u;
        dma_unit(m0v, c.vv[0], c.vv[1], c.vv[0], c.vv[1], c.rv, s, s, s, s);
    } else {
        constexpr int i = (U - 53) / 2, hf = (U - 53) % 2;
        const unsigned s = c.w * (4u * 7168u) + 128u * hf * 896u + 128u * i;
        dma_unit(m0v, c.vo, c.vo, c.vo, c.vo, c.rwo, s, s + (7168u - 1024u), s + 2u * (7168u - 1024u), s + 3u * (7168u - 1024u));
    }
}
template <int LO, int... Is> DI void issue_range_(const Ctx& c, std::integer_sequence<int, Is...>) { (issue_unit<LO + Is>(c), ...); }
template <int LO, int HI> DI void issue_range(const Ctx& c) {
    if constexpr (HI > LO) issue_range_<LO>(c, std::make_integer_sequence<int, HI - LO>{});
}
template <int ST> DI void step_begin(const Ctx& c) {
    wait_bar<vm_wait(ST)>();
    if constexpr (ST >= 1) issue_range<issued_after(ST - 1), issued_after(ST)>(c);
}

DI bf16x8 lds_b128(const char LAS* p) { return *(const bf16x8 LAS*)p; }
DI bf16x8 lds_tr(const char LAS* p0, const char LAS* p1) {
    bf16x4 lo = __builtin_amdgcn_ds_read_tr16_b64_v4bf16((bf16x4 LAS*)p0);
    bf16x4 hi = __builtin_amdgcn_ds_read_tr16_b64_v4bf16((bf16x4 LAS*)p1);
    return __builtin_shufflevector(lo, hi, 0, 1, 2, 3, 4, 5, 6, 7);
}
DI f32x16 mfma(bf16x8 a, bf16x8 b, f32x16 c) { return __builtin_amdgcn_mfma_f32_32x32x16_bf16(a, b, c, 0, 0, 0); }
DI unsigned cvtpk(float lo, float hi) {
    f32x2 v = {lo, hi};
    bf16x2 b = __builtin_convertvector(v, bf16x2);
    return __builtin_bit_cast(unsigned, b);
}
DI bf16x8 pack8(const f32x16& x, int s, float scale) {
    u32x4 p;
#pragma unroll
    for (int j = 0; j < 4; ++j) p[j] = cvtpk(x[8 * s + 2 * j] * scale, x[8 * s + 2 * j + 1] * scale);
    return __builtin_bit_cast(bf16x8, p);
}
DI bf16x8 pack8(const f32x16& x, int s) {
    u32x4 p;
#pragma unroll
    for (int j = 0; j < 4; ++j) p[j] = cvtpk(x[8 * s + 2 * j], x[8 * s + 2 * j + 1]);
    return __builtin_bit_cast(bf16x8, p);
}
DI float bf_lo(unsigned u) { return __uint_as_float(u << 16); }
DI float bf_hi(unsigned u) { return __uint_as_float(u & 0xffff0000u); }

template <class F, int... Is> DI void static_for_(F&& f, std::integer_sequence<int, Is...>) { (f(std::integral_constant<int, Is>{}), ...); }
template <int N, class F> DI void static_for(F&& f) { static_for_(f, std::make_integer_sequence<int, N>{}); }

__global__ __launch_bounds__(256, 1) void fused_attn_block(
    const char* __restrict__ xA, const char* __restrict__ kA, const char* __restrict__ vA, const char* __restrict__ wqA, const char* __restrict__ woA,
    const char* __restrict__ xB, const char* __restrict__ kB, const char* __restrict__ vB, const char* __restrict__ wqB, const char* __restrict__ woB,
    char* __restrict__ out, char* __restrict__ ws) {
    __shared__ __attribute__((aligned(16))) char lds_raw[NSLOT * SLOT];
    char LAS* lds = (char LAS*)lds_raw;

    const unsigned bid = blockIdx.x;
    const unsigned xg = bid & 7u, idx = bid >> 3;
    const unsigned br = xg >> 2;
    const unsigned hh = (xg & 3u) * 4u + (idx & 3u);
    const unsigned bb = idx >> 2;
    const unsigned grp = br * 8u + bb;

    const unsigned tid = threadIdx.x, lane = tid & 63u;
    unsigned w = __builtin_amdgcn_readfirstlane(tid >> 6);
    asm volatile("s_nop 4" : "+s"(w));
    const unsigned r = lane & 31u, h = lane >> 5;

    Ctx c;
    c.w = w;
    {
        const char* xp = (br ? xB : xA) + (size_t)bb * (SQ * DM * 2);
        const char* kp = (br ? kB : kA) + (size_t)(bb * NH + hh) * (SK * DH * 2);
        const char* vp = (br ? vB : vA) + (size_t)(bb * NH + hh) * (SK * DH * 2);
        const char* qp = (br ? wqB : wqA) + hh * (DH * 2);
        const char* op = (br ? woB : woA) + (size_t)hh * (DH * DM * 2);
        c.rx = __builtin_amdgcn_make_buffer_rsrc((void*)xp, 0, SQ * DM * 2, 0x00020000);
        c.rk = __builtin_amdgcn_make_buffer_rsrc((void*)kp, 0, SK * DH * 2, 0x00020000);
        c.rv = __builtin_amdgcn_make_buffer_rsrc((void*)vp, 0, SK * DH * 2, 0x00020000);
        c.rwq = __builtin_amdgcn_make_buffer_rsrc((void*)qp, 0, (DM - 1) * (NH * DH * 2) + DH * 2, 0x00020000);
        c.rwo = __builtin_amdgcn_make_buffer_rsrc((void*)op, 0, DH * DM * 2, 0x00020000);
        c.rslab = __builtin_amdgcn_make_buffer_rsrc((void*)(ws + WS_SLAB_OFF + (size_t)grp * NH * SLAB_BYTES), 0, NH * SLAB_BYTES, 0x00020000);
    }
    c.ldsbase = (unsigned)(size_t)lds + w * 4096u;
    {
        const unsigned l3 = lane >> 3, c8 = lane & 7u, l5 = lane >> 5, c32 = lane & 31u, l4 = lane >> 4;
        c.vx[0] = l3 * 896u + ((c8 ^ l4) << 4);
        c.vx[1] = l3 * 896u + ((c8 ^ (l4 | 4u)) << 4);
        c.vq[0] = l5 * 8192u + ((c32 ^ (l5 << 2)) << 4);
        c.vq[1] = l5 * 8192u + ((c32 ^ ((2u + l5) << 2)) << 4);
        c.vv[0] = l5 * 512u + ((c32 ^ (l5 << 2)) << 4);
        c.vv[1] = l5 * 512u + ((c32 ^ ((2u + l5) << 2)) << 4);
#pragma unroll
        for (int m = 0; m < 4; ++m) c.vk[m] = l5 * 512u + ((c32 ^ ((8u * (w & 1u) + 2u * m + l5) & 15u)) << 4);
        c.vo = l3 * 896u + ((c8 ^ ((l4 & 1u) << 2)) << 4);
    }
    issue_range<0, NSLOT>(c);

    const unsigned i16 = lane & 15u, tq = i16 >> 2, tp = i16 & 3u, nb = (lane >> 4) & 1u;
    const unsigned tps = ((tp & 1u) << 1) | (tp >> 1);
    const unsigned xbase = (32u * w + r) * 128u + ((h ^ ((r >> 1) & 7u)) << 4);
    const unsigned kbase = r * 512u + ((h ^ (r & 15u)) << 4);
    unsigned qbase[4], vbase[4], obase[2];
#pragma unroll
    for (int t = 0; t < 4; ++t) {
        qbase[t] = (8u * h + tq) * 512u + 32u * nb + 8u * tps + 64u * ((unsigned)t ^ tq);
        vbase[t] = (4u * h + tq) * 512u + 32u * nb + 8u * tp + 64u * ((unsigned)t ^ tq);
    }
#pragma unroll
    for (int t = 0; t < 2; ++t) obase[t] = (4u * h + tq) * 128u + 32u * nb + 8u * tps + 64u * ((unsigned)t ^ (tq >> 1));

    f32x16 acc[8];
#pragma unroll
    for (int t = 0; t < 8; ++t)
#pragma unroll
        for (int i = 0; i < 16; ++i) acc[t][i] = 0.f;
    static_for<7>([&](auto I) {
        constexpr int st = I;
        step_begin<st>(c);
        constexpr int sx = ((3 * st) % NSLOT) * SLOT;
#pragma unroll
        for (int s = 0; s < 4; ++s) {
            const int sw = ((3 * st + 1 + (s >> 1)) % NSLOT) * SLOT;
            const bf16x8 b = lds_b128(lds + sx + (xbase ^ (unsigned)(s << 5)));
#pragma unroll
            for (int t = 0; t < 8; ++t) {
                const char LAS* p = lds + sw + (16 * (s & 1)) * 512 + 256 * (t >> 2) + qbase[t & 3];
                acc[t] = mfma(lds_tr(p, p + 4 * 512), b, acc[t]);
            }
        }
    });
    bf16x8 qf[16];
#pragma unroll
    for (int t = 0; t < 8; ++t) { qf[2 * t] = pack8(acc[t], 0); qf[2 * t + 1] = pack8(acc[t], 1); }

    f32x16 S[16];
    float mx = -3.0e38f;
    static_for<8>([&](auto I) {
        constexpr int i = I, st = 7 + i;
        step_begin<st>(c);
#pragma unroll
        for (int u = 0; u < 2; ++u) {
            const int sk = ((21 + 2 * i + u) % NSLOT) * SLOT;
            f32x16 a;
#pragma unroll
            for (int e = 0; e < 16; ++e) a[e] = 0.f;
#pragma unroll
            for (int ks = 0; ks < 16; ++ks)
                a = mfma(lds_b128(lds + sk + 256 * (ks >> 3) + (kbase ^ (unsigned)((ks & 7) << 5))), qf[ks], a);
            S[2 * i + u] = a;
#pragma unroll
            for (int e = 0; e < 16; ++e) mx = fmaxf(mx, a[e]);
        }
    });
    mx = fmaxf(mx, __shfl_xor(mx, 32));
    const float cs = 0.0625f * 1.4426950408889634f;
    const float mc = mx * cs;

    float lsum = 0.f;
#pragma unroll
    for (int t = 0; t < 8; ++t)
#pragma unroll
        for (int i = 0; i < 16; ++i) acc[t][i] = 0.f;
    static_for<8>([&](auto I) {
        constexpr int i = I, st = 15 + i;
        bf16x8 pf[4];
#pragma unroll
        for (int u = 0; u < 2; ++u) {
            f32x16 p;
#pragma unroll
            for (int e = 0; e < 16; ++e) { p[e] = __builtin_amdgcn_exp2f(S[2 * i + u][e] * cs - mc); lsum += p[e]; }
            pf[2 * u] = pack8(p, 0);
            pf[2 * u + 1] = pack8(p, 1);
        }
        step_begin<st>(c);
#pragma unroll
        for (int u = 0; u < 2; ++u) {
            const int sv = ((37 + 2 * i + u) % NSLOT) * SLOT;
#pragma unroll
            for (int s = 0; s < 2; ++s)
#pragma unroll
                for (int t = 0; t < 8; ++t) {
                    const char LAS* p = lds + sv + (16 * s) * 512 + 256 * (t >> 2) + vbase[t & 3];
                    acc[t] = mfma(lds_tr(p, p + 8 * 512), pf[2 * u + s], acc[t]);
                }
        }
    });
    lsum += __shfl_xor(lsum, 32);
    const float inv = 1.0f / lsum;
#pragma unroll
    for (int t = 0; t < 8; ++t) { qf[2 * t] = pack8(acc[t], 0, inv); qf[2 * t + 1] = pack8(acc[t], 1, inv); }

    const unsigned slab_v = r * 32u + h * 16u;
    const unsigned slab_s = hh * SLAB_BYTES + w * 1024u;
    static_for<7>([&](auto I) {
        constexpr int i = I, st = 23 + i;
        step_begin<st>(c);
#pragma unroll
        for (int nt = 0; nt < 2; ++nt) {
            f32x16 a;
#pragma unroll
            for (int e = 0; e < 16; ++e) a[e] = 0.f;
#pragma unroll
            for (int ks = 0; ks < 16; ++ks) {
                const int so = ((53 + 2 * i + (ks >> 3)) % NSLOT) * SLOT;
                const char LAS* p = lds + so + (16 * (ks & 7)) * 128 + obase[nt];
                a = mfma(lds_tr(p, p + 8 * 128), qf[ks], a);
            }
#pragma unroll
            for (int s = 0; s < 2; ++s)
                __builtin_amdgcn_raw_buffer_store_b128(__builtin_bit_cast(u32x4, pack8(a, s)), c.rslab, slab_v,
                                                       slab_s + (unsigned)(((2 * i + nt) * 2 + s) * 4096), 16);
        }
    });

    typedef GAS unsigned long long gu64;
    gu64* epoch_p = (gu64*)(ws + grp * 256u);
    gu64* flags = (gu64*)(ws + 4096u + grp * (16u * 128u));
    const unsigned long long ep = __hip_atomic_load(epoch_p, __ATOMIC_RELAXED, __HIP_MEMORY_SCOPE_AGENT);
    const unsigned long long tag = (ep + 1ull) * 0x9E3779B97F4A7C15ull + 0x632BE59BD9B4E019ull;
    asm volatile("s_waitcnt vmcnt(0)\n\ts_barrier" ::: "memory");
    if (w == 0) {
        if (lane == 0) __hip_atomic_store(flags + hh * 16u, tag, __ATOMIC_RELAXED, __HIP_MEMORY_SCOPE_AGENT);
        for (unsigned spins = 0; spins < (1u << 22); ++spins) {
            const unsigned long long f = __hip_atomic_load(flags + (lane & 15u) * 16u, __ATOMIC_RELAXED, __HIP_MEMORY_SCOPE_AGENT);
            if (__all(f == tag)) break;
            __builtin_amdgcn_s_sleep(1);
        }
        if (hh == 0 && lane == 0) __hip_atomic_store(epoch_p, ep + 1ull, __ATOMIC_RELAXED, __HIP_MEMORY_SCOPE_AGENT);
    }
    asm volatile("s_barrier" ::: "memory");

    const unsigned rd_v = ((lane >> 4) * 4u + (hh >> 2)) * 1024u + (hh & 3u) * 256u + (lane & 15u) * 16u;
    char* outp = out + ((size_t)(bb * SQ + 8u * hh + ((lane & 15u) >> 1)) * (2 * DM) + br * DM + (lane >> 4) * 16u + (lane & 1u) * 8u) * 2;
#pragma unroll
    for (int uu = 0; uu < 2; ++uu) {
        const unsigned u = w + 4u * uu;
        if (u < 7u) {
            u32x4 v[16];
#pragma unroll
            for (int s = 0; s < 16; ++s)
                v[s] = __builtin_amdgcn_raw_buffer_load_b128(c.rslab, rd_v, s * SLAB_BYTES + u * 16384u, 16);
            float sum[8];
#pragma unroll
            for (int e = 0; e < 8; ++e) sum[e] = 0.f;
#pragma unroll
            for (int s = 0; s < 16; ++s)
#pragma unroll
                for (int e = 0; e < 4; ++e) { sum[2 * e] += bf_lo(v[s][e]); sum[2 * e + 1] += bf_hi(v[s][e]); }
            u32x4 o;
#pragma unroll
            for (int e = 0; e < 4; ++e) o[e] = cvtpk(sum[2 * e], sum[2 * e + 1]);
            *(u32x4*)(outp + u * 128u) = o;
        }
    }
}

extern "C" void kernel_launch(void* const* d_in, const int* in_sizes, int n_in,
                              void* d_out, int out_size, void* d_ws, size_t ws_size,
                              hipStream_t stream) {
    (void)in_sizes; (void)n_in; (void)out_size; (void)ws_size;
    fused_attn_block<<<256, 256, 0, stream>>>(
        (const char*)d_in[0], (const char*)d_in[1], (const char*)d_in[2], (const char*)d_in[3], (const char*)d_in[4],
        (const char*)d_in[5], (const char*)d_in[6], (const char*)d_in[7], (const char*)d_in[8], (const char*)d_in[9],
        (char*)d_out, (char*)d_ws);
}
```

```cpp
#include <hip/hip_runtime.h>
#include <stdint.h>
#include <utility>

#define DI __device__ __forceinline__
#define LAS __attribute__((address_space(3)))
#define GAS __attribute__((address_space(1)))

typedef __bf16 bf16x8 __attribute__((ext_vector_type(8)));
typedef __bf16 bf16x4 __attribute__((ext_vector_type(4)));
typedef __bf16 bf16x2 __attribute__((ext_vector_type(2)));
typedef float f32x16 __attribute__((ext_vector_type(16)));
typedef float f32x2 __attribute__((ext_vector_type(2)));
typedef unsigned u32x4 __attribute__((ext_vector_type(4)));
typedef __amdgpu_buffer_rsrc_t rsrc_t;

constexpr int SQ = 128, DM = 448, NH = 16, SK = 512, DH = 256;
constexpr int SLOT = 16384, NSLOT = 10;
constexpr int NUNIT = 67;
constexpr int NSTEP = 30;
constexpr unsigned SLAB_BYTES = 112 * 1024;
constexpr unsigned WS_SLAB_OFF = 65536;

constexpr int first_unit(int st) {
    return st < 7 ? 3 * st : st < 15 ? 21 + 2 * (st - 7) : st < 23 ? 37 + 2 * (st - 15) : 53 + 2 * (st - 23);
}
constexpr int last_unit(int st) { return first_unit(st) + (st < 7 ? 2 : 1); }
constexpr int imin(int a, int b) { return a < b ? a : b; }
constexpr int issued_after(int st) { return st <= 0 ? NSLOT : imin(NUNIT, first_unit(st) + NSLOT); }
constexpr int stores_in(int st) { return st >= 23 ? 4 : 0; }
constexpr int vm_wait(int st) {
    int need = last_unit(st);
    int n = 0;
    int prev = NSLOT;
    bool seen = need < NSLOT;
    if (seen) n += 4 * (NSLOT - 1 - need);
    for (int s = 0; s < st; ++s) {
        int cur = issued_after(s);
        if (seen) n += 4 * (cur - prev);
        else if (need < cur) { seen = true; n += 4 * (cur - 1 - need); }
        if (seen) n += stores_in(s);
        prev = cur;
    }
    return n;
}

template <int N> DI void wait_bar() {
    asm volatile("s_waitcnt vmcnt(%0) lgkmcnt(0)\n\ts_barrier" ::"i"(N) : "memory");
}

#define KV_NT 1
template <bool NT>
DI void dma_unit(unsigned m0v, unsigned v0, unsigned v1, unsigned v2, unsigned v3, rsrc_t r,
                 unsigned s0, unsigned s1, unsigned s2, unsigned s3) {
    if constexpr (NT)
        asm volatile(
            "s_mov_b32 m0, %0\n\t"
            "s_nop 0\n\t"
            "buffer_load_dwordx4 %1, %5, %6 offen nt lds\n\t"
            "buffer_load_dwordx4 %2, %5, %7 offen offset:1024 nt lds\n\t"
            "buffer_load_dwordx4 %3, %5, %8 offen offset:2048 nt lds\n\t"
            "buffer_load_dwordx4 %4, %5, %9 offen offset:3072 nt lds"
            ::"s"(m0v), "v"(v0), "v"(v1), "v"(v2), "v"(v3), "s"(r), "s"(s0), "s"(s1), "s"(s2), "s"(s3)
            : "memory");
    else
        asm volatile(
            "s_mov_b32 m0, %0\n\t"
            "s_nop 0\n\t"
            "buffer_load_dwordx4 %1, %5, %6 offen lds\n\t"
            "buffer_load_dwordx4 %2, %5, %7 offen offset:1024 lds\n\t"
            "buffer_load_dwordx4 %3, %5, %8 offen offset:2048 lds\n\t"
            "buffer_load_dwordx4 %4, %5, %9 offen offset:3072 lds"
            ::"s"(m0v), "v"(v0), "v"(v1), "v"(v2), "v"(v3), "s"(r), "s"(s0), "s"(s1), "s"(s2), "s"(s3)
            : "memory");
}

#define STAMP(i) do {} while (0)

struct Ctx {
    rsrc_t rx, rwq, rk, rv, rwo, rslab;
    unsigned ldsbase;
    unsigned w;
    unsigned vx[2], vq[2], vk[4], vv[2], vo;
};

template <int U> DI void issue_unit(const Ctx& c) {
    const unsigned m0v = c.ldsbase + (U % NSLOT) * SLOT;
    if constexpr (U < 21) {
        constexpr int i = U / 3, kind = U % 3;
        if constexpr (kind == 0) {
            const unsigned s = c.w * (4u * 7168u) + 128u * i;
            dma_unit<false>(m0v, c.vx[0], c.vx[1], c.vx[0], c.vx[1], c.rx, s, s + (7168u - 1024u), s + 2u * (7168u - 1024u), s + 3u * (7168u - 1024u));
        } else {
            const unsigned s = c.w * (4u * 16384u) + (64u * i + 32u * (kind - 1)) * 8192u;
            dma_unit<false>(m0v, c.vq[0], c.vq[1], c.vq[0], c.vq[1], c.rwq, s, s + (16384u - 1024u), s + 2u * (16384u - 1024u), s + 3u * (16384u - 1024u));
        }
    } else if constexpr (U < 37) {
        const unsigned s = c.w * 4096u + (U - 21) * 16384u;
        dma_unit<KV_NT>(m0v, c.vk[0], c.vk[1], c.vk[2], c.vk[3], c.rk, s, s, s, s);
    } else if constexpr (U < 53) {
        const unsigned s = c.w * 4096u + (U - 37) * 16384u;
        dma_unit<KV_NT>(m0v, c.vv[0], c.vv[1], c.vv[0], c.vv[1], c.rv, s, s, s, s);
    } else {
        constexpr int i = (U - 53) / 2, hf = (U - 53) % 2;
        const unsigned s = c.w * (4u * 7168u) + 128u * hf * 896u + 128u * i;
        dma_unit<false>(m0v, c.vo, c.vo, c.vo, c.vo, c.rwo, s, s + (7168u - 1024u), s + 2u * (7168u - 1024u), s + 3u * (7168u - 1024u));
    }
}
template <int LO, int... Is> DI void issue_range_(const Ctx& c, std::integer_sequence<int, Is...>) { (issue_unit<LO + Is>(c), ...); }
template <int LO, int HI> DI void issue_range(const Ctx& c) {
    if constexpr (HI > LO) issue_range_<LO>(c, std::make_integer_sequence<int, HI - LO>{});
}
template <int ST> DI void step_begin(Ctx& c) {
    wait_bar<vm_wait(ST)>();
    STAMP(1 + ST);
    if constexpr (ST >= 1) issue_range<issued_after(ST - 1), issued_after(ST)>(c);
}

DI bf16x8 lds_b128(const char LAS* p) { return *(const bf16x8 LAS*)p; }
DI bf16x8 lds_tr(const char LAS* p0, const char LAS* p1) {
    bf16x4 lo = __builtin_amdgcn_ds_read_tr16_b64_v4bf16((bf16x4 LAS*)p0);
    bf16x4 hi = __builtin_amdgcn_ds_read_tr16_b64_v4bf16((bf16x4 LAS*)p1);
    return __builtin_shufflevector(lo, hi, 0, 1, 2, 3, 4, 5, 6, 7);
}
DI f32x16 mfma(bf16x8 a, bf16x8 b, f32x16 c) { return __builtin_amdgcn_mfma_f32_32x32x16_bf16(a, b, c, 0, 0, 0); }
DI unsigned cvtpk(float lo, float hi) {
    f32x2 v = {lo, hi};
    bf16x2 b = __builtin_convertvector(v, bf16x2);
    return __builtin_bit_cast(unsigned, b);
}
DI bf16x8 pack8(const f32x16& x, int s, float scale) {
    u32x4 p;
#pragma unroll
    for (int j = 0; j < 4; ++j) p[j] = cvtpk(x[8 * s + 2 * j] * scale, x[8 * s + 2 * j + 1] * scale);
    return __builtin_bit_cast(bf16x8, p);
}
DI bf16x8 pack8(const f32x16& x, int s) {
    u32x4 p;
#pragma unroll
    for (int j = 0; j < 4; ++j) p[j] = cvtpk(x[8 * s + 2 * j], x[8 * s + 2 * j + 1]);
    return __builtin_bit_cast(bf16x8, p);
}
DI float bf_lo(unsigned u) { return __uint_as_float(u << 16); }
DI float bf_hi(unsigned u) { return __uint_as_float(u & 0xffff0000u); }

template <class F, int... Is> DI void static_for_(F&& f, std::integer_sequence<int, Is...>) { (f(std::integral_constant<int, Is>{}), ...); }
template <int N, class F> DI void static_for(F&& f) { static_for_(f, std::make_integer_sequence<int, N>{}); }

__global__ __launch_bounds__(256, 1) void fused_attn_block(
    const char* __restrict__ xA, const char* __restrict__ kA, const char* __restrict__ vA, const char* __restrict__ wqA, const char* __restrict__ woA,
    const char* __restrict__ xB, const char* __restrict__ kB, const char* __restrict__ vB, const char* __restrict__ wqB, const char* __restrict__ woB,
    char* __restrict__ out, char* __restrict__ ws) {
    __shared__ __attribute__((aligned(16))) char lds_raw[NSLOT * SLOT];
    char LAS* lds = (char LAS*)lds_raw;

    const unsigned bid = blockIdx.x;
    const unsigned xg = bid & 7u, idx = bid >> 3;
    const unsigned br = xg >> 2;
    const unsigned hh = (xg & 3u) * 4u + (idx & 3u);
    const unsigned bb = idx >> 2;
    const unsigned grp = br * 8u + bb;

    const unsigned tid = threadIdx.x, lane = tid & 63u;
    unsigned w = __builtin_amdgcn_readfirstlane(tid >> 6);
    asm volatile("s_nop 4" : "+s"(w));
    const unsigned r = lane & 31u, h = lane >> 5;

    Ctx c;
    STAMP(0);
    c.w = w;
    {
        const char* xp = (br ? xB : xA) + (size_t)bb * (SQ * DM * 2);
        const char* kp = (br ? kB : kA) + (size_t)(bb * NH + hh) * (SK * DH * 2);
        const char* vp = (br ? vB : vA) + (size_t)(bb * NH + hh) * (SK * DH * 2);
        const char* qp = (br ? wqB : wqA) + hh * (DH * 2);
        const char* op = (br ? woB : woA) + (size_t)hh * (DH * DM * 2);
        c.rx = __builtin_amdgcn_make_buffer_rsrc((void*)xp, 0, SQ * DM * 2, 0x00020000);
        c.rk = __builtin_amdgcn_make_buffer_rsrc((void*)kp, 0, SK * DH * 2, 0x00020000);
        c.rv = __builtin_amdgcn_make_buffer_rsrc((void*)vp, 0, SK * DH * 2, 0x00020000);
        c.rwq = __builtin_amdgcn_make_buffer_rsrc((void*)qp, 0, (DM - 1) * (NH * DH * 2) + DH * 2, 0x00020000);
        c.rwo = __builtin_amdgcn_make_buffer_rsrc((void*)op, 0, DH * DM * 2, 0x00020000);
        c.rslab = __builtin_amdgcn_make_buffer_rsrc((void*)(ws + WS_SLAB_OFF + (size_t)grp * NH * SLAB_BYTES), 0, NH * SLAB_BYTES, 0x00020000);
    }
    c.ldsbase = (unsigned)(size_t)lds + w * 4096u;
    {
        const unsigned l3 = lane >> 3, c8 = lane & 7u, l5 = lane >> 5, c32 = lane & 31u, l4 = lane >> 4;
        c.vx[0] = l3 * 896u + ((c8 ^ l4) << 4);
        c.vx[1] = l3 * 896u + ((c8 ^ (l4 | 4u)) << 4);
        c.vq[0] = l5 * 8192u + ((c32 ^ (l5 << 2)) << 4);
        c.vq[1] = l5 * 8192u + ((c32 ^ ((2u + l5) << 2)) << 4);
        c.vv[0] = l5 * 512u + ((c32 ^ (l5 << 2)) << 4);
        c.vv[1] = l5 * 512u + ((c32 ^ ((2u + l5) << 2)) << 4);
#pragma unroll
        for (int m = 0; m < 4; ++m) c.vk[m] = l5 * 512u + ((c32 ^ ((8u * (w & 1u) + 2u * m + l5) & 15u)) << 4);
        c.vo = l3 * 896u + ((c8 ^ ((l4 & 1u) << 2)) << 4);
    }
    issue_range<0, NSLOT>(c);

    const unsigned i16 = lane & 15u, tq = i16 >> 2, tp = i16 & 3u, nb = (lane >> 4) & 1u;
    const unsigned tps = ((tp & 1u) << 1) | (tp >> 1);
    const unsigned xbase = (32u * w + r) * 128u + ((h ^ ((r >> 1) & 7u)) << 4);
    const unsigned kbase = r * 512u + ((h ^ (r & 15u)) << 4);
    unsigned qbase[4], vbase[4], obase[2];
#pragma unroll
    for (int t = 0; t < 4; ++t) {
        qbase[t] = (8u * h + tq) * 512u + 32u * nb + 8u * tps + 64u * ((unsigned)t ^ tq);
        vbase[t] = (4u * h + tq) * 512u + 32u * nb + 8u * tp + 64u * ((unsigned)t ^ tq);
    }
#pragma unroll
    for (int t = 0; t < 2; ++t) obase[t] = (4u * h + tq) * 128u + 32u * nb + 8u * tps + 64u * ((unsigned)t ^ (tq >> 1));

    f32x16 acc[8];
#pragma unroll
    for (int t = 0; t < 8; ++t)
#pragma unroll
        for (int i = 0; i < 16; ++i) acc[t][i] = 0.f;
    static_for<7>([&](auto I) {
        constexpr int st = I;
        step_begin<st>(c);
        constexpr int sx = ((3 * st) % NSLOT) * SLOT;
#pragma unroll
        for (int s = 0; s < 4; ++s) {
            const int sw = ((3 * st + 1 + (s >> 1)) % NSLOT) * SLOT;
            const bf16x8 b = lds_b128(lds + sx + (xbase ^ (unsigned)(s << 5)));
#pragma unroll
            for (int t = 0; t < 8; ++t) {
                const char LAS* p = lds + sw + (16 * (s & 1)) * 512 + 256 * (t >> 2) + qbase[t & 3];
                acc[t] = mfma(lds_tr(p, p + 4 * 512), b, acc[t]);
            }
        }
    });
    bf16x8 qf[16];
#pragma unroll
    for (int t = 0; t < 8; ++t) { qf[2 * t] = pack8(acc[t], 0); qf[2 * t + 1] = pack8(acc[t], 1); }

    f32x16 S[16];
    float mx = -3.0e38f;
    static_for<8>([&](auto I) {
        constexpr int i = I, st = 7 + i;
        step_begin<st>(c);
#pragma unroll
        for (int u = 0; u < 2; ++u) {
            const int sk = ((21 + 2 * i + u) % NSLOT) * SLOT;
            f32x16 a;
#pragma unroll
            for (int e = 0; e < 16; ++e) a[e] = 0.f;
#pragma unroll
            for (int ks = 0; ks < 16; ++ks)
                a = mfma(lds_b128(lds + sk + 256 * (ks >> 3) + (kbase ^ (unsigned)((ks & 7) << 5))), qf[ks], a);
            S[2 * i + u] = a;
#pragma unroll
            for (int e = 0; e < 16; ++e) mx = fmaxf(mx, a[e]);
        }
    });
    mx = fmaxf(mx, __shfl_xor(mx, 32));
    const float cs = 0.0625f * 1.4426950408889634f;
    const float mc = mx * cs;

    float lsum = 0.f;
#pragma unroll
    for (int t = 0; t < 8; ++t)
#pragma unroll
        for (int i = 0; i < 16; ++i) acc[t][i] = 0.f;
    static_for<8>([&](auto I) {
        constexpr int i = I, st = 15 + i;
        bf16x8 pf[4];
#pragma unroll
        for (int u = 0; u < 2; ++u) {
            f32x16 p;
#pragma unroll
            for (int e = 0; e < 16; ++e) { p[e] = __builtin_amdgcn_exp2f(S[2 * i + u][e] * cs - mc); lsum += p[e]; }
            pf[2 * u] = pack8(p, 0);
            pf[2 * u + 1] = pack8(p, 1);
        }
        step_begin<st>(c);
#pragma unroll
        for (int u = 0; u < 2; ++u) {
            const int sv = ((37 + 2 * i + u) % NSLOT) * SLOT;
#pragma unroll
            for (int s = 0; s < 2; ++s)
#pragma unroll
                for (int t = 0; t < 8; ++t) {
                    const char LAS* p = lds + sv + (16 * s) * 512 + 256 * (t >> 2) + vbase[t & 3];
                    acc[t] = mfma(lds_tr(p, p + 8 * 512), pf[2 * u + s], acc[t]);
                }
        }
    });
    lsum += __shfl_xor(lsum, 32);
    const float inv = 1.0f / lsum;
#pragma unroll
    for (int t = 0; t < 8; ++t) { qf[2 * t] = pack8(acc[t], 0, inv); qf[2 * t + 1] = pack8(acc[t], 1, inv); }

    const unsigned slab_v = r * 32u + h * 16u;
    const unsigned slab_s = hh * SLAB_BYTES + w * 1024u;
    static_for<7>([&](auto I) {
        constexpr int i = I, st = 23 + i;
        step_begin<st>(c);
#pragma unroll
        for (int nt = 0; nt < 2; ++nt) {
            f32x16 a;
#pragma unroll
            for (int e = 0; e < 16; ++e) a[e] = 0.f;
#pragma unroll
            for (int ks = 0; ks < 16; ++ks) {
                const int so = ((53 + 2 * i + (ks >> 3)) % NSLOT) * SLOT;
                const char LAS* p = lds + so + (16 * (ks & 7)) * 128 + obase[nt];
                a = mfma(lds_tr(p, p + 8 * 128), qf[ks], a);
            }
#pragma unroll
            for (int s = 0; s < 2; ++s)
                __builtin_amdgcn_raw_buffer_store_b128(__builtin_bit_cast(u32x4, pack8(a, s)), c.rslab, slab_v,
                                                       slab_s + (unsigned)(((2 * i + nt) * 2 + s) * 4096), 16);
        }
    });

    typedef GAS unsigned long long gu64;
    gu64* epoch_p = (gu64*)(ws + grp * 256u);
    gu64* flags = (gu64*)(ws + 4096u + grp * (16u * 128u));
    const unsigned long long ep = __hip_atomic_load(epoch_p, __ATOMIC_RELAXED, __HIP_MEMORY_SCOPE_AGENT);
    const unsigned long long tag = (ep + 1ull) * 0x9E3779B97F4A7C15ull + 0x632BE59BD9B4E019ull;
    STAMP(31);
    asm volatile("s_waitcnt vmcnt(0)\n\ts_barrier" ::: "memory");
    STAMP(32);
    if (w == 0) {
        if (lane == 0) __hip_atomic_store(flags + hh * 16u, tag, __ATOMIC_RELAXED, __HIP_MEMORY_SCOPE_AGENT);
        for (unsigned spins = 0; spins < (1u << 22); ++spins) {
            const unsigned long long f = __hip_atomic_load(flags + (lane & 15u) * 16u, __ATOMIC_RELAXED, __HIP_MEMORY_SCOPE_AGENT);
            if (__all(f == tag)) break;
            __builtin_amdgcn_s_sleep(1);
        }
        if (hh == 0 && lane == 0) __hip_atomic_store(epoch_p, ep + 1ull, __ATOMIC_RELAXED, __HIP_MEMORY_SCOPE_AGENT);
    }
    asm volatile("s_barrier" ::: "memory");
    STAMP(33);

    const unsigned rd_v = ((lane >> 4) * 4u + (hh >> 2)) * 1024u + (hh & 3u) * 256u + (lane & 15u) * 16u;
    char* outp = out + ((size_t)(bb * SQ + 8u * hh + ((lane & 15u) >> 1)) * (2 * DM) + br * DM + (lane >> 4) * 16u + (lane & 1u) * 8u) * 2;
#pragma unroll
    for (int uu = 0; uu < 2; ++uu) {
        const unsigned u = w + 4u * uu;
        if (u < 7u) {
            u32x4 v[16];
#pragma unroll
            for (int s = 0; s < 16; ++s)
                v[s] = __builtin_amdgcn_raw_buffer_load_b128(c.rslab, rd_v, s * SLAB_BYTES + u * 16384u, 16);
            float sum[8];
#pragma unroll
            for (int e = 0; e < 8; ++e) sum[e] = 0.f;
#pragma unroll
            for (int s = 0; s < 16; ++s)
#pragma unroll
                for (int e = 0; e < 4; ++e) { sum[2 * e] += bf_lo(v[s][e]); sum[2 * e + 1] += bf_hi(v[s][e]); }
            u32x4 o;
#pragma unroll
            for (int e = 0; e < 4; ++e) o[e] = cvtpk(sum[2 * e], sum[2 * e + 1]);
            *(u32x4*)(outp + u * 128u) = o;
        }
    }
}

extern "C" void kernel_launch(void* const* d_in, const int* in_sizes, int n_in,
                              void* d_out, int out_size, void* d_ws, size_t ws_size,
                              hipStream_t stream) {
    (void)in_sizes; (void)n_in; (void)out_size; (void)ws_size;
    fused_attn_block<<<256, 256, 0, stream>>>(
        (const char*)d_in[0], (const char*)d_in[1], (const char*)d_in[2], (const char*)d_in[3], (const char*)d_in[4],
        (const char*)d_in[5], (const char*)d_in[6], (const char*)d_in[7], (const char*)d_in[8], (const char*)d_in[9],
        (char*)d_out, (char*)d_ws);
}
```

```cpp
#include <hip/hip_runtime.h>
#include <stdint.h>
#include <utility>

#define DI __device__ __forceinline__
#define LAS __attribute__((address_space(3)))
#define GAS __attribute__((address_space(1)))

typedef __bf16 bf16x8 __attribute__((ext_vector_type(8)));
typedef __bf16 bf16x4 __attribute__((ext_vector_type(4)));
typedef __bf16 bf16x2 __attribute__((ext_vector_type(2)));
typedef float f32x16 __attribute__((ext_vector_type(16)));
typedef float f32x2 __attribute__((ext_vector_type(2)));
typedef unsigned u32x4 __attribute__((ext_vector_type(4)));
typedef __amdgpu_buffer_rsrc_t rsrc_t;

constexpr int SQ = 128, DM = 448, NH = 16, SK = 512, DH = 256;
constexpr int SLOT = 16384, NSLOT = 10;
constexpr int NUNIT = 67;
constexpr int NSTEP = 30;
constexpr unsigned SLAB_BYTES = 112 * 1024;
constexpr unsigned WS_SLAB_OFF = 65536;

constexpr int first_unit(int st) {
    return st < 7 ? 3 * st : st < 15 ? 21 + 2 * (st - 7) : st < 23 ? 37 + 2 * (st - 15) : 53 + 2 * (st - 23);
}
constexpr int last_unit(int st) { return first_unit(st) + (st < 7 ? 2 : 1); }
constexpr int imin(int a, int b) { return a < b ? a : b; }
constexpr int issued_after(int st) { return st <= 0 ? NSLOT : imin(NUNIT, first_unit(st) + NSLOT); }
constexpr int stores_in(int st) { return st >= 23 ? 4 : 0; }
constexpr int vm_wait(int st) {
    int need = last_unit(st);
    int n = 0;
    int prev = NSLOT;
    bool seen = need < NSLOT;
    if (seen) n += 4 * (NSLOT - 1 - need);
    for (int s = 0; s < st; ++s) {
        int cur = issued_after(s);
        if (seen) n += 4 * (cur - prev);
        else if (need < cur) { seen = true; n += 4 * (cur - 1 - need); }
        if (seen) n += stores_in(s);
        prev = cur;
    }
    return n;
}

template <int N> DI void wait_bar() {
    asm volatile("s_waitcnt vmcnt(%0) lgkmcnt(0)\n\ts_barrier" ::"i"(N) : "memory");
}

#define KV_NT 1
template <bool NT>
DI void dma_unit(unsigned m0v, unsigned v0, unsigned v1, unsigned v2, unsigned v3, rsrc_t r,
                 unsigned s0, unsigned s1, unsigned s2, unsigned s3) {
    if constexpr (NT)
        asm volatile(
            "s_mov_b32 m0, %0\n\t"
            "s_nop 0\n\t"
            "buffer_load_dwordx4 %1, %5, %6 offen nt lds\n\t"
            "buffer_load_dwordx4 %2, %5, %7 offen offset:1024 nt lds\n\t"
            "buffer_load_dwordx4 %3, %5, %8 offen offset:2048 nt lds\n\t"
            "buffer_load_dwordx4 %4, %5, %9 offen offset:3072 nt lds"
            ::"s"(m0v), "v"(v0), "v"(v1), "v"(v2), "v"(v3), "s"(r), "s"(s0), "s"(s1), "s"(s2), "s"(s3)
            : "memory");
    else
        asm volatile(
            "s_mov_b32 m0, %0\n\t"
            "s_nop 0\n\t"
            "buffer_load_dwordx4 %1, %5, %6 offen lds\n\t"
            "buffer_load_dwordx4 %2, %5, %7 offen offset:1024 lds\n\t"
            "buffer_load_dwordx4 %3, %5, %8 offen offset:2048 lds\n\t"
            "buffer_load_dwordx4 %4, %5, %9 offen offset:3072 lds"
            ::"s"(m0v), "v"(v0), "v"(v1), "v"(v2), "v"(v3), "s"(r), "s"(s0), "s"(s1), "s"(s2), "s"(s3)
            : "memory");
}

#define STAMP(i) do {} while (0)

struct Ctx {
    rsrc_t rx, rwq, rk, rv, rwo, rslab;
    unsigned ldsbase;
    unsigned w;
    unsigned vx[2], vq[2], vk[4], vv[2], vo;
};

template <int U> DI void issue_unit(const Ctx& c) {
    const unsigned m0v = c.ldsbase + (U % NSLOT) * SLOT;
    if constexpr (U < 21) {
        constexpr int i = U / 3, kind = U % 3;
        if constexpr (kind == 0) {
            const unsigned s = c.w * (4u * 7168u) + 128u * i;
            dma_unit<false>(m0v, c.vx[0], c.vx[1], c.vx[0], c.vx[1], c.rx, s, s + (7168u - 1024u), s + 2u * (7168u - 1024u), s + 3u * (7168u - 1024u));
        } else {
            const unsigned s = c.w * (4u * 16384u) + (64u * i + 32u * (kind - 1)) * 8192u;
            dma_unit<false>(m0v, c.vq[0], c.vq[1], c.vq[0], c.vq[1], c.rwq, s, s + (16384u - 1024u), s + 2u * (16384u - 1024u), s + 3u * (16384u - 1024u));
        }
    } else if constexpr (U < 37) {
        const unsigned s = c.w * 4096u + (U - 21) * 16384u;
        dma_unit<KV_NT>(m0v, c.vk[0], c.vk[1], c.vk[2], c.vk[3], c.rk, s, s, s, s);
    } else if constexpr (U < 53) {
        const unsigned s = c.w * 4096u + (U - 37) * 16384u;
        dma_unit<KV_NT>(m0v, c.vv[0], c.vv[1], c.vv[0], c.vv[1], c.rv, s, s, s, s);
    } else {
        constexpr int i = (U - 53) / 2, hf = (U - 53) % 2;
        const unsigned s = c.w * (4u * 7168u) + 128u * hf * 896u + 128u * i;
        dma_unit<false>(m0v, c.vo, c.vo, c.vo, c.vo, c.rwo, s, s + (7168u - 1024u), s + 2u * (7168u - 1024u), s + 3u * (7168u - 1024u));
    }
}
template <int LO, int... Is> DI void issue_range_(const Ctx& c, std::integer_sequence<int, Is...>) { (issue_unit<LO + Is>(c), ...); }
template <int LO, int HI> DI void issue_range(const Ctx& c) {
    if constexpr (HI > LO) issue_range_<LO>(c, std::make_integer_sequence<int, HI - LO>{});
}
template <int ST> DI void step_begin(Ctx& c) {
    wait_bar<vm_wait(ST)>();
    STAMP(1 + ST);
    if constexpr (ST >= 1) issue_range<issued_after(ST - 1), issued_after(ST)>(c);
}

DI bf16x8 lds_b128(const char LAS* p) { return *(const bf16x8 LAS*)p; }
DI bf16x8 lds_tr(const char LAS* p0, const char LAS* p1) {
    bf16x4 lo = __builtin_amdgcn_ds_read_tr16_b64_v4bf16((bf16x4 LAS*)p0);
    bf16x4 hi = __builtin_amdgcn_ds_read_tr16_b64_v4bf16((bf16x4 LAS*)p1);
    return __builtin_shufflevector(lo, hi, 0, 1, 2, 3, 4, 5, 6, 7);
}
DI f32x16 mfma(bf16x8 a, bf16x8 b, f32x16 c) { return __builtin_amdgcn_mfma_f32_32x32x16_bf16(a, b, c, 0, 0, 0); }
DI unsigned cvtpk(float lo, float hi) {
    f32x2 v = {lo, hi};
    bf16x2 b = __builtin_convertvector(v, bf16x2);
    return __builtin_bit_cast(unsigned, b);
}
DI bf16x8 pack8(const f32x16& x, int s, float scale) {
    u32x4 p;
#pragma unroll
    for (int j = 0; j < 4; ++j) p[j] = cvtpk(x[8 * s + 2 * j] * scale, x[8 * s + 2 * j + 1] * scale);
    return __builtin_bit_cast(bf16x8, p);
}
DI bf16x8 pack8(const f32x16& x, int s) {
    u32x4 p;
#pragma unroll
    for (int j = 0; j < 4; ++j) p[j] = cvtpk(x[8 * s + 2 * j], x[8 * s + 2 * j + 1]);
    return __builtin_bit_cast(bf16x8, p);
}
DI float bf_lo(unsigned u) { return __uint_as_float(u << 16); }
DI float bf_hi(unsigned u) { return __uint_as_float(u & 0xffff0000u); }

template <class F, int... Is> DI void static_for_(F&& f, std::integer_sequence<int, Is...>) { (f(std::integral_constant<int, Is>{}), ...); }
template <int N, class F> DI void static_for(F&& f) { static_for_(f, std::make_integer_sequence<int, N>{}); }

__global__ __launch_bounds__(256, 1) void fused_attn_block(
    const char* __restrict__ xA, const char* __restrict__ kA, const char* __restrict__ vA, const char* __restrict__ wqA, const char* __restrict__ woA,
    const char* __restrict__ xB, const char* __restrict__ kB, const char* __restrict__ vB, const char* __restrict__ wqB, const char* __restrict__ woB,
    char* __restrict__ out, char* __restrict__ ws) {
    __shared__ __attribute__((aligned(16))) char lds_raw[NSLOT * SLOT];
    char LAS* lds = (char LAS*)lds_raw;

    const unsigned bid = blockIdx.x;
    const unsigned xg = bid & 7u, idx = bid >> 3;
    const unsigned br = xg >> 2;
    const unsigned hh = (xg & 3u) * 4u + (idx & 3u);
    const unsigned bb = idx >> 2;
    const unsigned grp = br * 8u + bb;

    const unsigned tid = threadIdx.x, lane = tid & 63u;
    unsigned w = __builtin_amdgcn_readfirstlane(tid >> 6);
    asm volatile("s_nop 4" : "+s"(w));
    const unsigned r = lane & 31u, h = lane >> 5;

    Ctx c;
    STAMP(0);
    c.w = w;
    {
        const char* xp = (br ? xB : xA) + (size_t)bb * (SQ * DM * 2);
        const char* kp = (br ? kB : kA) + (size_t)(bb * NH + hh) * (SK * DH * 2);
        const char* vp = (br ? vB : vA) + (size_t)(bb * NH + hh) * (SK * DH * 2);
        const char* qp = (br ? wqB : wqA) + hh * (DH * 2);
        const char* op = (br ? woB : woA) + (size_t)hh * (DH * DM * 2);
        c.rx = __builtin_amdgcn_make_buffer_rsrc((void*)xp, 0, SQ * DM * 2, 0x00020000);
        c.rk = __builtin_amdgcn_make_buffer_rsrc((void*)kp, 0, SK * DH * 2, 0x00020000);
        c.rv = __builtin_amdgcn_make_buffer_rsrc((void*)vp, 0, SK * DH * 2, 0x00020000);
        c.rwq = __builtin_amdgcn_make_buffer_rsrc((void*)qp, 0, (DM - 1) * (NH * DH * 2) + DH * 2, 0x00020000);
        c.rwo = __builtin_amdgcn_make_buffer_rsrc((void*)op, 0, DH * DM * 2, 0x00020000);
        c.rslab = __builtin_amdgcn_make_buffer_rsrc((void*)(ws + WS_SLAB_OFF + (size_t)grp * NH * SLAB_BYTES), 0, NH * SLAB_BYTES, 0x00020000);
    }
    c.ldsbase = (unsigned)(size_t)lds + w * 4096u;
    {
        const unsigned l3 = lane >> 3, c8 = lane & 7u, l5 = lane >> 5, c32 = lane & 31u, l4 = lane >> 4;
        c.vx[0] = l3 * 896u + ((c8 ^ l4) << 4);
        c.vx[1] = l3 * 896u + ((c8 ^ (l4 | 4u)) << 4);
        c.vq[0] = l5 * 8192u + ((c32 ^ (l5 << 2)) << 4);
        c.vq[1] = l5 * 8192u + ((c32 ^ ((2u + l5) << 2)) << 4);
        c.vv[0] = l5 * 512u + ((c32 ^ (l5 << 2)) << 4);
        c.vv[1] = l5 * 512u + ((c32 ^ ((2u + l5) << 2)) << 4);
#pragma unroll
        for (int m = 0; m < 4; ++m) c.vk[m] = l5 * 512u + ((c32 ^ ((8u * (w & 1u) + 2u * m + l5) & 15u)) << 4);
        c.vo = l3 * 896u + ((c8 ^ ((l4 & 1u) << 2)) << 4);
    }
    unsigned pf0, pf1;
    {
        unsigned long long pc; unsigned len;
        asm volatile("s_getpc_b64 %0\n.Lpf_here:\n\ts_mov_b32 %1, .Lpf_end-.Lpf_here" : "=s"(pc), "=s"(len));
        const unsigned o0 = (w * 128u + lane) * 128u, o1 = o0 + 64u * 128u;
        const unsigned a0 = o0 < len ? o0 : len, a1 = o1 < len ? o1 : len;
        asm volatile("global_load_dword %0, %2, %4\n\tglobal_load_dword %1, %3, %4" : "=&v"(pf0), "=&v"(pf1) : "v"(a0), "v"(a1), "s"(pc) : "memory");
    }
    issue_range<0, NSLOT>(c);

    const unsigned i16 = lane & 15u, tq = i16 >> 2, tp = i16 & 3u, nb = (lane >> 4) & 1u;
    const unsigned tps = ((tp & 1u) << 1) | (tp >> 1);
    const unsigned xbase = (32u * w + r) * 128u + ((h ^ ((r >> 1) & 7u)) << 4);
    const unsigned kbase = r * 512u + ((h ^ (r & 15u)) << 4);
    unsigned qbase[4], vbase[4], obase[2];
#pragma unroll
    for (int t = 0; t < 4; ++t) {
        qbase[t] = (8u * h + tq) * 512u + 32u * nb + 8u * tps + 64u * ((unsigned)t ^ tq);
        vbase[t] = (4u * h + tq) * 512u + 32u * nb + 8u * tp + 64u * ((unsigned)t ^ tq);
    }
#pragma unroll
    for (int t = 0; t < 2; ++t) obase[t] = (4u * h + tq) * 128u + 32u * nb + 8u * tps + 64u * ((unsigned)t ^ (tq >> 1));

    f32x16 acc[8];
#pragma unroll
    for (int t = 0; t < 8; ++t)
#pragma unroll
        for (int i = 0; i < 16; ++i) acc[t][i] = 0.f;
    static_for<7>([&](auto I) {
        constexpr int st = I;
        step_begin<st>(c);
        if constexpr (st == 0) asm volatile("" :: "v"(pf0), "v"(pf1));
        constexpr int sx = ((3 * st) % NSLOT) * SLOT;
#pragma unroll
        for (int s = 0; s < 4; ++s) {
            const int sw = ((3 * st + 1 + (s >> 1)) % NSLOT) * SLOT;
            const bf16x8 b = lds_b128(lds + sx + (xbase ^ (unsigned)(s << 5)));
#pragma unroll
            for (int t = 0; t < 8; ++t) {
                const char LAS* p = lds + sw + (16 * (s & 1)) * 512 + 256 * (t >> 2) + qbase[t & 3];
                acc[t] = mfma(lds_tr(p, p + 4 * 512), b, acc[t]);
            }
        }
    });
    bf16x8 qf[16];
#pragma unroll
    for (int t = 0; t < 8; ++t) { qf[2 * t] = pack8(acc[t], 0); qf[2 * t + 1] = pack8(acc[t], 1); }

    f32x16 S[16];
    float mx = -3.0e38f;
    static_for<8>([&](auto I) {
        constexpr int i = I, st = 7 + i;
        step_begin<st>(c);
#pragma unroll
        for (int u = 0; u < 2; ++u) {
            const int sk = ((21 + 2 * i + u) % NSLOT) * SLOT;
            f32x16 a;
#pragma unroll
            for (int e = 0; e < 16; ++e) a[e] = 0.f;
#pragma unroll
            for (int ks = 0; ks < 16; ++ks)
                a = mfma(lds_b128(lds + sk + 256 * (ks >> 3) + (kbase ^ (unsigned)((ks & 7) << 5))), qf[ks], a);
            S[2 * i + u] = a;
#pragma unroll
            for (int e = 0; e < 16; ++e) mx = fmaxf(mx, a[e]);
        }
    });
    mx = fmaxf(mx, __shfl_xor(mx, 32));
    const float cs = 0.0625f * 1.4426950408889634f;
    const float mc = mx * cs;

    float lsum = 0.f;
#pragma unroll
    for (int t = 0; t < 8; ++t)
#pragma unroll
        for (int i = 0; i < 16; ++i) acc[t][i] = 0.f;
    static_for<8>([&](auto I) {
        constexpr int i = I, st = 15 + i;
        bf16x8 pf[4];
#pragma unroll
        for (int u = 0; u < 2; ++u) {
            f32x16 p;
#pragma unroll
            for (int e = 0; e < 16; ++e) { p[e] = __builtin_amdgcn_exp2f(S[2 * i + u][e] * cs - mc); lsum += p[e]; }
            pf[2 * u] = pack8(p, 0);
            pf[2 * u + 1] = pack8(p, 1);
        }
        step_begin<st>(c);
#pragma unroll
        for (int u = 0; u < 2; ++u) {
            const int sv = ((37 + 2 * i + u) % NSLOT) * SLOT;
#pragma unroll
            for (int s = 0; s < 2; ++s)
#pragma unroll
                for (int t = 0; t < 8; ++t) {
                    const char LAS* p = lds + sv + (16 * s) * 512 + 256 * (t >> 2) + vbase[t & 3];
                    acc[t] = mfma(lds_tr(p, p + 8 * 512), pf[2 * u + s], acc[t]);
                }
        }
    });
    lsum += __shfl_xor(lsum, 32);
    const float inv = 1.0f / lsum;
#pragma unroll
    for (int t = 0; t < 8; ++t) { qf[2 * t] = pack8(acc[t], 0, inv); qf[2 * t + 1] = pack8(acc[t], 1, inv); }

    const unsigned slab_v = r * 32u + h * 16u;
    const unsigned slab_s = hh * SLAB_BYTES + w * 1024u;
    static_for<7>([&](auto I) {
        constexpr int i = I, st = 23 + i;
        step_begin<st>(c);
#pragma unroll
        for (int nt = 0; nt < 2; ++nt) {
            f32x16 a;
#pragma unroll
            for (int e = 0; e < 16; ++e) a[e] = 0.f;
#pragma unroll
            for (int ks = 0; ks < 16; ++ks) {
                const int so = ((53 + 2 * i + (ks >> 3)) % NSLOT) * SLOT;
                const char LAS* p = lds + so + (16 * (ks & 7)) * 128 + obase[nt];
                a = mfma(lds_tr(p, p + 8 * 128), qf[ks], a);
            }
#pragma unroll
            for (int s = 0; s < 2; ++s)
                __builtin_amdgcn_raw_buffer_store_b128(__builtin_bit_cast(u32x4, pack8(a, s)), c.rslab, slab_v,
                                                       slab_s + (unsigned)(((2 * i + nt) * 2 + s) * 4096), 16);
        }
    });

    typedef GAS unsigned long long gu64;
    gu64* epoch_p = (gu64*)(ws + grp * 256u);
    gu64* flags = (gu64*)(ws + 4096u + grp * (16u * 128u));
    const unsigned long long ep = __hip_atomic_load(epoch_p, __ATOMIC_RELAXED, __HIP_MEMORY_SCOPE_AGENT);
    const unsigned long long tag = (ep + 1ull) * 0x9E3779B97F4A7C15ull + 0x632BE59BD9B4E019ull;
    STAMP(31);
    asm volatile("s_waitcnt vmcnt(0)\n\ts_barrier" ::: "memory");
    STAMP(32);
    if (w == 0) {
        if (lane == 0) __hip_atomic_store(flags + hh * 16u, tag, __ATOMIC_RELAXED, __HIP_MEMORY_SCOPE_AGENT);
        for (unsigned spins = 0; spins < (1u << 22); ++spins) {
            const unsigned long long f = __hip_atomic_load(flags + (lane & 15u) * 16u, __ATOMIC_RELAXED, __HIP_MEMORY_SCOPE_AGENT);
            if (__all(f == tag)) break;
            __builtin_amdgcn_s_sleep(1);
        }
        if (hh == 0 && lane == 0) __hip_atomic_store(epoch_p, ep + 1ull, __ATOMIC_RELAXED, __HIP_MEMORY_SCOPE_AGENT);
    }
    asm volatile("s_barrier" ::: "memory");
    STAMP(33);

    const unsigned rd_v = ((lane >> 4) * 4u + (hh >> 2)) * 1024u + (hh & 3u) * 256u + (lane & 15u) * 16u;
    char* outp = out + ((size_t)(bb * SQ + 8u * hh + ((lane & 15u) >> 1)) * (2 * DM) + br * DM + (lane >> 4) * 16u + (lane & 1u) * 8u) * 2;
#pragma unroll
    for (int uu = 0; uu < 2; ++uu) {
        const unsigned u = w + 4u * uu;
        if (u < 7u) {
            u32x4 v[16];
#pragma unroll
            for (int s = 0; s < 16; ++s)
                v[s] = __builtin_amdgcn_raw_buffer_load_b128(c.rslab, rd_v, s * SLAB_BYTES + u * 16384u, 16);
            float sum[8];
#pragma unroll
            for (int e = 0; e < 8; ++e) sum[e] = 0.f;
#pragma unroll
            for (int s = 0; s < 16; ++s)
#pragma unroll
                for (int e = 0; e < 4; ++e) { sum[2 * e] += bf_lo(v[s][e]); sum[2 * e + 1] += bf_hi(v[s][e]); }
            u32x4 o;
#pragma unroll
            for (int e = 0; e < 4; ++e) o[e] = cvtpk(sum[2 * e], sum[2 * e + 1]);
            *(u32x4*)(outp + u * 128u) = o;
        }
    }
    asm volatile(".Lpf_end:");
}

extern "C" void kernel_launch(void* const* d_in, const int* in_sizes, int n_in,
                              void* d_out, int out_size, void* d_ws, size_t ws_size,
                              hipStream_t stream) {
    (void)in_sizes; (void)n_in; (void)out_size; (void)ws_size;
    fused_attn_block<<<256, 256, 0, stream>>>(
        (const char*)d_in[0], (const char*)d_in[1], (const char*)d_in[2], (const char*)d_in[3], (const char*)d_in[4],
        (const char*)d_in[5], (const char*)d_in[6], (const char*)d_in[7], (const char*)d_in[8], (const char*)d_in[9],
        (char*)d_out, (char*)d_ws);
}
```

```cpp
#include <hip/hip_runtime.h>
#include <stdint.h>

#define DI __device__ __forceinline__
#define LAS __attribute__((address_space(3)))
#define GAS __attribute__((address_space(1)))

typedef __bf16 bf16x2 __attribute__((ext_vector_type(2)));
typedef float f32x2 __attribute__((ext_vector_type(2)));
typedef unsigned u32x4 __attribute__((ext_vector_type(4)));
typedef __amdgpu_buffer_rsrc_t rsrc_t;

constexpr int SQ = 128, DM = 448, NH = 16, SK = 512, DH = 256;
constexpr unsigned SLOT = 32768, RING = 5 * SLOT;
constexpr unsigned SLAB_BYTES = 112 * 1024;
constexpr unsigned WS_SLAB_OFF = 65536;

#define STAMP(i) do {} while (0)

DI unsigned cvtpk(float lo, float hi) {
    f32x2 v = {lo, hi};
    bf16x2 b = __builtin_convertvector(v, bf16x2);
    return __builtin_bit_cast(unsigned, b);
}
DI float bf_lo(unsigned u) { return __uint_as_float(u << 16); }
DI float bf_hi(unsigned u) { return __uint_as_float(u & 0xffff0000u); }
DI u32x4 srd(const char* p, unsigned bytes) {
    const unsigned long long a = (unsigned long long)p;
    return (u32x4){(unsigned)a, (unsigned)(a >> 32) & 0xffffu, bytes, 0x00020000u};
}

__global__ __launch_bounds__(256, 1) void fused_attn_block(
    const char* __restrict__ xA, const char* __restrict__ kA, const char* __restrict__ vA, const char* __restrict__ wqA, const char* __restrict__ woA,
    const char* __restrict__ xB, const char* __restrict__ kB, const char* __restrict__ vB, const char* __restrict__ wqB, const char* __restrict__ woB,
    char* __restrict__ out, char* __restrict__ ws) {
    __shared__ __attribute__((aligned(16))) char lds_raw[RING];
    const unsigned lds0 = (unsigned)(uintptr_t)(char LAS*)lds_raw;

    const unsigned bid = blockIdx.x;
    const unsigned xg = bid & 7u, idx = bid >> 3;
    const unsigned br = xg >> 2;
    const unsigned hh = (xg & 3u) * 4u + (idx & 3u);
    const unsigned bb = idx >> 2;
    const unsigned grp = br * 8u + bb;

    const unsigned tid = threadIdx.x, lane = tid & 63u;
    const unsigned w = __builtin_amdgcn_readfirstlane(tid >> 6);
    const char* slabp = ws + WS_SLAB_OFF + (size_t)grp * NH * SLAB_BYTES;
    {
        const unsigned r = lane & 31u, h = lane >> 5;
        const u32x4 rx = srd((br ? xB : xA) + (size_t)bb * (SQ * DM * 2), SQ * DM * 2);
        const u32x4 rk = srd((br ? kB : kA) + (size_t)(bb * NH + hh) * (SK * DH * 2), SK * DH * 2);
        const u32x4 rv = srd((br ? vB : vA) + (size_t)(bb * NH + hh) * (SK * DH * 2), SK * DH * 2);
        const u32x4 rwq = srd((br ? wqB : wqA) + hh * (DH * 2), (DM - 1) * (NH * DH * 2) + DH * 2);
        const u32x4 rwo = srd((br ? woB : woA) + (size_t)hh * (DH * DM * 2), DH * DM * 2);
        const u32x4 rsl = srd(slabp, NH * SLAB_BYTES);
        const unsigned l3 = lane >> 3, c8 = lane & 7u, l4 = lane >> 4;
        const unsigned ctr = (c8 ^ ((l4 & 1u) << 2)) << 4;
        const unsigned crw = (c8 ^ ((4u * w + l4) & 7u)) << 4;
        unsigned vx = l3 * 896u + c8 * 16u;
        unsigned vwq = (8u * w + l3) * 8192u + ctr;
        unsigned vk = (8u * w + l3) * 512u + crw;
        unsigned vv = (8u * w + l3) * 512u + ctr;
        unsigned vwo = (8u * w + l3) * 896u + ctr;
        const unsigned i16 = lane & 15u, tq = i16 >> 2, tp = i16 & 3u, nb = (lane >> 4) & 1u;
        const unsigned tps = ((tp & 1u) << 1) | (tp >> 1);
        unsigned trb = lds0 + (8u * h + tq) * 128u + 32u * nb + 8u * tps + 64u * (tq >> 1);
        const unsigned rp = (r & 19u) | ((r & 4u) << 1) | ((r & 8u) >> 1);
        unsigned kb = lds0 + rp * 128u + ((h ^ ((rp >> 1) & 7u)) << 4);
        const unsigned xs = lds0 + 4u * SLOT + w * 8192u;
        unsigned xw0 = xs + l3 * 128u + ((c8 ^ l4) << 4);
        unsigned xw1 = xs + l3 * 128u + ((c8 ^ (4u + l4)) << 4);
        unsigned xb = xs + r * 128u + ((h ^ ((r >> 1) & 7u)) << 4);
        unsigned slab_v = r * 32u + h * 16u;
        unsigned pfo = (idx * 16u + (tid & 15u)) * 128u;
        unsigned toff = lane < 16u ? (w * 16u + lane) * 128u : 0x80000000u;
        const unsigned rot = (2u * (hh & 3u) + bb) % 7u;
        unsigned kx[7], no[7];
#pragma unroll
        for (unsigned j = 0; j < 7; ++j) {
            kx[j] = (j + rot >= 7u ? j + rot - 7u : j + rot) * 128u;
            no[j] = ((j + bb) % 7u) * 128u;
        }
        const unsigned m0base = lds0 + w * 1024u, sx = w * (32u * 896u), slab_s = hh * SLAB_BYTES + w * 1024u;
        asm volatile(
        "s_nop 4\n\t"
"s_getpc_b64 s[90:91]\n"
".Lpf_here:\n"
"s_mov_b32 s87, .Lpf_end-.Lpf_here\n"
"v_min_u32 v29, s87, v13\n"
"s_add_u32 s96, s61, 0\n"
"s_add_u32 s97, s61, 7168\n"
"s_add_u32 s98, s61, 14336\n"
"s_add_u32 s99, s61, 21504\n"
"s_lshl_b32 s80, s64, 12\n"
"s_lshl_b32 s81, s65, 12\n"
"s_lshl_b32 s82, s66, 12\n"
"s_lshl_b32 s83, s67, 12\n"
"s_lshl_b32 s84, s68, 12\n"
"s_lshl_b32 s85, s69, 12\n"
"s_lshl_b32 s86, s70, 12\n"
"s_add_u32 s87, s64, s96\n"
"buffer_load_dwordx4 a[80:83], v2, s[36:39], s87 offen\n"
"s_add_u32 s87, s64, s97\n"
"buffer_load_dwordx4 a[84:87], v2, s[36:39], s87 offen\n"
"s_add_u32 s87, s64, s98\n"
"buffer_load_dwordx4 a[88:91], v2, s[36:39], s87 offen\n"
"s_add_u32 s87, s64, s99\n"
"buffer_load_dwordx4 a[92:95], v2, s[36:39], s87 offen\n"
"s_add_u32 m0, s60, 0\n"
"s_add_u32 s87, s80, 0\n"
"buffer_load_dwordx4 v3, s[40:43], s87 offen lds\n"
"s_add_u32 m0, s60, 4096\n"
"s_add_u32 s87, s80, 262144\n"
"buffer_load_dwordx4 v3, s[40:43], s87 offen lds\n"
"global_load_dword v28, v29, s[90:91]\n"
"s_mov_b32 s71, 0x9e3779b1\n"
"v_mul_lo_u32 v64, v3, s71\n"
"v_xor_b32 v64, v64, v7\n"
"v_mul_lo_u32 v65, v64, s71\n"
"v_xor_b32 v65, v65, v7\n"
"v_mul_lo_u32 v66, v65, s71\n"
"v_xor_b32 v66, v66, v7\n"
"v_mul_lo_u32 v67, v66, s71\n"
"v_xor_b32 v67, v67, v7\n"
"v_mul_lo_u32 v68, v67, s71\n"
"v_xor_b32 v68, v68, v7\n"
"v_mul_lo_u32 v69, v68, s71\n"
"v_xor_b32 v69, v69, v7\n"
"v_mul_lo_u32 v70, v69, s71\n"
"v_xor_b32 v70, v70, v7\n"
"v_mul_lo_u32 v71, v70, s71\n"
"v_xor_b32 v71, v71, v7\n"
"v_mul_lo_u32 v72, v71, s71\n"
"v_xor_b32 v72, v72, v7\n"
"v_mul_lo_u32 v73, v72, s71\n"
"v_xor_b32 v73, v73, v7\n"
"v_mul_lo_u32 v74, v73, s71\n"
"v_xor_b32 v74, v74, v7\n"
"v_mul_lo_u32 v75, v74, s71\n"
"v_xor_b32 v75, v75, v7\n"
"v_mul_lo_u32 v76, v75, s71\n"
"v_xor_b32 v76, v76, v7\n"
"v_mul_lo_u32 v77, v76, s71\n"
"v_xor_b32 v77, v77, v7\n"
"v_mul_lo_u32 v78, v77, s71\n"
"v_xor_b32 v78, v78, v7\n"
"v_mul_lo_u32 v79, v78, s71\n"
"v_xor_b32 v79, v79, v7\n"
"v_and_b32 v64, 0xbfffbfff, v64\n"
"v_or_b32 v64, 0x3c003c00, v64\n"
"v_and_b32 v65, 0xbfffbfff, v65\n"
"v_or_b32 v65, 0x3c003c00, v65\n"
"v_and_b32 v66, 0xbfffbfff, v66\n"
"v_or_b32 v66, 0x3c003c00, v66\n"
"v_and_b32 v67, 0xbfffbfff, v67\n"
"v_or_b32 v67, 0x3c003c00, v67\n"
"v_and_b32 v68, 0xbfffbfff, v68\n"
"v_or_b32 v68, 0x3c003c00, v68\n"
"v_and_b32 v69, 0xbfffbfff, v69\n"
"v_or_b32 v69, 0x3c003c00, v69\n"
"v_and_b32 v70, 0xbfffbfff, v70\n"
"v_or_b32 v70, 0x3c003c00, v70\n"
"v_and_b32 v71, 0xbfffbfff, v71\n"
"v_or_b32 v71, 0x3c003c00, v71\n"
"v_and_b32 v72, 0xbfffbfff, v72\n"
"v_or_b32 v72, 0x3c003c00, v72\n"
"v_and_b32 v73, 0xbfffbfff, v73\n"
"v_or_b32 v73, 0x3c003c00, v73\n"
"v_and_b32 v74, 0xbfffbfff, v74\n"
"v_or_b32 v74, 0x3c003c00, v74\n"
"v_and_b32 v75, 0xbfffbfff, v75\n"
"v_or_b32 v75, 0x3c003c00, v75\n"
"v_and_b32 v76, 0xbfffbfff, v76\n"
"v_or_b32 v76, 0x3c003c00, v76\n"
"v_and_b32 v77, 0xbfffbfff, v77\n"
"v_or_b32 v77, 0x3c003c00, v77\n"
"v_and_b32 v78, 0xbfffbfff, v78\n"
"v_or_b32 v78, 0x3c003c00, v78\n"
"v_and_b32 v79, 0xbfffbfff, v79\n"
"v_or_b32 v79, 0x3c003c00, v79\n"
"s_nop 1\n"
"s_add_u32 s87, s65, s96\n"
"buffer_load_dwordx4 a[96:99], v2, s[36:39], s87 offen\n"
"v_mfma_f32_32x32x16_bf16 v[80:95], v[64:67], v[68:71], 0\n"
"v_mfma_f32_32x32x16_bf16 v[80:95], v[68:71], v[72:75], 0\n"
"v_mfma_f32_32x32x16_bf16 v[80:95], v[72:75], v[76:79], 0\n"
"s_add_u32 s87, s65, s97\n"
"buffer_load_dwordx4 a[100:103], v2, s[36:39], s87 offen\n"
"v_mfma_f32_32x32x16_bf16 v[80:95], v[76:79], v[64:67], 0\n"
"v_mfma_f32_32x32x16_bf16 v[80:95], v[64:67], v[72:75], 0\n"
"v_mfma_f32_32x32x16_bf16 v[80:95], v[68:71], v[76:79], 0\n"
"s_add_u32 s87, s65, s98\n"
"buffer_load_dwordx4 a[104:107], v2, s[36:39], s87 offen\n"
"v_mfma_f32_32x32x16_bf16 v[80:95], v[72:75], v[64:67], 0\n"
"v_mfma_f32_32x32x16_bf16 v[80:95], v[76:79], v[68:71], 0\n"
"v_mfma_f32_32x32x16_bf16 v[80:95], v[64:67], v[76:79], 0\n"
"s_add_u32 s87, s65, s99\n"
"buffer_load_dwordx4 a[108:111], v2, s[36:39], s87 offen\n"
"v_mfma_f32_32x32x16_bf16 v[80:95], v[68:71], v[64:67], 0\n"
"v_mfma_f32_32x32x16_bf16 v[80:95], v[72:75], v[68:71], 0\n"
"v_mfma_f32_32x32x16_bf16 v[80:95], v[76:79], v[72:75], 0\n"
"s_add_u32 m0, s60, 8192\n"
"s_add_u32 s87, s81, 0\n"
"buffer_load_dwordx4 v3, s[40:43], s87 offen lds\n"
"v_mfma_f32_32x32x16_bf16 v[80:95], v[64:67], v[64:67], 0\n"
"v_mfma_f32_32x32x16_bf16 v[80:95], v[68:71], v[68:71], 0\n"
"v_mfma_f32_32x32x16_bf16 v[80:95], v[72:75], v[72:75], 0\n"
"s_add_u32 m0, s60, 12288\n"
"s_add_u32 s87, s81, 262144\n"
"buffer_load_dwordx4 v3, s[40:43], s87 offen lds\n"
"v_mfma_f32_32x32x16_bf16 v[80:95], v[76:79], v[76:79], 0\n"
"v_mfma_f32_32x32x16_bf16 v[80:95], v[64:67], v[68:71], 0\n"
"v_mfma_f32_32x32x16_bf16 v[80:95], v[68:71], v[72:75], 0\n"
"s_add_u32 m0, s60, 65536\n"
"s_mov_b32 s87, 0\n"
"buffer_load_dwordx4 v4, s[44:47], s87 offen nt lds\n"
"v_mfma_f32_32x32x16_bf16 v[80:95], v[72:75], v[76:79], 0\n"
"v_mfma_f32_32x32x16_bf16 v[80:95], v[76:79], v[64:67], 0\n"
"v_mfma_f32_32x32x16_bf16 v[80:95], v[64:67], v[72:75], 0\n"
"v_mov_b32 v24, v11\n"
"v_xor_b32 v25, 32, v11\n"
"v_xor_b32 v26, 64, v11\n"
"v_xor_b32 v27, 0x60, v11\n"
"s_mov_b32 s88, 0x3db8aa3b\n"
"s_mov_b32 s89, 0x3db8aa3b\n"
"s_waitcnt vmcnt(10)\n"
"ds_write_b128 v9, a[80:83] offset:0\n"
"ds_write_b128 v10, a[84:87] offset:1024\n"
"ds_write_b128 v9, a[88:91] offset:2048\n"
"ds_write_b128 v10, a[92:95] offset:3072\n"
"ds_read_b128 a[80:83], v24 offset:0\n"
"ds_read_b128 a[84:87], v25 offset:0\n"
"ds_read_b128 a[88:91], v26 offset:0\n"
"ds_read_b128 a[92:95], v27 offset:0\n"
"s_add_u32 s87, s66, s96\n"
"buffer_load_dwordx4 a[112:115], v2, s[36:39], s87 offen\n"
"v_mfma_f32_32x32x16_bf16 v[80:95], v[68:71], v[76:79], 0\n"
"v_mfma_f32_32x32x16_bf16 v[80:95], v[72:75], v[64:67], 0\n"
"v_mfma_f32_32x32x16_bf16 v[80:95], v[76:79], v[68:71], 0\n"
"s_add_u32 s87, s66, s97\n"
"buffer_load_dwordx4 a[116:119], v2, s[36:39], s87 offen\n"
"v_mfma_f32_32x32x16_bf16 v[80:95], v[64:67], v[76:79], 0\n"
"v_mfma_f32_32x32x16_bf16 v[80:95], v[68:71], v[64:67], 0\n"
"v_mfma_f32_32x32x16_bf16 v[80:95], v[72:75], v[68:71], 0\n"
"s_add_u32 s87, s66, s98\n"
"buffer_load_dwordx4 a[120:123], v2, s[36:39], s87 offen\n"
"v_mfma_f32_32x32x16_bf16 v[80:95], v[76:79], v[72:75], 0\n"
"v_mfma_f32_32x32x16_bf16 v[80:95], v[64:67], v[64:67], 0\n"
"v_mfma_f32_32x32x16_bf16 v[80:95], v[68:71], v[68:71], 0\n"
"s_add_u32 s87, s66, s99\n"
"buffer_load_dwordx4 a[124:127], v2, s[36:39], s87 offen\n"
"v_mfma_f32_32x32x16_bf16 v[80:95], v[72:75], v[72:75], 0\n"
"v_mfma_f32_32x32x16_bf16 v[80:95], v[76:79], v[76:79], 0\n"
"v_mfma_f32_32x32x16_bf16 v[80:95], v[64:67], v[68:71], 0\n"
"s_add_u32 m0, s60, 16384\n"
"s_add_u32 s87, s82, 0\n"
"buffer_load_dwordx4 v3, s[40:43], s87 offen lds\n"
"v_mfma_f32_32x32x16_bf16 v[80:95], v[68:71], v[72:75], 0\n"
"v_mfma_f32_32x32x16_bf16 v[80:95], v[72:75], v[76:79], 0\n"
"v_mfma_f32_32x32x16_bf16 v[80:95], v[76:79], v[64:67], 0\n"
"s_add_u32 m0, s60, 20480\n"
"s_add_u32 s87, s82, 262144\n"
"buffer_load_dwordx4 v3, s[40:43], s87 offen lds\n"
"v_mfma_f32_32x32x16_bf16 v[80:95], v[64:67], v[72:75], 0\n"
"v_mfma_f32_32x32x16_bf16 v[80:95], v[68:71], v[76:79], 0\n"
"v_mfma_f32_32x32x16_bf16 v[80:95], v[72:75], v[64:67], 0\n"
"s_add_u32 m0, s60, 69632\n"
"s_mov_b32 s87, 16384\n"
"buffer_load_dwordx4 v4, s[44:47], s87 offen nt lds\n"
"v_mfma_f32_32x32x16_bf16 v[80:95], v[76:79], v[68:71], 0\n"
"v_mfma_f32_32x32x16_bf16 v[80:95], v[64:67], v[76:79], 0\n"
"v_mfma_f32_32x32x16_bf16 v[80:95], v[68:71], v[64:67], 0\n"
"s_waitcnt vmcnt(10)\n"
"ds_write_b128 v9, a[96:99] offset:4096\n"
"ds_write_b128 v10, a[100:103] offset:5120\n"
"ds_write_b128 v9, a[104:107] offset:6144\n"
"ds_write_b128 v10, a[108:111] offset:7168\n"
"ds_read_b128 a[96:99], v24 offset:4096\n"
"ds_read_b128 a[100:103], v25 offset:4096\n"
"ds_read_b128 a[104:107], v26 offset:4096\n"
"s_waitcnt lgkmcnt(14)\n"
"ds_read_b128 a[108:111], v27 offset:4096\n"
"s_add_u32 s87, s67, s96\n"
"buffer_load_dwordx4 a[128:131], v2, s[36:39], s87 offen\n"
"v_mfma_f32_32x32x16_bf16 v[80:95], v[72:75], v[68:71], 0\n"
"v_mfma_f32_32x32x16_bf16 v[80:95], v[76:79], v[72:75], 0\n"
"v_mfma_f32_32x32x16_bf16 v[80:95], v[64:67], v[64:67], 0\n"
"s_add_u32 s87, s67, s97\n"
"buffer_load_dwordx4 a[132:135], v2, s[36:39], s87 offen\n"
"v_mfma_f32_32x32x16_bf16 v[80:95], v[68:71], v[68:71], 0\n"
"v_mfma_f32_32x32x16_bf16 v[80:95], v[72:75], v[72:75], 0\n"
"v_mfma_f32_32x32x16_bf16 v[80:95], v[76:79], v[76:79], 0\n"
"s_add_u32 s87, s67, s98\n"
"buffer_load_dwordx4 a[136:139], v2, s[36:39], s87 offen\n"
"v_mfma_f32_32x32x16_bf16 v[80:95], v[64:67], v[68:71], 0\n"
"v_mfma_f32_32x32x16_bf16 v[80:95], v[68:71], v[72:75], 0\n"
"v_mfma_f32_32x32x16_bf16 v[80:95], v[72:75], v[76:79], 0\n"
"s_add_u32 s87, s67, s99\n"
"buffer_load_dwordx4 a[140:143], v2, s[36:39], s87 offen\n"
"v_mfma_f32_32x32x16_bf16 v[80:95], v[76:79], v[64:67], 0\n"
"v_mfma_f32_32x32x16_bf16 v[80:95], v[64:67], v[72:75], 0\n"
"v_mfma_f32_32x32x16_bf16 v[80:95], v[68:71], v[76:79], 0\n"
"s_add_u32 m0, s60, 24576\n"
"s_add_u32 s87, s83, 0\n"
"buffer_load_dwordx4 v3, s[40:43], s87 offen lds\n"
"v_mfma_f32_32x32x16_bf16 v[80:95], v[72:75], v[64:67], 0\n"
"v_mfma_f32_32x32x16_bf16 v[80:95], v[76:79], v[68:71], 0\n"
"v_mfma_f32_32x32x16_bf16 v[80:95], v[64:67], v[76:79], 0\n"
"s_add_u32 m0, s60, 32768\n"
"s_add_u32 s87, s83, 262144\n"
"buffer_load_dwordx4 v3, s[40:43], s87 offen lds\n"
"v_mfma_f32_32x32x16_bf16 v[80:95], v[68:71], v[64:67], 0\n"
"v_mfma_f32_32x32x16_bf16 v[80:95], v[72:75], v[68:71], 0\n"
"v_mfma_f32_32x32x16_bf16 v[80:95], v[76:79], v[72:75], 0\n"
"s_add_u32 m0, s60, 73728\n"
"s_mov_b32 s87, 32768\n"
"buffer_load_dwordx4 v4, s[44:47], s87 offen nt lds\n"
"v_mfma_f32_32x32x16_bf16 v[80:95], v[64:67], v[64:67], 0\n"
"v_mfma_f32_32x32x16_bf16 v[80:95], v[68:71], v[68:71], 0\n"
"v_mfma_f32_32x32x16_bf16 v[80:95], v[72:75], v[72:75], 0\n"
"s_waitcnt vmcnt(10)\n"
"s_waitcnt lgkmcnt(14)\n"
"ds_write_b128 v9, a[112:115] offset:0\n"
"s_waitcnt lgkmcnt(14)\n"
"ds_write_b128 v10, a[116:119] offset:1024\n"
"s_waitcnt lgkmcnt(14)\n"
"ds_write_b128 v9, a[120:123] offset:2048\n"
"s_waitcnt lgkmcnt(14)\n"
"ds_write_b128 v10, a[124:127] offset:3072\n"
"s_waitcnt lgkmcnt(14)\n"
"ds_read_b128 a[112:115], v24 offset:0\n"
"s_waitcnt lgkmcnt(14)\n"
"ds_read_b128 a[116:119], v25 offset:0\n"
"s_waitcnt lgkmcnt(14)\n"
"ds_read_b128 a[120:123], v26 offset:0\n"
"s_waitcnt lgkmcnt(14)\n"
"ds_read_b128 a[124:127], v27 offset:0\n"
"s_add_u32 s87, s68, s96\n"
"buffer_load_dwordx4 a[144:147], v2, s[36:39], s87 offen\n"
"v_mfma_f32_32x32x16_bf16 v[80:95], v[76:79], v[76:79], 0\n"
"v_mfma_f32_32x32x16_bf16 v[80:95], v[64:67], v[68:71], 0\n"
"v_mfma_f32_32x32x16_bf16 v[80:95], v[68:71], v[72:75], 0\n"
"s_add_u32 s87, s68, s97\n"
"buffer_load_dwordx4 a[148:151], v2, s[36:39], s87 offen\n"
"v_mfma_f32_32x32x16_bf16 v[80:95], v[72:75], v[76:79], 0\n"
"v_mfma_f32_32x32x16_bf16 v[80:95], v[76:79], v[64:67], 0\n"
"v_mfma_f32_32x32x16_bf16 v[80:95], v[64:67], v[72:75], 0\n"
"s_add_u32 s87, s68, s98\n"
"buffer_load_dwordx4 a[152:155], v2, s[36:39], s87 offen\n"
"v_mfma_f32_32x32x16_bf16 v[80:95], v[68:71], v[76:79], 0\n"
"v_mfma_f32_32x32x16_bf16 v[80:95], v[72:75], v[64:67], 0\n"
"v_mfma_f32_32x32x16_bf16 v[80:95], v[76:79], v[68:71], 0\n"
"s_add_u32 s87, s68, s99\n"
"buffer_load_dwordx4 a[156:159], v2, s[36:39], s87 offen\n"
"v_mfma_f32_32x32x16_bf16 v[80:95], v[64:67], v[76:79], 0\n"
"v_mfma_f32_32x32x16_bf16 v[80:95], v[68:71], v[64:67], 0\n"
"v_mfma_f32_32x32x16_bf16 v[80:95], v[72:75], v[68:71], 0\n"
"s_add_u32 m0, s60, 36864\n"
"s_add_u32 s87, s84, 0\n"
"buffer_load_dwordx4 v3, s[40:43], s87 offen lds\n"
"v_mfma_f32_32x32x16_bf16 v[80:95], v[76:79], v[72:75], 0\n"
"v_mfma_f32_32x32x16_bf16 v[80:95], v[64:67], v[64:67], 0\n"
"v_mfma_f32_32x32x16_bf16 v[80:95], v[68:71], v[68:71], 0\n"
"s_add_u32 m0, s60, 40960\n"
"s_add_u32 s87, s84, 262144\n"
"buffer_load_dwordx4 v3, s[40:43], s87 offen lds\n"
"v_mfma_f32_32x32x16_bf16 v[80:95], v[72:75], v[72:75], 0\n"
"v_mfma_f32_32x32x16_bf16 v[80:95], v[76:79], v[76:79], 0\n"
"v_mfma_f32_32x32x16_bf16 v[80:95], v[64:67], v[68:71], 0\n"
"s_add_u32 m0, s60, 77824\n"
"s_mov_b32 s87, 49152\n"
"buffer_load_dwordx4 v4, s[44:47], s87 offen nt lds\n"
"v_mfma_f32_32x32x16_bf16 v[80:95], v[68:71], v[72:75], 0\n"
"v_mfma_f32_32x32x16_bf16 v[80:95], v[72:75], v[76:79], 0\n"
"v_mfma_f32_32x32x16_bf16 v[80:95], v[76:79], v[64:67], 0\n"
"s_waitcnt vmcnt(10)\n"
"s_waitcnt lgkmcnt(14)\n"
"ds_write_b128 v9, a[128:131] offset:4096\n"
"s_waitcnt lgkmcnt(14)\n"
"ds_write_b128 v10, a[132:135] offset:5120\n"
"s_waitcnt lgkmcnt(14)\n"
"ds_write_b128 v9, a[136:139] offset:6144\n"
"s_waitcnt lgkmcnt(14)\n"
"ds_write_b128 v10, a[140:143] offset:7168\n"
"s_waitcnt lgkmcnt(14)\n"
"ds_read_b128 a[128:131], v24 offset:4096\n"
"s_waitcnt lgkmcnt(14)\n"
"ds_read_b128 a[132:135], v25 offset:4096\n"
"s_waitcnt lgkmcnt(14)\n"
"ds_read_b128 a[136:139], v26 offset:4096\n"
"s_waitcnt lgkmcnt(14)\n"
"ds_read_b128 a[140:143], v27 offset:4096\n"
"s_add_u32 s87, s69, s96\n"
"buffer_load_dwordx4 a[160:163], v2, s[36:39], s87 offen\n"
"v_mfma_f32_32x32x16_bf16 v[80:95], v[64:67], v[72:75], 0\n"
"v_mfma_f32_32x32x16_bf16 v[80:95], v[68:71], v[76:79], 0\n"
"v_mfma_f32_32x32x16_bf16 v[80:95], v[72:75], v[64:67], 0\n"
"s_add_u32 s87, s69, s97\n"
"buffer_load_dwordx4 a[164:167], v2, s[36:39], s87 offen\n"
"v_mfma_f32_32x32x16_bf16 v[80:95], v[76:79], v[68:71], 0\n"
"v_mfma_f32_32x32x16_bf16 v[80:95], v[64:67], v[76:79], 0\n"
"v_mfma_f32_32x32x16_bf16 v[80:95], v[68:71], v[64:67], 0\n"
"s_add_u32 s87, s69, s98\n"
"buffer_load_dwordx4 a[168:171], v2, s[36:39], s87 offen\n"
"v_mfma_f32_32x32x16_bf16 v[80:95], v[72:75], v[68:71], 0\n"
"v_mfma_f32_32x32x16_bf16 v[80:95], v[76:79], v[72:75], 0\n"
"v_mfma_f32_32x32x16_bf16 v[80:95], v[64:67], v[64:67], 0\n"
"s_add_u32 s87, s69, s99\n"
"buffer_load_dwordx4 a[172:175], v2, s[36:39], s87 offen\n"
"v_mfma_f32_32x32x16_bf16 v[80:95], v[68:71], v[68:71], 0\n"
"v_mfma_f32_32x32x16_bf16 v[80:95], v[72:75], v[72:75], 0\n"
"v_mfma_f32_32x32x16_bf16 v[80:95], v[76:79], v[76:79], 0\n"
"s_add_u32 m0, s60, 45056\n"
"s_add_u32 s87, s85, 0\n"
"buffer_load_dwordx4 v3, s[40:43], s87 offen lds\n"
"v_mfma_f32_32x32x16_bf16 v[80:95], v[64:67], v[68:71], 0\n"
"v_mfma_f32_32x32x16_bf16 v[80:95], v[68:71], v[72:75], 0\n"
"v_mfma_f32_32x32x16_bf16 v[80:95], v[72:75], v[76:79], 0\n"
"s_add_u32 m0, s60, 49152\n"
"s_add_u32 s87, s85, 262144\n"
"buffer_load_dwordx4 v3, s[40:43], s87 offen lds\n"
"v_mfma_f32_32x32x16_bf16 v[80:95], v[76:79], v[64:67], 0\n"
"v_mfma_f32_32x32x16_bf16 v[80:95], v[64:67], v[72:75], 0\n"
"v_mfma_f32_32x32x16_bf16 v[80:95], v[68:71], v[76:79], 0\n"
"s_add_u32 m0, s60, 81920\n"
"s_mov_b32 s87, 65536\n"
"buffer_load_dwordx4 v4, s[44:47], s87 offen nt lds\n"
"v_mfma_f32_32x32x16_bf16 v[80:95], v[72:75], v[64:67], 0\n"
"v_mfma_f32_32x32x16_bf16 v[80:95], v[76:79], v[68:71], 0\n"
"v_mfma_f32_32x32x16_bf16 v[80:95], v[64:67], v[76:79], 0\n"
"s_barrier\n"
"v_mov_b32 v20, v7\n"
"v_xor_b32 v21, 64, v20\n"
"s_waitcnt lgkmcnt(14)\n"
"ds_read_b64_tr_b16 a[192:193], v20 offset:0\n"
"s_waitcnt lgkmcnt(14)\n"
"ds_read_b64_tr_b16 a[194:195], v20 offset:512\n"
"s_waitcnt lgkmcnt(14)\n"
"ds_read_b64_tr_b16 a[196:197], v21 offset:0\n"
"s_waitcnt lgkmcnt(14)\n"
"ds_read_b64_tr_b16 a[198:199], v21 offset:512\n"
"s_waitcnt lgkmcnt(14)\n"
"ds_read_b64_tr_b16 a[200:201], v20 offset:2048\n"
"s_waitcnt lgkmcnt(14)\n"
"ds_read_b64_tr_b16 a[202:203], v20 offset:2560\n"
"s_waitcnt lgkmcnt(14)\n"
"ds_read_b64_tr_b16 a[204:205], v21 offset:2048\n"
"s_waitcnt lgkmcnt(14)\n"
"ds_read_b64_tr_b16 a[206:207], v21 offset:2560\n"
"s_waitcnt lgkmcnt(6)\n"
"v_mfma_f32_32x32x16_bf16 v[32:47], a[192:195], a[80:83], 0\n"
"s_waitcnt vmcnt(15)\n"
"s_barrier\n"
"ds_read_b64_tr_b16 a[208:209], v20 offset:4096\n"
"ds_read_b64_tr_b16 a[210:211], v20 offset:4608\n"
"s_add_u32 s87, s70, s96\n"
"buffer_load_dwordx4 a[176:179], v2, s[36:39], s87 offen\n"
"s_add_u32 s87, s70, s97\n"
"buffer_load_dwordx4 a[180:183], v2, s[36:39], s87 offen\n"
"s_add_u32 s87, s70, s98\n"
"buffer_load_dwordx4 a[184:187], v2, s[36:39], s87 offen\n"
"s_add_u32 s87, s70, s99\n"
"buffer_load_dwordx4 a[188:191], v2, s[36:39], s87 offen\n"
"s_add_u32 m0, s60, 53248\n"
"s_add_u32 s87, s86, 0\n"
"buffer_load_dwordx4 v3, s[40:43], s87 offen lds\n"
"s_add_u32 m0, s60, 57344\n"
"s_add_u32 s87, s86, 262144\n"
"buffer_load_dwordx4 v3, s[40:43], s87 offen lds\n"
"s_waitcnt lgkmcnt(6)\n"
"v_mfma_f32_32x32x16_bf16 v[48:63], a[196:199], a[80:83], 0\n"
"ds_read_b64_tr_b16 a[212:213], v21 offset:4096\n"
"ds_read_b64_tr_b16 a[214:215], v21 offset:4608\n"
"s_waitcnt vmcnt(16)\n"
"s_waitcnt lgkmcnt(6)\n"
"v_mfma_f32_32x32x16_bf16 v[32:47], a[200:203], a[84:87], v[32:47]\n"
"ds_read_b64_tr_b16 a[216:217], v20 offset:6144\n"
"ds_read_b64_tr_b16 a[218:219], v20 offset:6656\n"
"ds_write_b128 v9, a[144:147] offset:0\n"
"s_add_u32 m0, s60, 86016\n"
"s_mov_b32 s87, 81920\n"
"buffer_load_dwordx4 v4, s[44:47], s87 offen nt lds\n"
"s_waitcnt lgkmcnt(7)\n"
"v_mfma_f32_32x32x16_bf16 v[48:63], a[204:207], a[84:87], v[48:63]\n"
"ds_read_b64_tr_b16 a[220:221], v21 offset:6144\n"
"ds_read_b64_tr_b16 a[222:223], v21 offset:6656\n"
"ds_write_b128 v10, a[148:151] offset:1024\n"
"s_waitcnt lgkmcnt(8)\n"
"v_mfma_f32_32x32x16_bf16 v[32:47], a[208:211], a[88:91], v[32:47]\n"
"ds_read_b64_tr_b16 a[192:193], v20 offset:8192\n"
"ds_read_b64_tr_b16 a[194:195], v20 offset:8704\n"
"ds_write_b128 v9, a[152:155] offset:2048\n"
"s_add_u32 m0, s60, 98304\n"
"s_add_u32 s87, s80, 128\n"
"buffer_load_dwordx4 v3, s[40:43], s87 offen lds\n"
"s_waitcnt lgkmcnt(9)\n"
"v_mfma_f32_32x32x16_bf16 v[48:63], a[212:215], a[88:91], v[48:63]\n"
"ds_read_b64_tr_b16 a[196:197], v21 offset:8192\n"
"ds_read_b64_tr_b16 a[198:199], v21 offset:8704\n"
"ds_write_b128 v10, a[156:159] offset:3072\n"
"s_waitcnt lgkmcnt(10)\n"
"v_mfma_f32_32x32x16_bf16 v[32:47], a[216:219], a[92:95], v[32:47]\n"
"ds_read_b64_tr_b16 a[200:201], v20 offset:10240\n"
"ds_read_b64_tr_b16 a[202:203], v20 offset:10752\n"
"ds_read_b128 a[144:147], v24 offset:0\n"
"s_add_u32 m0, s60, 90112\n"
"s_mov_b32 s87, 98304\n"
"buffer_load_dwordx4 v4, s[44:47], s87 offen nt lds\n"
"s_waitcnt lgkmcnt(10)\n"
"v_mfma_f32_32x32x16_bf16 v[48:63], a[220:223], a[92:95], v[48:63]\n"
"ds_read_b64_tr_b16 a[204:205], v21 offset:10240\n"
"ds_read_b64_tr_b16 a[206:207], v21 offset:10752\n"
"ds_read_b128 a[148:151], v25 offset:0\n"
"s_waitcnt lgkmcnt(10)\n"
"v_mfma_f32_32x32x16_bf16 v[32:47], a[192:195], a[96:99], v[32:47]\n"
"ds_read_b64_tr_b16 a[208:209], v20 offset:12288\n"
"ds_read_b64_tr_b16 a[210:211], v20 offset:12800\n"
"ds_read_b128 a[152:155], v26 offset:0\n"
"s_add_u32 m0, s60, 102400\n"
"s_add_u32 s87, s80, 262272\n"
"buffer_load_dwordx4 v3, s[40:43], s87 offen lds\n"
"s_waitcnt lgkmcnt(10)\n"
"v_mfma_f32_32x32x16_bf16 v[48:63], a[196:199], a[96:99], v[48:63]\n"
"ds_read_b64_tr_b16 a[212:213], v21 offset:12288\n"
"ds_read_b64_tr_b16 a[214:215], v21 offset:12800\n"
"ds_read_b128 a[156:159], v27 offset:0\n"
"s_waitcnt lgkmcnt(10)\n"
"v_mfma_f32_32x32x16_bf16 v[32:47], a[200:203], a[100:103], v[32:47]\n"
"ds_read_b64_tr_b16 a[216:217], v20 offset:14336\n"
"ds_read_b64_tr_b16 a[218:219], v20 offset:14848\n"
"s_waitcnt vmcnt(13)\n"
"s_add_u32 m0, s60, 94208\n"
"s_mov_b32 s87, 114688\n"
"buffer_load_dwordx4 v4, s[44:47], s87 offen nt lds\n"
"s_waitcnt lgkmcnt(9)\n"
"v_mfma_f32_32x32x16_bf16 v[48:63], a[204:207], a[100:103], v[48:63]\n"
"ds_read_b64_tr_b16 a[220:221], v21 offset:14336\n"
"ds_read_b64_tr_b16 a[222:223], v21 offset:14848\n"
"ds_write_b128 v9, a[160:163] offset:4096\n"
"s_waitcnt lgkmcnt(9)\n"
"v_mfma_f32_32x32x16_bf16 v[32:47], a[208:211], a[104:107], v[32:47]\n"
"ds_read_b64_tr_b16 a[192:193], v20 offset:16384\n"
"ds_read_b64_tr_b16 a[194:195], v20 offset:16896\n"
"ds_write_b128 v10, a[164:167] offset:5120\n"
"s_add_u32 m0, s60, 106496\n"
"s_add_u32 s87, s81, 128\n"
"buffer_load_dwordx4 v3, s[40:43], s87 offen lds\n"
"s_waitcnt lgkmcnt(9)\n"
"v_mfma_f32_32x32x16_bf16 v[48:63], a[212:215], a[104:107], v[48:63]\n"
"ds_read_b64_tr_b16 a[196:197], v21 offset:16384\n"
"ds_read_b64_tr_b16 a[198:199], v21 offset:16896\n"
"ds_write_b128 v9, a[168:171] offset:6144\n"
"s_waitcnt lgkmcnt(9)\n"
"v_mfma_f32_32x32x16_bf16 v[32:47], a[216:219], a[108:111], v[32:47]\n"
"ds_read_b64_tr_b16 a[200:201], v20 offset:18432\n"
"ds_read_b64_tr_b16 a[202:203], v20 offset:18944\n"
"ds_write_b128 v10, a[172:175] offset:7168\n"
"s_add_u32 m0, s60, 110592\n"
"s_add_u32 s87, s81, 262272\n"
"buffer_load_dwordx4 v3, s[40:43], s87 offen lds\n"
"s_waitcnt lgkmcnt(10)\n"
"v_mfma_f32_32x32x16_bf16 v[48:63], a[220:223], a[108:111], v[48:63]\n"
"ds_read_b64_tr_b16 a[204:205], v21 offset:18432\n"
"ds_read_b64_tr_b16 a[206:207], v21 offset:18944\n"
"ds_read_b128 a[160:163], v24 offset:4096\n"
"s_waitcnt lgkmcnt(10)\n"
"v_mfma_f32_32x32x16_bf16 v[32:47], a[192:195], a[112:115], v[32:47]\n"
"ds_read_b64_tr_b16 a[208:209], v20 offset:20480\n"
"ds_read_b64_tr_b16 a[210:211], v20 offset:20992\n"
"ds_read_b128 a[164:167], v25 offset:4096\n"
"s_add_u32 m0, s60, 114688\n"
"s_add_u32 s87, s82, 128\n"
"buffer_load_dwordx4 v3, s[40:43], s87 offen lds\n"
"s_waitcnt lgkmcnt(10)\n"
"v_mfma_f32_32x32x16_bf16 v[48:63], a[196:199], a[112:115], v[48:63]\n"
"ds_read_b64_tr_b16 a[212:213], v21 offset:20480\n"
"ds_read_b64_tr_b16 a[214:215], v21 offset:20992\n"
"ds_read_b128 a[168:171], v26 offset:4096\n"
"s_waitcnt lgkmcnt(10)\n"
"v_mfma_f32_32x32x16_bf16 v[32:47], a[200:203], a[116:119], v[32:47]\n"
"ds_read_b64_tr_b16 a[216:217], v20 offset:22528\n"
"ds_read_b64_tr_b16 a[218:219], v20 offset:23040\n"
"ds_read_b128 a[172:175], v27 offset:4096\n"
"s_add_u32 m0, s60, 118784\n"
"s_add_u32 s87, s82, 262272\n"
"buffer_load_dwordx4 v3, s[40:43], s87 offen lds\n"
"s_waitcnt lgkmcnt(10)\n"
"v_mfma_f32_32x32x16_bf16 v[48:63], a[204:207], a[116:119], v[48:63]\n"
"ds_read_b64_tr_b16 a[220:221], v21 offset:22528\n"
"ds_read_b64_tr_b16 a[222:223], v21 offset:23040\n"
"s_waitcnt vmcnt(11)\n"
"s_waitcnt lgkmcnt(9)\n"
"v_mfma_f32_32x32x16_bf16 v[32:47], a[208:211], a[120:123], v[32:47]\n"
"ds_read_b64_tr_b16 a[192:193], v20 offset:24576\n"
"ds_read_b64_tr_b16 a[194:195], v20 offset:25088\n"
"ds_write_b128 v9, a[176:179] offset:0\n"
"s_add_u32 m0, s60, 122880\n"
"s_add_u32 s87, s83, 128\n"
"buffer_load_dwordx4 v3, s[40:43], s87 offen lds\n"
"s_waitcnt lgkmcnt(9)\n"
"v_mfma_f32_32x32x16_bf16 v[48:63], a[212:215], a[120:123], v[48:63]\n"
"ds_read_b64_tr_b16 a[196:197], v21 offset:24576\n"
"ds_read_b64_tr_b16 a[198:199], v21 offset:25088\n"
"ds_write_b128 v10, a[180:183] offset:1024\n"
"s_waitcnt lgkmcnt(9)\n"
"v_mfma_f32_32x32x16_bf16 v[32:47], a[216:219], a[124:127], v[32:47]\n"
"ds_read_b64_tr_b16 a[200:201], v20 offset:26624\n"
"ds_read_b64_tr_b16 a[202:203], v20 offset:27136\n"
"ds_write_b128 v9, a[184:187] offset:2048\n"
"s_waitcnt lgkmcnt(9)\n"
"v_mfma_f32_32x32x16_bf16 v[48:63], a[220:223], a[124:127], v[48:63]\n"
"ds_read_b64_tr_b16 a[204:205], v21 offset:26624\n"
"ds_read_b64_tr_b16 a[206:207], v21 offset:27136\n"
"v_add_u32 v20, 32768, v7\n"
"v_xor_b32 v21, 64, v20\n"
"ds_write_b128 v10, a[188:191] offset:3072\n"
"s_waitcnt lgkmcnt(10)\n"
"v_mfma_f32_32x32x16_bf16 v[32:47], a[192:195], a[128:131], v[32:47]\n"
"ds_read_b64_tr_b16 a[208:209], v20 offset:0\n"
"ds_read_b64_tr_b16 a[210:211], v20 offset:512\n"
"ds_read_b128 a[176:179], v24 offset:0\n"
"s_waitcnt lgkmcnt(10)\n"
"v_mfma_f32_32x32x16_bf16 v[48:63], a[196:199], a[128:131], v[48:63]\n"
"ds_read_b64_tr_b16 a[212:213], v21 offset:0\n"
"ds_read_b64_tr_b16 a[214:215], v21 offset:512\n"
"ds_read_b128 a[180:183], v25 offset:0\n"
"s_waitcnt lgkmcnt(10)\n"
"v_mfma_f32_32x32x16_bf16 v[32:47], a[200:203], a[132:135], v[32:47]\n"
"ds_read_b64_tr_b16 a[216:217], v20 offset:2048\n"
"ds_read_b64_tr_b16 a[218:219], v20 offset:2560\n"
"ds_read_b128 a[184:187], v26 offset:0\n"
"s_waitcnt lgkmcnt(10)\n"
"v_mfma_f32_32x32x16_bf16 v[48:63], a[204:207], a[132:135], v[48:63]\n"
"ds_read_b64_tr_b16 a[220:221], v21 offset:2048\n"
"ds_read_b64_tr_b16 a[222:223], v21 offset:2560\n"
"ds_read_b128 a[188:191], v27 offset:0\n"
"s_waitcnt lgkmcnt(0)\n"
"v_mfma_f32_32x32x16_bf16 v[32:47], a[208:211], a[136:139], v[32:47]\n"
"s_nop 5\n"
"s_waitcnt vmcnt(10)\n"
"s_barrier\n"
"ds_read_b64_tr_b16 a[192:193], v20 offset:4096\n"
"ds_read_b64_tr_b16 a[194:195], v20 offset:4608\n"
"s_add_u32 m0, s60, 131072\n"
"s_mov_b32 s87, 131072\n"
"buffer_load_dwordx4 v4, s[44:47], s87 offen nt lds\n"
"v_mfma_f32_32x32x16_bf16 v[48:63], a[212:215], a[136:139], v[48:63]\n"
"s_nop 5\n"
"ds_read_b64_tr_b16 a[196:197], v21 offset:4096\n"
"ds_read_b64_tr_b16 a[198:199], v21 offset:4608\n"
"v_mfma_f32_32x32x16_bf16 v[32:47], a[216:219], a[140:143], v[32:47]\n"
"s_nop 5\n"
"ds_read_b64_tr_b16 a[200:201], v20 offset:6144\n"
"ds_read_b64_tr_b16 a[202:203], v20 offset:6656\n"
"s_add_u32 m0, s60, 0\n"
"s_add_u32 s87, s83, 262272\n"
"buffer_load_dwordx4 v3, s[40:43], s87 offen lds\n"
"v_mfma_f32_32x32x16_bf16 v[48:63], a[220:223], a[140:143], v[48:63]\n"
"s_nop 5\n"
"ds_read_b64_tr_b16 a[204:205], v21 offset:6144\n"
"ds_read_b64_tr_b16 a[206:207], v21 offset:6656\n"
"s_waitcnt lgkmcnt(6)\n"
"v_mfma_f32_32x32x16_bf16 v[32:47], a[192:195], a[144:147], v[32:47]\n"
"s_nop 5\n"
"ds_read_b64_tr_b16 a[208:209], v20 offset:8192\n"
"ds_read_b64_tr_b16 a[210:211], v20 offset:8704\n"
"s_add_u32 m0, s60, 135168\n"
"s_mov_b32 s87, 147456\n"
"buffer_load_dwordx4 v4, s[44:47], s87 offen nt lds\n"
"s_waitcnt lgkmcnt(6)\n"
"v_mfma_f32_32x32x16_bf16 v[48:63], a[196:199], a[144:147], v[48:63]\n"
"s_nop 5\n"
"ds_read_b64_tr_b16 a[212:213], v21 offset:8192\n"
"ds_read_b64_tr_b16 a[214:215], v21 offset:8704\n"
"s_waitcnt lgkmcnt(6)\n"
"v_mfma_f32_32x32x16_bf16 v[32:47], a[200:203], a[148:151], v[32:47]\n"
"s_nop 5\n"
"ds_read_b64_tr_b16 a[216:217], v20 offset:10240\n"
"ds_read_b64_tr_b16 a[218:219], v20 offset:10752\n"
"s_add_u32 m0, s60, 4096\n"
"s_add_u32 s87, s84, 128\n"
"buffer_load_dwordx4 v3, s[40:43], s87 offen lds\n"
"s_waitcnt lgkmcnt(6)\n"
"v_mfma_f32_32x32x16_bf16 v[48:63], a[204:207], a[148:151], v[48:63]\n"
"s_nop 5\n"
"ds_read_b64_tr_b16 a[220:221], v21 offset:10240\n"
"ds_read_b64_tr_b16 a[222:223], v21 offset:10752\n"
"s_waitcnt lgkmcnt(6)\n"
"v_mfma_f32_32x32x16_bf16 v[32:47], a[208:211], a[152:155], v[32:47]\n"
"s_nop 5\n"
"ds_read_b64_tr_b16 a[192:193], v20 offset:12288\n"
"ds_read_b64_tr_b16 a[194:195], v20 offset:12800\n"
"s_add_u32 m0, s60, 139264\n"
"s_mov_b32 s87, 163840\n"
"buffer_load_dwordx4 v4, s[44:47], s87 offen nt lds\n"
"s_waitcnt lgkmcnt(6)\n"
"v_mfma_f32_32x32x16_bf16 v[48:63], a[212:215], a[152:155], v[48:63]\n"
"s_nop 5\n"
"ds_read_b64_tr_b16 a[196:197], v21 offset:12288\n"
"ds_read_b64_tr_b16 a[198:199], v21 offset:12800\n"
"s_waitcnt lgkmcnt(6)\n"
"v_mfma_f32_32x32x16_bf16 v[32:47], a[216:219], a[156:159], v[32:47]\n"
"s_nop 5\n"
"ds_read_b64_tr_b16 a[200:201], v20 offset:14336\n"
"ds_read_b64_tr_b16 a[202:203], v20 offset:14848\n"
"s_add_u32 m0, s60, 8192\n"
"s_add_u32 s87, s84, 262272\n"
"buffer_load_dwordx4 v3, s[40:43], s87 offen lds\n"
"s_waitcnt lgkmcnt(6)\n"
"v_mfma_f32_32x32x16_bf16 v[48:63], a[220:223], a[156:159], v[48:63]\n"
"s_nop 5\n"
"ds_read_b64_tr_b16 a[204:205], v21 offset:14336\n"
"ds_read_b64_tr_b16 a[206:207], v21 offset:14848\n"
"s_waitcnt lgkmcnt(6)\n"
"v_mfma_f32_32x32x16_bf16 v[32:47], a[192:195], a[160:163], v[32:47]\n"
"s_nop 5\n"
"ds_read_b64_tr_b16 a[208:209], v20 offset:16384\n"
"ds_read_b64_tr_b16 a[210:211], v20 offset:16896\n"
"s_add_u32 m0, s60, 143360\n"
"s_mov_b32 s87, 180224\n"
"buffer_load_dwordx4 v4, s[44:47], s87 offen nt lds\n"
"s_waitcnt lgkmcnt(6)\n"
"v_mfma_f32_32x32x16_bf16 v[48:63], a[196:199], a[160:163], v[48:63]\n"
"s_nop 5\n"
"ds_read_b64_tr_b16 a[212:213], v21 offset:16384\n"
"ds_read_b64_tr_b16 a[214:215], v21 offset:16896\n"
"s_waitcnt lgkmcnt(6)\n"
"v_mfma_f32_32x32x16_bf16 v[32:47], a[200:203], a[164:167], v[32:47]\n"
"s_nop 5\n"
"ds_read_b64_tr_b16 a[216:217], v20 offset:18432\n"
"ds_read_b64_tr_b16 a[218:219], v20 offset:18944\n"
"s_add_u32 m0, s60, 12288\n"
"s_add_u32 s87, s85, 128\n"
"buffer_load_dwordx4 v3, s[40:43], s87 offen lds\n"
"s_waitcnt lgkmcnt(6)\n"
"v_mfma_f32_32x32x16_bf16 v[48:63], a[204:207], a[164:167], v[48:63]\n"
"s_nop 5\n"
"ds_read_b64_tr_b16 a[220:221], v21 offset:18432\n"
"ds_read_b64_tr_b16 a[222:223], v21 offset:18944\n"
"s_waitcnt lgkmcnt(6)\n"
"v_mfma_f32_32x32x16_bf16 v[32:47], a[208:211], a[168:171], v[32:47]\n"
"s_nop 5\n"
"ds_read_b64_tr_b16 a[192:193], v20 offset:20480\n"
"ds_read_b64_tr_b16 a[194:195], v20 offset:20992\n"
"s_add_u32 m0, s60, 147456\n"
"s_mov_b32 s87, 196608\n"
"buffer_load_dwordx4 v4, s[44:47], s87 offen nt lds\n"
"s_waitcnt lgkmcnt(6)\n"
"v_mfma_f32_32x32x16_bf16 v[48:63], a[212:215], a[168:171], v[48:63]\n"
"s_nop 5\n"
"ds_read_b64_tr_b16 a[196:197], v21 offset:20480\n"
"ds_read_b64_tr_b16 a[198:199], v21 offset:20992\n"
"s_waitcnt lgkmcnt(6)\n"
"v_mfma_f32_32x32x16_bf16 v[32:47], a[216:219], a[172:175], v[32:47]\n"
"s_nop 5\n"
"ds_read_b64_tr_b16 a[200:201], v20 offset:22528\n"
"ds_read_b64_tr_b16 a[202:203], v20 offset:23040\n"
"s_add_u32 m0, s60, 16384\n"
"s_add_u32 s87, s85, 262272\n"
"buffer_load_dwordx4 v3, s[40:43], s87 offen lds\n"
"s_waitcnt lgkmcnt(6)\n"
"v_mfma_f32_32x32x16_bf16 v[48:63], a[220:223], a[172:175], v[48:63]\n"
"s_nop 5\n"
"ds_read_b64_tr_b16 a[204:205], v21 offset:22528\n"
"ds_read_b64_tr_b16 a[206:207], v21 offset:23040\n"
"s_waitcnt lgkmcnt(6)\n"
"v_mfma_f32_32x32x16_bf16 v[32:47], a[192:195], a[176:179], v[32:47]\n"
"s_nop 5\n"
"ds_read_b64_tr_b16 a[208:209], v20 offset:24576\n"
"ds_read_b64_tr_b16 a[210:211], v20 offset:25088\n"
"s_add_u32 m0, s60, 151552\n"
"s_mov_b32 s87, 212992\n"
"buffer_load_dwordx4 v4, s[44:47], s87 offen nt lds\n"
"s_waitcnt lgkmcnt(6)\n"
"v_mfma_f32_32x32x16_bf16 v[48:63], a[196:199], a[176:179], v[48:63]\n"
"s_nop 5\n"
"ds_read_b64_tr_b16 a[216:217], v20 offset:26624\n"
"ds_read_b64_tr_b16 a[218:219], v20 offset:27136\n"
"s_waitcnt lgkmcnt(6)\n"
"v_mfma_f32_32x32x16_bf16 v[32:47], a[200:203], a[180:183], v[32:47]\n"
"s_nop 5\n"
"ds_read_b64_tr_b16 a[212:213], v21 offset:24576\n"
"ds_read_b64_tr_b16 a[214:215], v21 offset:25088\n"
"s_add_u32 m0, s60, 20480\n"
"s_add_u32 s87, s86, 128\n"
"buffer_load_dwordx4 v3, s[40:43], s87 offen lds\n"
"s_waitcnt lgkmcnt(6)\n"
"v_mfma_f32_32x32x16_bf16 v[48:63], a[204:207], a[180:183], v[48:63]\n"
"s_nop 5\n"
"ds_read_b64_tr_b16 a[220:221], v21 offset:26624\n"
"ds_read_b64_tr_b16 a[222:223], v21 offset:27136\n"
"v_add_u32 v20, 65536, v8\n"
"v_xor_b32 v21, 32, v20\n"
"v_xor_b32 v22, 64, v20\n"
"v_xor_b32 v23, 0x60, v20\n"
"s_waitcnt lgkmcnt(6)\n"
"v_mfma_f32_32x32x16_bf16 v[32:47], a[208:211], a[184:187], v[32:47]\n"
"s_nop 5\n"
"ds_read_b128 a[192:195], v20 offset:0\n"
"s_add_u32 m0, s60, 155648\n"
"s_mov_b32 s87, 229376\n"
"buffer_load_dwordx4 v4, s[44:47], s87 offen nt lds\n"
"s_waitcnt lgkmcnt(5)\n"
"v_mfma_f32_32x32x16_bf16 v[32:47], a[216:219], a[188:191], v[32:47]\n"
"s_nop 5\n"
"ds_read_b128 a[196:199], v21 offset:0\n"
"s_waitcnt lgkmcnt(4)\n"
"v_mfma_f32_32x32x16_bf16 v[48:63], a[212:215], a[184:187], v[48:63]\n"
"s_nop 5\n"
"ds_read_b128 a[200:203], v22 offset:0\n"
"s_add_u32 m0, s60, 24576\n"
"s_add_u32 s87, s86, 262272\n"
"buffer_load_dwordx4 v3, s[40:43], s87 offen lds\n"
"s_waitcnt lgkmcnt(3)\n"
"v_mfma_f32_32x32x16_bf16 v[48:63], a[220:223], a[188:191], v[48:63]\n"
"s_nop 5\n"
"ds_read_b128 a[204:207], v23 offset:0\n"
"s_nop 7\n"
"v_cvt_pk_bf16_f32 v64, v32, v33\n"
"v_cvt_pk_bf16_f32 v65, v34, v35\n"
"v_cvt_pk_bf16_f32 v66, v36, v37\n"
"v_cvt_pk_bf16_f32 v67, v38, v39\n"
"s_nop 1\n"
"s_waitcnt lgkmcnt(3)\n"
"v_mfma_f32_32x32x16_bf16 v[80:95], a[192:195], v[64:67], 0\n"
"s_nop 4\n"
"s_waitcnt vmcnt(19)\n"
"s_barrier\n"
"ds_read_b128 a[208:211], v20 offset:4096\n"
"v_cvt_pk_bf16_f32 v68, v40, v41\n"
"v_cvt_pk_bf16_f32 v69, v42, v43\n"
"v_cvt_pk_bf16_f32 v70, v44, v45\n"
"v_cvt_pk_bf16_f32 v71, v46, v47\n"
"s_nop 1\n"
"s_add_u32 m0, s60, 159744\n"
"s_mov_b32 s87, 245760\n"
"buffer_load_dwordx4 v4, s[44:47], s87 offen nt lds\n"
"s_waitcnt lgkmcnt(3)\n"
"v_mfma_f32_32x32x16_bf16 v[80:95], a[196:199], v[68:71], v[80:95]\n"
"s_nop 4\n"
"ds_read_b128 a[212:215], v21 offset:4096\n"
"v_cvt_pk_bf16_f32 v72, v48, v49\n"
"v_cvt_pk_bf16_f32 v73, v50, v51\n"
"v_cvt_pk_bf16_f32 v74, v52, v53\n"
"v_cvt_pk_bf16_f32 v75, v54, v55\n"
"s_nop 1\n"
"s_waitcnt lgkmcnt(3)\n"
"v_mfma_f32_32x32x16_bf16 v[80:95], a[200:203], v[72:75], v[80:95]\n"
"s_nop 4\n"
"ds_read_b128 a[216:219], v22 offset:4096\n"
"v_cvt_pk_bf16_f32 v76, v56, v57\n"
"v_cvt_pk_bf16_f32 v77, v58, v59\n"
"v_cvt_pk_bf16_f32 v78, v60, v61\n"
"v_cvt_pk_bf16_f32 v79, v62, v63\n"
"s_nop 1\n"
"s_waitcnt lgkmcnt(3)\n"
"v_mfma_f32_32x32x16_bf16 v[80:95], a[204:207], v[76:79], v[80:95]\n"
"s_nop 4\n"
"ds_read_b128 a[220:223], v23 offset:4096\n"
"s_add_u32 m0, s60, 32768\n"
"s_mov_b32 s87, 128\n"
"buffer_load_dwordx4 v4, s[44:47], s87 offen nt lds\n"
"s_waitcnt lgkmcnt(3)\n"
"v_mfma_f32_32x32x16_bf16 v[96:111], a[208:211], v[64:67], 0\n"
"s_nop 4\n"
"ds_read_b128 a[192:195], v20 offset:8192\n"
"s_waitcnt lgkmcnt(3)\n"
"v_mfma_f32_32x32x16_bf16 v[96:111], a[212:215], v[68:71], v[96:111]\n"
"s_nop 4\n"
"ds_read_b128 a[196:199], v21 offset:8192\n"
"s_waitcnt lgkmcnt(3)\n"
"v_mfma_f32_32x32x16_bf16 v[96:111], a[216:219], v[72:75], v[96:111]\n"
"s_nop 4\n"
"ds_read_b128 a[200:203], v22 offset:8192\n"
"s_add_u32 m0, s60, 36864\n"
"s_mov_b32 s87, 16512\n"
"buffer_load_dwordx4 v4, s[44:47], s87 offen nt lds\n"
"s_waitcnt lgkmcnt(3)\n"
"v_mfma_f32_32x32x16_bf16 v[96:111], a[220:223], v[76:79], v[96:111]\n"
"s_nop 4\n"
"ds_read_b128 a[204:207], v23 offset:8192\n"
"s_waitcnt lgkmcnt(3)\n"
"v_mfma_f32_32x32x16_bf16 v[112:127], a[192:195], v[64:67], 0\n"
"s_nop 4\n"
"ds_read_b128 a[208:211], v20 offset:12288\n"
"s_waitcnt lgkmcnt(3)\n"
"v_mfma_f32_32x32x16_bf16 v[112:127], a[196:199], v[68:71], v[112:127]\n"
"s_nop 4\n"
"ds_read_b128 a[212:215], v21 offset:12288\n"
"s_add_u32 m0, s60, 40960\n"
"s_mov_b32 s87, 32896\n"
"buffer_load_dwordx4 v4, s[44:47], s87 offen nt lds\n"
"s_waitcnt lgkmcnt(3)\n"
"v_mfma_f32_32x32x16_bf16 v[112:127], a[200:203], v[72:75], v[112:127]\n"
"s_nop 4\n"
"ds_read_b128 a[216:219], v22 offset:12288\n"
"s_waitcnt lgkmcnt(3)\n"
"v_mfma_f32_32x32x16_bf16 v[112:127], a[204:207], v[76:79], v[112:127]\n"
"s_nop 4\n"
"ds_read_b128 a[220:223], v23 offset:12288\n"
"s_waitcnt lgkmcnt(3)\n"
"v_mfma_f32_32x32x16_bf16 a[0:15], a[208:211], v[64:67], 0\n"
"s_nop 4\n"
"ds_read_b128 a[192:195], v20 offset:16384\n"
"s_add_u32 m0, s60, 45056\n"
"s_mov_b32 s87, 49280\n"
"buffer_load_dwordx4 v4, s[44:47], s87 offen nt lds\n"
"s_waitcnt lgkmcnt(3)\n"
"v_mfma_f32_32x32x16_bf16 a[0:15], a[212:215], v[68:71], a[0:15]\n"
"s_nop 4\n"
"ds_read_b128 a[196:199], v21 offset:16384\n"
"s_waitcnt lgkmcnt(3)\n"
"v_mfma_f32_32x32x16_bf16 a[0:15], a[216:219], v[72:75], a[0:15]\n"
"s_nop 4\n"
"ds_read_b128 a[200:203], v22 offset:16384\n"
"s_waitcnt lgkmcnt(3)\n"
"v_mfma_f32_32x32x16_bf16 a[0:15], a[220:223], v[76:79], a[0:15]\n"
"s_nop 4\n"
"ds_read_b128 a[204:207], v23 offset:16384\n"
"s_add_u32 m0, s60, 49152\n"
"s_mov_b32 s87, 65664\n"
"buffer_load_dwordx4 v4, s[44:47], s87 offen nt lds\n"
"s_waitcnt lgkmcnt(3)\n"
"v_mfma_f32_32x32x16_bf16 a[16:31], a[192:195], v[64:67], 0\n"
"s_nop 4\n"
"ds_read_b128 a[208:211], v20 offset:20480\n"
"s_waitcnt lgkmcnt(3)\n"
"v_mfma_f32_32x32x16_bf16 a[16:31], a[196:199], v[68:71], a[16:31]\n"
"s_nop 4\n"
"ds_read_b128 a[212:215], v21 offset:20480\n"
"s_waitcnt lgkmcnt(3)\n"
"v_mfma_f32_32x32x16_bf16 a[16:31], a[200:203], v[72:75], a[16:31]\n"
"s_nop 4\n"
"ds_read_b128 a[216:219], v22 offset:20480\n"
"s_waitcnt lgkmcnt(3)\n"
"v_mfma_f32_32x32x16_bf16 a[16:31], a[204:207], v[76:79], a[16:31]\n"
"s_nop 4\n"
"ds_read_b128 a[220:223], v23 offset:20480\n"
"s_add_u32 m0, s60, 53248\n"
"s_mov_b32 s87, 82048\n"
"buffer_load_dwordx4 v4, s[44:47], s87 offen nt lds\n"
"s_waitcnt lgkmcnt(3)\n"
"v_mfma_f32_32x32x16_bf16 a[32:47], a[208:211], v[64:67], 0\n"
"s_nop 4\n"
"ds_read_b128 a[192:195], v20 offset:24576\n"
"s_waitcnt lgkmcnt(3)\n"
"v_mfma_f32_32x32x16_bf16 a[32:47], a[212:215], v[68:71], a[32:47]\n"
"s_nop 4\n"
"ds_read_b128 a[196:199], v21 offset:24576\n"
"s_waitcnt lgkmcnt(3)\n"
"v_mfma_f32_32x32x16_bf16 a[32:47], a[216:219], v[72:75], a[32:47]\n"
"s_nop 4\n"
"ds_read_b128 a[200:203], v22 offset:24576\n"
"s_waitcnt lgkmcnt(3)\n"
"v_mfma_f32_32x32x16_bf16 a[32:47], a[220:223], v[76:79], a[32:47]\n"
"s_nop 4\n"
"ds_read_b128 a[204:207], v23 offset:24576\n"
"s_waitcnt lgkmcnt(3)\n"
"v_mfma_f32_32x32x16_bf16 a[48:63], a[192:195], v[64:67], 0\n"
"s_nop 4\n"
"ds_read_b128 a[208:211], v20 offset:28672\n"
"s_add_u32 m0, s60, 57344\n"
"s_mov_b32 s87, 98432\n"
"buffer_load_dwordx4 v4, s[44:47], s87 offen nt lds\n"
"s_waitcnt lgkmcnt(3)\n"
"v_mfma_f32_32x32x16_bf16 a[48:63], a[196:199], v[68:71], a[48:63]\n"
"s_nop 4\n"
"ds_read_b128 a[212:215], v21 offset:28672\n"
"s_waitcnt lgkmcnt(3)\n"
"v_mfma_f32_32x32x16_bf16 a[48:63], a[200:203], v[72:75], a[48:63]\n"
"s_nop 4\n"
"ds_read_b128 a[216:219], v22 offset:28672\n"
"s_waitcnt lgkmcnt(3)\n"
"v_mfma_f32_32x32x16_bf16 a[48:63], a[204:207], v[76:79], a[48:63]\n"
"s_nop 4\n"
"ds_read_b128 a[220:223], v23 offset:28672\n"
"v_add_u32 v20, 98304, v7\n"
"v_xor_b32 v21, 64, v20\n"
"s_waitcnt lgkmcnt(3)\n"
"v_mfma_f32_32x32x16_bf16 a[64:79], a[208:211], v[64:67], 0\n"
"s_nop 4\n"
"ds_read_b64_tr_b16 a[192:193], v20 offset:0\n"
"ds_read_b64_tr_b16 a[194:195], v20 offset:512\n"
"s_waitcnt lgkmcnt(4)\n"
"v_mfma_f32_32x32x16_bf16 a[64:79], a[212:215], v[68:71], a[64:79]\n"
"s_nop 4\n"
"ds_read_b64_tr_b16 a[196:197], v21 offset:0\n"
"ds_read_b64_tr_b16 a[198:199], v21 offset:512\n"
"s_waitcnt lgkmcnt(5)\n"
"v_mfma_f32_32x32x16_bf16 a[64:79], a[216:219], v[72:75], a[64:79]\n"
"s_nop 4\n"
"ds_read_b64_tr_b16 a[200:201], v20 offset:2048\n"
"ds_read_b64_tr_b16 a[202:203], v20 offset:2560\n"
"s_add_u32 m0, s60, 61440\n"
"s_mov_b32 s87, 114816\n"
"buffer_load_dwordx4 v4, s[44:47], s87 offen nt lds\n"
"s_waitcnt lgkmcnt(6)\n"
"v_mfma_f32_32x32x16_bf16 a[64:79], a[220:223], v[76:79], a[64:79]\n"
"s_nop 4\n"
"ds_read_b64_tr_b16 a[204:205], v21 offset:2048\n"
"ds_read_b64_tr_b16 a[206:207], v21 offset:2560\n"
"s_waitcnt lgkmcnt(6)\n"
"v_mfma_f32_32x32x16_bf16 v[32:47], a[192:195], a[80:83], 0\n"
"s_nop 3\n"
"s_waitcnt vmcnt(22)\n"
"s_barrier\n"
"ds_read_b64_tr_b16 a[208:209], v20 offset:4096\n"
"ds_read_b64_tr_b16 a[210:211], v20 offset:4608\n"
"s_add_u32 m0, s60, 65536\n"
"s_add_u32 s87, s80, 256\n"
"buffer_load_dwordx4 v3, s[40:43], s87 offen lds\n"
"s_waitcnt lgkmcnt(6)\n"
"v_mfma_f32_32x32x16_bf16 v[48:63], a[196:199], a[80:83], 0\n"
"s_nop 3\n"
"ds_read_b64_tr_b16 a[212:213], v21 offset:4096\n"
"ds_read_b64_tr_b16 a[214:215], v21 offset:4608\n"
"s_waitcnt lgkmcnt(6)\n"
"v_mfma_f32_32x32x16_bf16 v[32:47], a[200:203], a[84:87], v[32:47]\n"
"s_nop 3\n"
"ds_read_b64_tr_b16 a[216:217], v20 offset:6144\n"
"ds_read_b64_tr_b16 a[218:219], v20 offset:6656\n"
"s_add_u32 m0, s60, 69632\n"
"s_add_u32 s87, s80, 262400\n"
"buffer_load_dwordx4 v3, s[40:43], s87 offen lds\n"
"s_waitcnt lgkmcnt(6)\n"
"v_mfma_f32_32x32x16_bf16 v[48:63], a[204:207], a[84:87], v[48:63]\n"
"s_nop 3\n"
"ds_read_b64_tr_b16 a[220:221], v21 offset:6144\n"
"ds_read_b64_tr_b16 a[222:223], v21 offset:6656\n"
"s_waitcnt lgkmcnt(6)\n"
"v_mfma_f32_32x32x16_bf16 v[32:47], a[208:211], a[88:91], v[32:47]\n"
"s_nop 3\n"
"ds_read_b64_tr_b16 a[192:193], v20 offset:8192\n"
"ds_read_b64_tr_b16 a[194:195], v20 offset:8704\n"
"s_add_u32 m0, s60, 73728\n"
"s_add_u32 s87, s81, 256\n"
"buffer_load_dwordx4 v3, s[40:43], s87 offen lds\n"
"s_waitcnt lgkmcnt(6)\n"
"v_mfma_f32_32x32x16_bf16 v[48:63], a[212:215], a[88:91], v[48:63]\n"
"s_nop 3\n"
"ds_read_b64_tr_b16 a[196:197], v21 offset:8192\n"
"ds_read_b64_tr_b16 a[198:199], v21 offset:8704\n"
"s_waitcnt lgkmcnt(6)\n"
"v_mfma_f32_32x32x16_bf16 v[32:47], a[216:219], a[92:95], v[32:47]\n"
"s_nop 3\n"
"ds_read_b64_tr_b16 a[200:201], v20 offset:10240\n"
"ds_read_b64_tr_b16 a[202:203], v20 offset:10752\n"
"s_add_u32 m0, s60, 77824\n"
"s_add_u32 s87, s81, 262400\n"
"buffer_load_dwordx4 v3, s[40:43], s87 offen lds\n"
"s_waitcnt lgkmcnt(6)\n"
"v_mfma_f32_32x32x16_bf16 v[48:63], a[220:223], a[92:95], v[48:63]\n"
"s_nop 3\n"
"ds_read_b64_tr_b16 a[204:205], v21 offset:10240\n"
"ds_read_b64_tr_b16 a[206:207], v21 offset:10752\n"
"s_waitcnt lgkmcnt(6)\n"
"v_mfma_f32_32x32x16_bf16 v[32:47], a[192:195], a[96:99], v[32:47]\n"
"s_nop 3\n"
"ds_read_b64_tr_b16 a[208:209], v20 offset:12288\n"
"ds_read_b64_tr_b16 a[210:211], v20 offset:12800\n"
"s_waitcnt lgkmcnt(6)\n"
"v_mfma_f32_32x32x16_bf16 v[48:63], a[196:199], a[96:99], v[48:63]\n"
"s_nop 3\n"
"ds_read_b64_tr_b16 a[212:213], v21 offset:12288\n"
"ds_read_b64_tr_b16 a[214:215], v21 offset:12800\n"
"s_waitcnt lgkmcnt(6)\n"
"v_mfma_f32_32x32x16_bf16 v[32:47], a[200:203], a[100:103], v[32:47]\n"
"s_nop 3\n"
"ds_read_b64_tr_b16 a[216:217], v20 offset:14336\n"
"ds_read_b64_tr_b16 a[218:219], v20 offset:14848\n"
"s_add_u32 m0, s60, 81920\n"
"s_add_u32 s87, s82, 256\n"
"buffer_load_dwordx4 v3, s[40:43], s87 offen lds\n"
"s_waitcnt lgkmcnt(6)\n"
"v_mfma_f32_32x32x16_bf16 v[48:63], a[204:207], a[100:103], v[48:63]\n"
"s_nop 3\n"
"ds_read_b64_tr_b16 a[220:221], v21 offset:14336\n"
"ds_read_b64_tr_b16 a[222:223], v21 offset:14848\n"
"s_waitcnt lgkmcnt(6)\n"
"v_mfma_f32_32x32x16_bf16 v[32:47], a[208:211], a[104:107], v[32:47]\n"
"s_nop 3\n"
"ds_read_b64_tr_b16 a[192:193], v20 offset:16384\n"
"ds_read_b64_tr_b16 a[194:195], v20 offset:16896\n"
"s_waitcnt lgkmcnt(6)\n"
"v_mfma_f32_32x32x16_bf16 v[48:63], a[212:215], a[104:107], v[48:63]\n"
"s_nop 3\n"
"ds_read_b64_tr_b16 a[196:197], v21 offset:16384\n"
"ds_read_b64_tr_b16 a[198:199], v21 offset:16896\n"
"s_waitcnt lgkmcnt(6)\n"
"v_mfma_f32_32x32x16_bf16 v[32:47], a[216:219], a[108:111], v[32:47]\n"
"s_nop 3\n"
"ds_read_b64_tr_b16 a[200:201], v20 offset:18432\n"
"ds_read_b64_tr_b16 a[202:203], v20 offset:18944\n"
"s_waitcnt lgkmcnt(6)\n"
"v_mfma_f32_32x32x16_bf16 v[48:63], a[220:223], a[108:111], v[48:63]\n"
"s_nop 3\n"
"ds_read_b64_tr_b16 a[204:205], v21 offset:18432\n"
"ds_read_b64_tr_b16 a[206:207], v21 offset:18944\n"
"s_waitcnt lgkmcnt(6)\n"
"v_mfma_f32_32x32x16_bf16 v[32:47], a[192:195], a[112:115], v[32:47]\n"
"s_nop 3\n"
"ds_read_b64_tr_b16 a[208:209], v20 offset:20480\n"
"ds_read_b64_tr_b16 a[210:211], v20 offset:20992\n"
"s_add_u32 m0, s60, 86016\n"
"s_add_u32 s87, s82, 262400\n"
"buffer_load_dwordx4 v3, s[40:43], s87 offen lds\n"
"s_waitcnt lgkmcnt(6)\n"
"v_mfma_f32_32x32x16_bf16 v[48:63], a[196:199], a[112:115], v[48:63]\n"
"s_nop 3\n"
"ds_read_b64_tr_b16 a[212:213], v21 offset:20480\n"
"ds_read_b64_tr_b16 a[214:215], v21 offset:20992\n"
"s_waitcnt lgkmcnt(6)\n"
"v_mfma_f32_32x32x16_bf16 v[32:47], a[200:203], a[116:119], v[32:47]\n"
"s_nop 3\n"
"ds_read_b64_tr_b16 a[216:217], v20 offset:22528\n"
"ds_read_b64_tr_b16 a[218:219], v20 offset:23040\n"
"s_add_u32 m0, s60, 90112\n"
"s_add_u32 s87, s83, 256\n"
"buffer_load_dwordx4 v3, s[40:43], s87 offen lds\n"
"s_waitcnt lgkmcnt(6)\n"
"v_mfma_f32_32x32x16_bf16 v[48:63], a[204:207], a[116:119], v[48:63]\n"
"s_nop 3\n"
"ds_read_b64_tr_b16 a[220:221], v21 offset:22528\n"
"ds_read_b64_tr_b16 a[222:223], v21 offset:23040\n"
"s_waitcnt lgkmcnt(6)\n"
"v_mfma_f32_32x32x16_bf16 v[32:47], a[208:211], a[120:123], v[32:47]\n"
"s_nop 3\n"
"ds_read_b64_tr_b16 a[192:193], v20 offset:24576\n"
"ds_read_b64_tr_b16 a[194:195], v20 offset:25088\n"
"s_waitcnt lgkmcnt(6)\n"
"v_mfma_f32_32x32x16_bf16 v[48:63], a[212:215], a[120:123], v[48:63]\n"
"s_nop 3\n"
"ds_read_b64_tr_b16 a[196:197], v21 offset:24576\n"
"ds_read_b64_tr_b16 a[198:199], v21 offset:25088\n"
"s_waitcnt lgkmcnt(6)\n"
"v_mfma_f32_32x32x16_bf16 v[32:47], a[216:219], a[124:127], v[32:47]\n"
"s_nop 3\n"
"ds_read_b64_tr_b16 a[200:201], v20 offset:26624\n"
"ds_read_b64_tr_b16 a[202:203], v20 offset:27136\n"
"s_waitcnt lgkmcnt(6)\n"
"v_mfma_f32_32x32x16_bf16 v[48:63], a[220:223], a[124:127], v[48:63]\n"
"s_nop 3\n"
"ds_read_b64_tr_b16 a[204:205], v21 offset:26624\n"
"ds_read_b64_tr_b16 a[206:207], v21 offset:27136\n"
"v_add_u32 v20, 131072, v8\n"
"v_xor_b32 v21, 32, v20\n"
"v_xor_b32 v22, 64, v20\n"
"v_xor_b32 v23, 0x60, v20\n"
"s_waitcnt lgkmcnt(6)\n"
"v_mfma_f32_32x32x16_bf16 v[32:47], a[192:195], a[128:131], v[32:47]\n"
"s_nop 3\n"
"ds_read_b128 a[208:211], v20 offset:0\n"
"s_waitcnt lgkmcnt(5)\n"
"v_mfma_f32_32x32x16_bf16 v[48:63], a[196:199], a[128:131], v[48:63]\n"
"s_nop 3\n"
"ds_read_b128 a[212:215], v21 offset:0\n"
"s_waitcnt lgkmcnt(4)\n"
"v_mfma_f32_32x32x16_bf16 v[32:47], a[200:203], a[132:135], v[32:47]\n"
"s_nop 3\n"
"ds_read_b128 a[216:219], v22 offset:0\n"
"s_waitcnt lgkmcnt(3)\n"
"v_mfma_f32_32x32x16_bf16 v[48:63], a[204:207], a[132:135], v[48:63]\n"
"s_nop 3\n"
"ds_read_b128 a[220:223], v23 offset:0\n"
"s_waitcnt lgkmcnt(3)\n"
"v_mfma_f32_32x32x16_bf16 v[128:143], a[208:211], v[64:67], 0\n"
"s_nop 1\n"
"s_waitcnt vmcnt(15)\n"
"s_barrier\n"
"ds_read_b128 a[192:195], v20 offset:4096\n"
"s_add_u32 m0, s60, 98304\n"
"s_mov_b32 s87, 131200\n"
"buffer_load_dwordx4 v4, s[44:47], s87 offen nt lds\n"
"s_waitcnt lgkmcnt(3)\n"
"v_mfma_f32_32x32x16_bf16 v[128:143], a[212:215], v[68:71], v[128:143]\n"
"s_nop 1\n"
"ds_read_b128 a[196:199], v21 offset:4096\n"
"s_waitcnt lgkmcnt(3)\n"
"v_mfma_f32_32x32x16_bf16 v[128:143], a[216:219], v[72:75], v[128:143]\n"
"s_nop 1\n"
"ds_read_b128 a[200:203], v22 offset:4096\n"
"s_waitcnt lgkmcnt(3)\n"
"v_mfma_f32_32x32x16_bf16 v[128:143], a[220:223], v[76:79], v[128:143]\n"
"s_nop 1\n"
"ds_read_b128 a[204:207], v23 offset:4096\n"
"s_add_u32 m0, s60, 102400\n"
"s_mov_b32 s87, 147584\n"
"buffer_load_dwordx4 v4, s[44:47], s87 offen nt lds\n"
"s_waitcnt lgkmcnt(3)\n"
"v_mfma_f32_32x32x16_bf16 v[144:159], a[192:195], v[64:67], 0\n"
"s_nop 1\n"
"ds_read_b128 a[208:211], v20 offset:8192\n"
"s_waitcnt lgkmcnt(3)\n"
"v_mfma_f32_32x32x16_bf16 v[144:159], a[196:199], v[68:71], v[144:159]\n"
"s_nop 1\n"
"ds_read_b128 a[212:215], v21 offset:8192\n"
"s_waitcnt lgkmcnt(3)\n"
"v_mfma_f32_32x32x16_bf16 v[144:159], a[200:203], v[72:75], v[144:159]\n"
"s_nop 1\n"
"ds_read_b128 a[216:219], v22 offset:8192\n"
"s_add_u32 m0, s60, 106496\n"
"s_mov_b32 s87, 163968\n"
"buffer_load_dwordx4 v4, s[44:47], s87 offen nt lds\n"
"s_waitcnt lgkmcnt(3)\n"
"v_mfma_f32_32x32x16_bf16 v[144:159], a[204:207], v[76:79], v[144:159]\n"
"s_nop 1\n"
"ds_read_b128 a[220:223], v23 offset:8192\n"
"s_waitcnt lgkmcnt(3)\n"
"v_mfma_f32_32x32x16_bf16 v[160:175], a[208:211], v[64:67], 0\n"
"s_nop 1\n"
"ds_read_b128 a[192:195], v20 offset:12288\n"
"s_waitcnt lgkmcnt(3)\n"
"v_mfma_f32_32x32x16_bf16 v[160:175], a[212:215], v[68:71], v[160:175]\n"
"s_nop 1\n"
"ds_read_b128 a[196:199], v21 offset:12288\n"
"s_add_u32 m0, s60, 110592\n"
"s_mov_b32 s87, 180352\n"
"buffer_load_dwordx4 v4, s[44:47], s87 offen nt lds\n"
"s_waitcnt lgkmcnt(3)\n"
"v_mfma_f32_32x32x16_bf16 v[160:175], a[216:219], v[72:75], v[160:175]\n"
"s_nop 1\n"
"ds_read_b128 a[200:203], v22 offset:12288\n"
"s_waitcnt lgkmcnt(3)\n"
"v_mfma_f32_32x32x16_bf16 v[160:175], a[220:223], v[76:79], v[160:175]\n"
"s_nop 1\n"
"ds_read_b128 a[204:207], v23 offset:12288\n"
"s_waitcnt lgkmcnt(3)\n"
"v_mfma_f32_32x32x16_bf16 v[176:191], a[192:195], v[64:67], 0\n"
"s_nop 1\n"
"ds_read_b128 a[208:211], v20 offset:16384\n"
"s_add_u32 m0, s60, 114688\n"
"s_mov_b32 s87, 196736\n"
"buffer_load_dwordx4 v4, s[44:47], s87 offen nt lds\n"
"s_waitcnt lgkmcnt(3)\n"
"v_mfma_f32_32x32x16_bf16 v[176:191], a[196:199], v[68:71], v[176:191]\n"
"s_nop 1\n"
"ds_read_b128 a[212:215], v21 offset:16384\n"
"s_waitcnt lgkmcnt(3)\n"
"v_mfma_f32_32x32x16_bf16 v[176:191], a[200:203], v[72:75], v[176:191]\n"
"s_nop 1\n"
"ds_read_b128 a[216:219], v22 offset:16384\n"
"s_waitcnt lgkmcnt(3)\n"
"v_mfma_f32_32x32x16_bf16 v[176:191], a[204:207], v[76:79], v[176:191]\n"
"s_nop 1\n"
"ds_read_b128 a[220:223], v23 offset:16384\n"
"s_add_u32 m0, s60, 118784\n"
"s_mov_b32 s87, 213120\n"
"buffer_load_dwordx4 v4, s[44:47], s87 offen nt lds\n"
"s_waitcnt lgkmcnt(3)\n"
"v_mfma_f32_32x32x16_bf16 v[192:207], a[208:211], v[64:67], 0\n"
"s_nop 1\n"
"ds_read_b128 a[192:195], v20 offset:20480\n"
"s_waitcnt lgkmcnt(3)\n"
"v_mfma_f32_32x32x16_bf16 v[192:207], a[212:215], v[68:71], v[192:207]\n"
"s_nop 1\n"
"ds_read_b128 a[196:199], v21 offset:20480\n"
"s_waitcnt lgkmcnt(3)\n"
"v_mfma_f32_32x32x16_bf16 v[192:207], a[216:219], v[72:75], v[192:207]\n"
"s_nop 1\n"
"ds_read_b128 a[200:203], v22 offset:20480\n"
"s_add_u32 m0, s60, 122880\n"
"s_mov_b32 s87, 229504\n"
"buffer_load_dwordx4 v4, s[44:47], s87 offen nt lds\n"
"s_waitcnt lgkmcnt(3)\n"
"v_mfma_f32_32x32x16_bf16 v[192:207], a[220:223], v[76:79], v[192:207]\n"
"s_nop 1\n"
"ds_read_b128 a[204:207], v23 offset:20480\n"
"s_waitcnt lgkmcnt(3)\n"
"v_mfma_f32_32x32x16_bf16 v[208:223], a[192:195], v[64:67], 0\n"
"s_nop 1\n"
"ds_read_b128 a[208:211], v20 offset:24576\n"
"s_waitcnt lgkmcnt(3)\n"
"v_mfma_f32_32x32x16_bf16 v[208:223], a[196:199], v[68:71], v[208:223]\n"
"s_nop 1\n"
"ds_read_b128 a[212:215], v21 offset:24576\n"
"s_add_u32 m0, s60, 126976\n"
"s_mov_b32 s87, 245888\n"
"buffer_load_dwordx4 v4, s[44:47], s87 offen nt lds\n"
"s_waitcnt lgkmcnt(3)\n"
"v_mfma_f32_32x32x16_bf16 v[208:223], a[200:203], v[72:75], v[208:223]\n"
"s_nop 1\n"
"ds_read_b128 a[216:219], v22 offset:24576\n"
"s_waitcnt lgkmcnt(3)\n"
"v_mfma_f32_32x32x16_bf16 v[208:223], a[204:207], v[76:79], v[208:223]\n"
"s_nop 1\n"
"ds_read_b128 a[220:223], v23 offset:24576\n"
"s_waitcnt lgkmcnt(3)\n"
"v_mfma_f32_32x32x16_bf16 v[224:239], a[208:211], v[64:67], 0\n"
"s_nop 1\n"
"ds_read_b128 a[192:195], v20 offset:28672\n"
"s_waitcnt lgkmcnt(3)\n"
"v_mfma_f32_32x32x16_bf16 v[224:239], a[212:215], v[68:71], v[224:239]\n"
"s_nop 1\n"
"ds_read_b128 a[196:199], v21 offset:28672\n"
"s_waitcnt lgkmcnt(3)\n"
"v_mfma_f32_32x32x16_bf16 v[224:239], a[216:219], v[72:75], v[224:239]\n"
"s_nop 1\n"
"ds_read_b128 a[200:203], v22 offset:28672\n"
"s_waitcnt lgkmcnt(3)\n"
"v_mfma_f32_32x32x16_bf16 v[224:239], a[220:223], v[76:79], v[224:239]\n"
"s_nop 1\n"
"ds_read_b128 a[204:207], v23 offset:28672\n"
"v_mov_b32 v20, v7\n"
"v_xor_b32 v21, 64, v20\n"
"s_waitcnt lgkmcnt(3)\n"
"v_mfma_f32_32x32x16_bf16 v[240:255], a[192:195], v[64:67], 0\n"
"s_nop 1\n"
"ds_read_b64_tr_b16 a[208:209], v20 offset:0\n"
"ds_read_b64_tr_b16 a[210:211], v20 offset:512\n"
"s_waitcnt lgkmcnt(4)\n"
"v_mfma_f32_32x32x16_bf16 v[240:255], a[196:199], v[68:71], v[240:255]\n"
"s_nop 1\n"
"ds_read_b64_tr_b16 a[212:213], v21 offset:0\n"
"ds_read_b64_tr_b16 a[214:215], v21 offset:512\n"
"s_waitcnt lgkmcnt(5)\n"
"v_mfma_f32_32x32x16_bf16 v[240:255], a[200:203], v[72:75], v[240:255]\n"
"s_nop 1\n"
"ds_read_b64_tr_b16 a[216:217], v20 offset:2048\n"
"ds_read_b64_tr_b16 a[218:219], v20 offset:2560\n"
"s_waitcnt lgkmcnt(6)\n"
"v_mfma_f32_32x32x16_bf16 v[240:255], a[204:207], v[76:79], v[240:255]\n"
"s_nop 1\n"
"ds_read_b64_tr_b16 a[220:221], v21 offset:2048\n"
"ds_read_b64_tr_b16 a[222:223], v21 offset:2560\n"
"s_waitcnt lgkmcnt(6)\n"
"v_mfma_f32_32x32x16_bf16 v[32:47], a[208:211], a[136:139], v[32:47]\n"
"s_waitcnt vmcnt(22)\n"
"s_barrier\n"
"ds_read_b64_tr_b16 a[192:193], v20 offset:4096\n"
"ds_read_b64_tr_b16 a[194:195], v20 offset:4608\n"
"s_add_u32 m0, s60, 131072\n"
"s_add_u32 s87, s83, 262400\n"
"buffer_load_dwordx4 v3, s[40:43], s87 offen lds\n"
"s_waitcnt lgkmcnt(6)\n"
"v_mfma_f32_32x32x16_bf16 v[48:63], a[212:215], a[136:139], v[48:63]\n"
"ds_read_b64_tr_b16 a[196:197], v21 offset:4096\n"
"ds_read_b64_tr_b16 a[198:199], v21 offset:4608\n"
"s_waitcnt lgkmcnt(6)\n"
"v_mfma_f32_32x32x16_bf16 v[32:47], a[216:219], a[140:143], v[32:47]\n"
"ds_read_b64_tr_b16 a[200:201], v20 offset:6144\n"
"ds_read_b64_tr_b16 a[202:203], v20 offset:6656\n"
"s_add_u32 m0, s60, 135168\n"
"s_add_u32 s87, s84, 256\n"
"buffer_load_dwordx4 v3, s[40:43], s87 offen lds\n"
"s_waitcnt lgkmcnt(6)\n"
"v_mfma_f32_32x32x16_bf16 v[48:63], a[220:223], a[140:143], v[48:63]\n"
"ds_read_b64_tr_b16 a[204:205], v21 offset:6144\n"
"ds_read_b64_tr_b16 a[206:207], v21 offset:6656\n"
"s_waitcnt lgkmcnt(6)\n"
"v_mfma_f32_32x32x16_bf16 v[32:47], a[192:195], a[144:147], v[32:47]\n"
"ds_read_b64_tr_b16 a[208:209], v20 offset:8192\n"
"ds_read_b64_tr_b16 a[210:211], v20 offset:8704\n"
"s_add_u32 m0, s60, 139264\n"
"s_add_u32 s87, s84, 262400\n"
"buffer_load_dwordx4 v3, s[40:43], s87 offen lds\n"
"s_waitcnt lgkmcnt(6)\n"
"v_mfma_f32_32x32x16_bf16 v[48:63], a[196:199], a[144:147], v[48:63]\n"
"ds_read_b64_tr_b16 a[212:213], v21 offset:8192\n"
"ds_read_b64_tr_b16 a[214:215], v21 offset:8704\n"
"s_waitcnt lgkmcnt(6)\n"
"v_mfma_f32_32x32x16_bf16 v[32:47], a[200:203], a[148:151], v[32:47]\n"
"ds_read_b64_tr_b16 a[216:217], v20 offset:10240\n"
"ds_read_b64_tr_b16 a[218:219], v20 offset:10752\n"
"s_add_u32 m0, s60, 143360\n"
"s_add_u32 s87, s85, 256\n"
"buffer_load_dwordx4 v3, s[40:43], s87 offen lds\n"
"s_waitcnt lgkmcnt(6)\n"
"v_mfma_f32_32x32x16_bf16 v[48:63], a[204:207], a[148:151], v[48:63]\n"
"ds_read_b64_tr_b16 a[220:221], v21 offset:10240\n"
"ds_read_b64_tr_b16 a[222:223], v21 offset:10752\n"
"s_waitcnt lgkmcnt(6)\n"
"v_mfma_f32_32x32x16_bf16 v[32:47], a[208:211], a[152:155], v[32:47]\n"
"ds_read_b64_tr_b16 a[192:193], v20 offset:12288\n"
"ds_read_b64_tr_b16 a[194:195], v20 offset:12800\n"
"s_add_u32 m0, s60, 147456\n"
"s_add_u32 s87, s85, 262400\n"
"buffer_load_dwordx4 v3, s[40:43], s87 offen lds\n"
"s_waitcnt lgkmcnt(6)\n"
"v_mfma_f32_32x32x16_bf16 v[48:63], a[212:215], a[152:155], v[48:63]\n"
"ds_read_b64_tr_b16 a[196:197], v21 offset:12288\n"
"ds_read_b64_tr_b16 a[198:199], v21 offset:12800\n"
"s_waitcnt lgkmcnt(6)\n"
"v_mfma_f32_32x32x16_bf16 v[32:47], a[216:219], a[156:159], v[32:47]\n"
"ds_read_b64_tr_b16 a[200:201], v20 offset:14336\n"
"ds_read_b64_tr_b16 a[202:203], v20 offset:14848\n"
"s_add_u32 m0, s60, 151552\n"
"s_add_u32 s87, s86, 256\n"
"buffer_load_dwordx4 v3, s[40:43], s87 offen lds\n"
"s_waitcnt lgkmcnt(6)\n"
"v_mfma_f32_32x32x16_bf16 v[48:63], a[220:223], a[156:159], v[48:63]\n"
"ds_read_b64_tr_b16 a[204:205], v21 offset:14336\n"
"ds_read_b64_tr_b16 a[206:207], v21 offset:14848\n"
"s_waitcnt lgkmcnt(6)\n"
"v_mfma_f32_32x32x16_bf16 v[32:47], a[192:195], a[160:163], v[32:47]\n"
"ds_read_b64_tr_b16 a[208:209], v20 offset:16384\n"
"ds_read_b64_tr_b16 a[210:211], v20 offset:16896\n"
"s_waitcnt lgkmcnt(6)\n"
"v_mfma_f32_32x32x16_bf16 v[48:63], a[196:199], a[160:163], v[48:63]\n"
"ds_read_b64_tr_b16 a[212:213], v21 offset:16384\n"
"ds_read_b64_tr_b16 a[214:215], v21 offset:16896\n"
"s_add_u32 m0, s60, 155648\n"
"s_add_u32 s87, s86, 262400\n"
"buffer_load_dwordx4 v3, s[40:43], s87 offen lds\n"
"s_waitcnt lgkmcnt(6)\n"
"v_mfma_f32_32x32x16_bf16 v[32:47], a[200:203], a[164:167], v[32:47]\n"
"ds_read_b64_tr_b16 a[216:217], v20 offset:18432\n"
"ds_read_b64_tr_b16 a[218:219], v20 offset:18944\n"
"s_waitcnt lgkmcnt(6)\n"
"v_mfma_f32_32x32x16_bf16 v[48:63], a[204:207], a[164:167], v[48:63]\n"
"ds_read_b64_tr_b16 a[220:221], v21 offset:18432\n"
"ds_read_b64_tr_b16 a[222:223], v21 offset:18944\n"
"s_waitcnt lgkmcnt(6)\n"
"v_mfma_f32_32x32x16_bf16 v[32:47], a[208:211], a[168:171], v[32:47]\n"
"ds_read_b64_tr_b16 a[192:193], v20 offset:20480\n"
"ds_read_b64_tr_b16 a[194:195], v20 offset:20992\n"
"s_waitcnt lgkmcnt(6)\n"
"v_mfma_f32_32x32x16_bf16 v[48:63], a[212:215], a[168:171], v[48:63]\n"
"ds_read_b64_tr_b16 a[196:197], v21 offset:20480\n"
"ds_read_b64_tr_b16 a[198:199], v21 offset:20992\n"
"s_waitcnt lgkmcnt(6)\n"
"v_mfma_f32_32x32x16_bf16 v[32:47], a[216:219], a[172:175], v[32:47]\n"
"ds_read_b64_tr_b16 a[200:201], v20 offset:22528\n"
"ds_read_b64_tr_b16 a[202:203], v20 offset:23040\n"
"s_waitcnt lgkmcnt(6)\n"
"v_mfma_f32_32x32x16_bf16 v[48:63], a[220:223], a[172:175], v[48:63]\n"
"ds_read_b64_tr_b16 a[204:205], v21 offset:22528\n"
"ds_read_b64_tr_b16 a[206:207], v21 offset:23040\n"
"s_waitcnt lgkmcnt(6)\n"
"v_mfma_f32_32x32x16_bf16 v[32:47], a[192:195], a[176:179], v[32:47]\n"
"ds_read_b64_tr_b16 a[208:209], v20 offset:24576\n"
"ds_read_b64_tr_b16 a[210:211], v20 offset:25088\n"
"s_waitcnt lgkmcnt(6)\n"
"v_mfma_f32_32x32x16_bf16 v[48:63], a[196:199], a[176:179], v[48:63]\n"
"ds_read_b64_tr_b16 a[216:217], v20 offset:26624\n"
"ds_read_b64_tr_b16 a[218:219], v20 offset:27136\n"
"s_waitcnt lgkmcnt(6)\n"
"v_mfma_f32_32x32x16_bf16 v[32:47], a[200:203], a[180:183], v[32:47]\n"
"ds_read_b64_tr_b16 a[212:213], v21 offset:24576\n"
"ds_read_b64_tr_b16 a[214:215], v21 offset:25088\n"
"s_waitcnt lgkmcnt(6)\n"
"v_mfma_f32_32x32x16_bf16 v[48:63], a[204:207], a[180:183], v[48:63]\n"
"ds_read_b64_tr_b16 a[220:221], v21 offset:26624\n"
"ds_read_b64_tr_b16 a[222:223], v21 offset:27136\n"
"v_add_u32 v20, 32768, v8\n"
"v_xor_b32 v21, 32, v20\n"
"v_xor_b32 v22, 64, v20\n"
"v_xor_b32 v23, 0x60, v20\n"
"s_waitcnt lgkmcnt(6)\n"
"v_mfma_f32_32x32x16_bf16 v[32:47], a[208:211], a[184:187], v[32:47]\n"
"ds_read_b128 a[192:195], v20 offset:0\n"
"s_waitcnt lgkmcnt(5)\n"
"v_mfma_f32_32x32x16_bf16 v[32:47], a[216:219], a[188:191], v[32:47]\n"
"ds_read_b128 a[196:199], v21 offset:0\n"
"s_waitcnt lgkmcnt(4)\n"
"v_mfma_f32_32x32x16_bf16 v[48:63], a[212:215], a[184:187], v[48:63]\n"
"ds_read_b128 a[200:203], v22 offset:0\n"
"s_waitcnt lgkmcnt(3)\n"
"v_mfma_f32_32x32x16_bf16 v[48:63], a[220:223], a[188:191], v[48:63]\n"
"ds_read_b128 a[204:207], v23 offset:0\n"
"s_nop 7\n"
"v_cvt_pk_bf16_f32 v64, v32, v33\n"
"v_cvt_pk_bf16_f32 v65, v34, v35\n"
"v_cvt_pk_bf16_f32 v66, v36, v37\n"
"v_cvt_pk_bf16_f32 v67, v38, v39\n"
"s_nop 1\n"
"s_waitcnt lgkmcnt(3)\n"
"v_mfma_f32_32x32x16_bf16 v[80:95], a[192:195], v[64:67], v[80:95]\n"
"s_waitcnt vmcnt(21)\n"
"s_barrier\n"
"ds_read_b128 a[208:211], v20 offset:4096\n"
"v_cvt_pk_bf16_f32 v68, v40, v41\n"
"v_cvt_pk_bf16_f32 v69, v42, v43\n"
"v_cvt_pk_bf16_f32 v70, v44, v45\n"
"v_cvt_pk_bf16_f32 v71, v46, v47\n"
"s_nop 1\n"
"s_add_u32 m0, s60, 0\n"
"s_mov_b32 s87, 256\n"
"buffer_load_dwordx4 v4, s[44:47], s87 offen nt lds\n"
"s_waitcnt lgkmcnt(3)\n"
"v_mfma_f32_32x32x16_bf16 v[80:95], a[196:199], v[68:71], v[80:95]\n"
"ds_read_b128 a[212:215], v21 offset:4096\n"
"v_cvt_pk_bf16_f32 v72, v48, v49\n"
"v_cvt_pk_bf16_f32 v73, v50, v51\n"
"v_cvt_pk_bf16_f32 v74, v52, v53\n"
"v_cvt_pk_bf16_f32 v75, v54, v55\n"
"s_nop 1\n"
"s_waitcnt lgkmcnt(3)\n"
"v_mfma_f32_32x32x16_bf16 v[80:95], a[200:203], v[72:75], v[80:95]\n"
"ds_read_b128 a[216:219], v22 offset:4096\n"
"v_cvt_pk_bf16_f32 v76, v56, v57\n"
"v_cvt_pk_bf16_f32 v77, v58, v59\n"
"v_cvt_pk_bf16_f32 v78, v60, v61\n"
"v_cvt_pk_bf16_f32 v79, v62, v63\n"
"s_nop 1\n"
"s_waitcnt lgkmcnt(3)\n"
"v_mfma_f32_32x32x16_bf16 v[80:95], a[204:207], v[76:79], v[80:95]\n"
"ds_read_b128 a[220:223], v23 offset:4096\n"
"s_add_u32 m0, s60, 4096\n"
"s_mov_b32 s87, 16640\n"
"buffer_load_dwordx4 v4, s[44:47], s87 offen nt lds\n"
"s_waitcnt lgkmcnt(3)\n"
"v_mfma_f32_32x32x16_bf16 v[96:111], a[208:211], v[64:67], v[96:111]\n"
"ds_read_b128 a[192:195], v20 offset:8192\n"
"s_waitcnt lgkmcnt(3)\n"
"v_mfma_f32_32x32x16_bf16 v[96:111], a[212:215], v[68:71], v[96:111]\n"
"ds_read_b128 a[196:199], v21 offset:8192\n"
"s_waitcnt lgkmcnt(3)\n"
"v_mfma_f32_32x32x16_bf16 v[96:111], a[216:219], v[72:75], v[96:111]\n"
"ds_read_b128 a[200:203], v22 offset:8192\n"
"s_add_u32 m0, s60, 8192\n"
"s_mov_b32 s87, 33024\n"
"buffer_load_dwordx4 v4, s[44:47], s87 offen nt lds\n"
"s_waitcnt lgkmcnt(3)\n"
"v_mfma_f32_32x32x16_bf16 v[96:111], a[220:223], v[76:79], v[96:111]\n"
"ds_read_b128 a[204:207], v23 offset:8192\n"
"s_waitcnt lgkmcnt(3)\n"
"v_mfma_f32_32x32x16_bf16 v[112:127], a[192:195], v[64:67], v[112:127]\n"
"ds_read_b128 a[208:211], v20 offset:12288\n"
"s_waitcnt lgkmcnt(3)\n"
"v_mfma_f32_32x32x16_bf16 v[112:127], a[196:199], v[68:71], v[112:127]\n"
"ds_read_b128 a[212:215], v21 offset:12288\n"
"s_add_u32 m0, s60, 12288\n"
"s_mov_b32 s87, 49408\n"
"buffer_load_dwordx4 v4, s[44:47], s87 offen nt lds\n"
"s_waitcnt lgkmcnt(3)\n"
"v_mfma_f32_32x32x16_bf16 v[112:127], a[200:203], v[72:75], v[112:127]\n"
"ds_read_b128 a[216:219], v22 offset:12288\n"
"s_waitcnt lgkmcnt(3)\n"
"v_mfma_f32_32x32x16_bf16 v[112:127], a[204:207], v[76:79], v[112:127]\n"
"ds_read_b128 a[220:223], v23 offset:12288\n"
"s_waitcnt lgkmcnt(3)\n"
"v_mfma_f32_32x32x16_bf16 a[0:15], a[208:211], v[64:67], a[0:15]\n"
"ds_read_b128 a[192:195], v20 offset:16384\n"
"s_add_u32 m0, s60, 16384\n"
"s_mov_b32 s87, 65792\n"
"buffer_load_dwordx4 v4, s[44:47], s87 offen nt lds\n"
"s_waitcnt lgkmcnt(3)\n"
"v_mfma_f32_32x32x16_bf16 a[0:15], a[212:215], v[68:71], a[0:15]\n"
"ds_read_b128 a[196:199], v21 offset:16384\n"
"s_waitcnt lgkmcnt(3)\n"
"v_mfma_f32_32x32x16_bf16 a[0:15], a[216:219], v[72:75], a[0:15]\n"
"ds_read_b128 a[200:203], v22 offset:16384\n"
"s_waitcnt lgkmcnt(3)\n"
"v_mfma_f32_32x32x16_bf16 a[0:15], a[220:223], v[76:79], a[0:15]\n"
"ds_read_b128 a[204:207], v23 offset:16384\n"
"s_add_u32 m0, s60, 20480\n"
"s_mov_b32 s87, 82176\n"
"buffer_load_dwordx4 v4, s[44:47], s87 offen nt lds\n"
"s_waitcnt lgkmcnt(3)\n"
"v_mfma_f32_32x32x16_bf16 a[16:31], a[192:195], v[64:67], a[16:31]\n"
"ds_read_b128 a[208:211], v20 offset:20480\n"
"s_waitcnt lgkmcnt(3)\n"
"v_mfma_f32_32x32x16_bf16 a[16:31], a[196:199], v[68:71], a[16:31]\n"
"ds_read_b128 a[212:215], v21 offset:20480\n"
"s_waitcnt lgkmcnt(3)\n"
"v_mfma_f32_32x32x16_bf16 a[16:31], a[200:203], v[72:75], a[16:31]\n"
"ds_read_b128 a[216:219], v22 offset:20480\n"
"s_add_u32 m0, s60, 24576\n"
"s_mov_b32 s87, 98560\n"
"buffer_load_dwordx4 v4, s[44:47], s87 offen nt lds\n"
"s_waitcnt lgkmcnt(3)\n"
"v_mfma_f32_32x32x16_bf16 a[16:31], a[204:207], v[76:79], a[16:31]\n"
"ds_read_b128 a[220:223], v23 offset:20480\n"
"s_waitcnt lgkmcnt(3)\n"
"v_mfma_f32_32x32x16_bf16 a[32:47], a[208:211], v[64:67], a[32:47]\n"
"ds_read_b128 a[192:195], v20 offset:24576\n"
"s_waitcnt lgkmcnt(3)\n"
"v_mfma_f32_32x32x16_bf16 a[32:47], a[212:215], v[68:71], a[32:47]\n"
"ds_read_b128 a[196:199], v21 offset:24576\n"
"s_add_u32 m0, s60, 28672\n"
"s_mov_b32 s87, 114944\n"
"buffer_load_dwordx4 v4, s[44:47], s87 offen nt lds\n"
"s_waitcnt lgkmcnt(3)\n"
"v_mfma_f32_32x32x16_bf16 a[32:47], a[216:219], v[72:75], a[32:47]\n"
"ds_read_b128 a[200:203], v22 offset:24576\n"
"s_waitcnt lgkmcnt(3)\n"
"v_mfma_f32_32x32x16_bf16 a[32:47], a[220:223], v[76:79], a[32:47]\n"
"ds_read_b128 a[204:207], v23 offset:24576\n"
"s_waitcnt lgkmcnt(3)\n"
"v_mfma_f32_32x32x16_bf16 a[48:63], a[192:195], v[64:67], a[48:63]\n"
"ds_read_b128 a[208:211], v20 offset:28672\n"
"s_waitcnt lgkmcnt(3)\n"
"v_mfma_f32_32x32x16_bf16 a[48:63], a[196:199], v[68:71], a[48:63]\n"
"ds_read_b128 a[212:215], v21 offset:28672\n"
"s_waitcnt lgkmcnt(3)\n"
"v_mfma_f32_32x32x16_bf16 a[48:63], a[200:203], v[72:75], a[48:63]\n"
"ds_read_b128 a[216:219], v22 offset:28672\n"
"s_waitcnt lgkmcnt(3)\n"
"v_mfma_f32_32x32x16_bf16 a[48:63], a[204:207], v[76:79], a[48:63]\n"
"ds_read_b128 a[220:223], v23 offset:28672\n"
"v_add_u32 v20, 65536, v7\n"
"v_xor_b32 v21, 64, v20\n"
"s_waitcnt lgkmcnt(3)\n"
"v_mfma_f32_32x32x16_bf16 a[64:79], a[208:211], v[64:67], a[64:79]\n"
"ds_read_b64_tr_b16 a[192:193], v20 offset:0\n"
"ds_read_b64_tr_b16 a[194:195], v20 offset:512\n"
"s_waitcnt lgkmcnt(4)\n"
"v_mfma_f32_32x32x16_bf16 a[64:79], a[212:215], v[68:71], a[64:79]\n"
"ds_read_b64_tr_b16 a[196:197], v21 offset:0\n"
"ds_read_b64_tr_b16 a[198:199], v21 offset:512\n"
"s_waitcnt lgkmcnt(5)\n"
"v_mfma_f32_32x32x16_bf16 a[64:79], a[216:219], v[72:75], a[64:79]\n"
"ds_read_b64_tr_b16 a[200:201], v20 offset:2048\n"
"ds_read_b64_tr_b16 a[202:203], v20 offset:2560\n"
"s_waitcnt lgkmcnt(6)\n"
"v_mfma_f32_32x32x16_bf16 a[64:79], a[220:223], v[76:79], a[64:79]\n"
"ds_read_b64_tr_b16 a[204:205], v21 offset:2048\n"
"ds_read_b64_tr_b16 a[206:207], v21 offset:2560\n"
"s_waitcnt lgkmcnt(6)\n"
"v_mfma_f32_32x32x16_bf16 v[32:47], a[192:195], a[80:83], 0\n"
"s_waitcnt vmcnt(22)\n"
"s_barrier\n"
"ds_read_b64_tr_b16 a[208:209], v20 offset:4096\n"
"ds_read_b64_tr_b16 a[210:211], v20 offset:4608\n"
"s_add_u32 m0, s60, 32768\n"
"s_add_u32 s87, s80, 384\n"
"buffer_load_dwordx4 v3, s[40:43], s87 offen lds\n"
"s_waitcnt lgkmcnt(6)\n"
"v_mfma_f32_32x32x16_bf16 v[48:63], a[196:199], a[80:83], 0\n"
"ds_read_b64_tr_b16 a[212:213], v21 offset:4096\n"
"ds_read_b64_tr_b16 a[214:215], v21 offset:4608\n"
"s_waitcnt lgkmcnt(6)\n"
"v_mfma_f32_32x32x16_bf16 v[32:47], a[200:203], a[84:87], v[32:47]\n"
"ds_read_b64_tr_b16 a[216:217], v20 offset:6144\n"
"ds_read_b64_tr_b16 a[218:219], v20 offset:6656\n"
"s_add_u32 m0, s60, 36864\n"
"s_add_u32 s87, s80, 262528\n"
"buffer_load_dwordx4 v3, s[40:43], s87 offen lds\n"
"s_waitcnt lgkmcnt(6)\n"
"v_mfma_f32_32x32x16_bf16 v[48:63], a[204:207], a[84:87], v[48:63]\n"
"ds_read_b64_tr_b16 a[220:221], v21 offset:6144\n"
"ds_read_b64_tr_b16 a[222:223], v21 offset:6656\n"
"s_waitcnt lgkmcnt(6)\n"
"v_mfma_f32_32x32x16_bf16 v[32:47], a[208:211], a[88:91], v[32:47]\n"
"ds_read_b64_tr_b16 a[192:193], v20 offset:8192\n"
"ds_read_b64_tr_b16 a[194:195], v20 offset:8704\n"
"s_add_u32 m0, s60, 40960\n"
"s_add_u32 s87, s81, 384\n"
"buffer_load_dwordx4 v3, s[40:43], s87 offen lds\n"
"s_waitcnt lgkmcnt(6)\n"
"v_mfma_f32_32x32x16_bf16 v[48:63], a[212:215], a[88:91], v[48:63]\n"
"ds_read_b64_tr_b16 a[196:197], v21 offset:8192\n"
"ds_read_b64_tr_b16 a[198:199], v21 offset:8704\n"
"s_waitcnt lgkmcnt(6)\n"
"v_mfma_f32_32x32x16_bf16 v[32:47], a[216:219], a[92:95], v[32:47]\n"
"ds_read_b64_tr_b16 a[200:201], v20 offset:10240\n"
"ds_read_b64_tr_b16 a[202:203], v20 offset:10752\n"
"s_add_u32 m0, s60, 45056\n"
"s_add_u32 s87, s81, 262528\n"
"buffer_load_dwordx4 v3, s[40:43], s87 offen lds\n"
"s_waitcnt lgkmcnt(6)\n"
"v_mfma_f32_32x32x16_bf16 v[48:63], a[220:223], a[92:95], v[48:63]\n"
"ds_read_b64_tr_b16 a[204:205], v21 offset:10240\n"
"ds_read_b64_tr_b16 a[206:207], v21 offset:10752\n"
"s_waitcnt lgkmcnt(6)\n"
"v_mfma_f32_32x32x16_bf16 v[32:47], a[192:195], a[96:99], v[32:47]\n"
"ds_read_b64_tr_b16 a[208:209], v20 offset:12288\n"
"ds_read_b64_tr_b16 a[210:211], v20 offset:12800\n"
"s_add_u32 m0, s60, 49152\n"
"s_add_u32 s87, s82, 384\n"
"buffer_load_dwordx4 v3, s[40:43], s87 offen lds\n"
"s_waitcnt lgkmcnt(6)\n"
"v_mfma_f32_32x32x16_bf16 v[48:63], a[196:199], a[96:99], v[48:63]\n"
"ds_read_b64_tr_b16 a[212:213], v21 offset:12288\n"
"ds_read_b64_tr_b16 a[214:215], v21 offset:12800\n"
"s_waitcnt lgkmcnt(6)\n"
"v_mfma_f32_32x32x16_bf16 v[32:47], a[200:203], a[100:103], v[32:47]\n"
"ds_read_b64_tr_b16 a[216:217], v20 offset:14336\n"
"ds_read_b64_tr_b16 a[218:219], v20 offset:14848\n"
"s_add_u32 m0, s60, 53248\n"
"s_add_u32 s87, s82, 262528\n"
"buffer_load_dwordx4 v3, s[40:43], s87 offen lds\n"
"s_waitcnt lgkmcnt(6)\n"
"v_mfma_f32_32x32x16_bf16 v[48:63], a[204:207], a[100:103], v[48:63]\n"
"ds_read_b64_tr_b16 a[220:221], v21 offset:14336\n"
"ds_read_b64_tr_b16 a[222:223], v21 offset:14848\n"
"s_waitcnt lgkmcnt(6)\n"
"v_mfma_f32_32x32x16_bf16 v[32:47], a[208:211], a[104:107], v[32:47]\n"
"ds_read_b64_tr_b16 a[192:193], v20 offset:16384\n"
"ds_read_b64_tr_b16 a[194:195], v20 offset:16896\n"
"s_add_u32 m0, s60, 57344\n"
"s_add_u32 s87, s83, 384\n"
"buffer_load_dwordx4 v3, s[40:43], s87 offen lds\n"
"s_waitcnt lgkmcnt(6)\n"
"v_mfma_f32_32x32x16_bf16 v[48:63], a[212:215], a[104:107], v[48:63]\n"
"ds_read_b64_tr_b16 a[196:197], v21 offset:16384\n"
"ds_read_b64_tr_b16 a[198:199], v21 offset:16896\n"
"s_waitcnt lgkmcnt(6)\n"
"v_mfma_f32_32x32x16_bf16 v[32:47], a[216:219], a[108:111], v[32:47]\n"
"ds_read_b64_tr_b16 a[200:201], v20 offset:18432\n"
"ds_read_b64_tr_b16 a[202:203], v20 offset:18944\n"
"s_waitcnt lgkmcnt(6)\n"
"v_mfma_f32_32x32x16_bf16 v[48:63], a[220:223], a[108:111], v[48:63]\n"
"ds_read_b64_tr_b16 a[204:205], v21 offset:18432\n"
"ds_read_b64_tr_b16 a[206:207], v21 offset:18944\n"
"s_waitcnt lgkmcnt(6)\n"
"v_mfma_f32_32x32x16_bf16 v[32:47], a[192:195], a[112:115], v[32:47]\n"
"ds_read_b64_tr_b16 a[208:209], v20 offset:20480\n"
"ds_read_b64_tr_b16 a[210:211], v20 offset:20992\n"
"s_waitcnt lgkmcnt(6)\n"
"v_mfma_f32_32x32x16_bf16 v[48:63], a[196:199], a[112:115], v[48:63]\n"
"ds_read_b64_tr_b16 a[212:213], v21 offset:20480\n"
"ds_read_b64_tr_b16 a[214:215], v21 offset:20992\n"
"s_waitcnt lgkmcnt(6)\n"
"v_mfma_f32_32x32x16_bf16 v[32:47], a[200:203], a[116:119], v[32:47]\n"
"ds_read_b64_tr_b16 a[216:217], v20 offset:22528\n"
"ds_read_b64_tr_b16 a[218:219], v20 offset:23040\n"
"s_waitcnt lgkmcnt(6)\n"
"v_mfma_f32_32x32x16_bf16 v[48:63], a[204:207], a[116:119], v[48:63]\n"
"ds_read_b64_tr_b16 a[220:221], v21 offset:22528\n"
"ds_read_b64_tr_b16 a[222:223], v21 offset:23040\n"
"s_waitcnt lgkmcnt(6)\n"
"v_mfma_f32_32x32x16_bf16 v[32:47], a[208:211], a[120:123], v[32:47]\n"
"ds_read_b64_tr_b16 a[192:193], v20 offset:24576\n"
"ds_read_b64_tr_b16 a[194:195], v20 offset:25088\n"
"s_waitcnt lgkmcnt(6)\n"
"v_mfma_f32_32x32x16_bf16 v[48:63], a[212:215], a[120:123], v[48:63]\n"
"ds_read_b64_tr_b16 a[196:197], v21 offset:24576\n"
"ds_read_b64_tr_b16 a[198:199], v21 offset:25088\n"
"s_waitcnt lgkmcnt(6)\n"
"v_mfma_f32_32x32x16_bf16 v[32:47], a[216:219], a[124:127], v[32:47]\n"
"ds_read_b64_tr_b16 a[200:201], v20 offset:26624\n"
"ds_read_b64_tr_b16 a[202:203], v20 offset:27136\n"
"s_waitcnt lgkmcnt(6)\n"
"v_mfma_f32_32x32x16_bf16 v[48:63], a[220:223], a[124:127], v[48:63]\n"
"ds_read_b64_tr_b16 a[204:205], v21 offset:26624\n"
"ds_read_b64_tr_b16 a[206:207], v21 offset:27136\n"
"v_add_u32 v20, 98304, v8\n"
"v_xor_b32 v21, 32, v20\n"
"v_xor_b32 v22, 64, v20\n"
"v_xor_b32 v23, 0x60, v20\n"
"s_waitcnt lgkmcnt(6)\n"
"v_mfma_f32_32x32x16_bf16 v[32:47], a[192:195], a[128:131], v[32:47]\n"
"ds_read_b128 a[208:211], v20 offset:0\n"
"s_waitcnt lgkmcnt(5)\n"
"v_mfma_f32_32x32x16_bf16 v[48:63], a[196:199], a[128:131], v[48:63]\n"
"ds_read_b128 a[212:215], v21 offset:0\n"
"s_waitcnt lgkmcnt(4)\n"
"v_mfma_f32_32x32x16_bf16 v[32:47], a[200:203], a[132:135], v[32:47]\n"
"ds_read_b128 a[216:219], v22 offset:0\n"
"s_waitcnt lgkmcnt(3)\n"
"v_mfma_f32_32x32x16_bf16 v[48:63], a[204:207], a[132:135], v[48:63]\n"
"ds_read_b128 a[220:223], v23 offset:0\n"
"s_waitcnt lgkmcnt(3)\n"
"v_mfma_f32_32x32x16_bf16 v[128:143], a[208:211], v[64:67], v[128:143]\n"
"s_waitcnt vmcnt(21)\n"
"s_barrier\n"
"ds_read_b128 a[192:195], v20 offset:4096\n"
"s_add_u32 m0, s60, 65536\n"
"s_mov_b32 s87, 131328\n"
"buffer_load_dwordx4 v4, s[44:47], s87 offen nt lds\n"
"s_waitcnt lgkmcnt(3)\n"
"v_mfma_f32_32x32x16_bf16 v[128:143], a[212:215], v[68:71], v[128:143]\n"
"ds_read_b128 a[196:199], v21 offset:4096\n"
"s_waitcnt lgkmcnt(3)\n"
"v_mfma_f32_32x32x16_bf16 v[128:143], a[216:219], v[72:75], v[128:143]\n"
"ds_read_b128 a[200:203], v22 offset:4096\n"
"s_waitcnt lgkmcnt(3)\n"
"v_mfma_f32_32x32x16_bf16 v[128:143], a[220:223], v[76:79], v[128:143]\n"
"ds_read_b128 a[204:207], v23 offset:4096\n"
"s_add_u32 m0, s60, 69632\n"
"s_mov_b32 s87, 147712\n"
"buffer_load_dwordx4 v4, s[44:47], s87 offen nt lds\n"
"s_waitcnt lgkmcnt(3)\n"
"v_mfma_f32_32x32x16_bf16 v[144:159], a[192:195], v[64:67], v[144:159]\n"
"ds_read_b128 a[208:211], v20 offset:8192\n"
"s_waitcnt lgkmcnt(3)\n"
"v_mfma_f32_32x32x16_bf16 v[144:159], a[196:199], v[68:71], v[144:159]\n"
"ds_read_b128 a[212:215], v21 offset:8192\n"
"s_waitcnt lgkmcnt(3)\n"
"v_mfma_f32_32x32x16_bf16 v[144:159], a[200:203], v[72:75], v[144:159]\n"
"ds_read_b128 a[216:219], v22 offset:8192\n"
"s_add_u32 m0, s60, 73728\n"
"s_mov_b32 s87, 164096\n"
"buffer_load_dwordx4 v4, s[44:47], s87 offen nt lds\n"
"s_waitcnt lgkmcnt(3)\n"
"v_mfma_f32_32x32x16_bf16 v[144:159], a[204:207], v[76:79], v[144:159]\n"
"ds_read_b128 a[220:223], v23 offset:8192\n"
"s_waitcnt lgkmcnt(3)\n"
"v_mfma_f32_32x32x16_bf16 v[160:175], a[208:211], v[64:67], v[160:175]\n"
"ds_read_b128 a[192:195], v20 offset:12288\n"
"s_waitcnt lgkmcnt(3)\n"
"v_mfma_f32_32x32x16_bf16 v[160:175], a[212:215], v[68:71], v[160:175]\n"
"ds_read_b128 a[196:199], v21 offset:12288\n"
"s_add_u32 m0, s60, 77824\n"
"s_mov_b32 s87, 180480\n"
"buffer_load_dwordx4 v4, s[44:47], s87 offen nt lds\n"
"s_waitcnt lgkmcnt(3)\n"
"v_mfma_f32_32x32x16_bf16 v[160:175], a[216:219], v[72:75], v[160:175]\n"
"ds_read_b128 a[200:203], v22 offset:12288\n"
"s_waitcnt lgkmcnt(3)\n"
"v_mfma_f32_32x32x16_bf16 v[160:175], a[220:223], v[76:79], v[160:175]\n"
"ds_read_b128 a[204:207], v23 offset:12288\n"
"s_waitcnt lgkmcnt(3)\n"
"v_mfma_f32_32x32x16_bf16 v[176:191], a[192:195], v[64:67], v[176:191]\n"
"ds_read_b128 a[208:211], v20 offset:16384\n"
"s_add_u32 m0, s60, 81920\n"
"s_mov_b32 s87, 196864\n"
"buffer_load_dwordx4 v4, s[44:47], s87 offen nt lds\n"
"s_waitcnt lgkmcnt(3)\n"
"v_mfma_f32_32x32x16_bf16 v[176:191], a[196:199], v[68:71], v[176:191]\n"
"ds_read_b128 a[212:215], v21 offset:16384\n"
"s_waitcnt lgkmcnt(3)\n"
"v_mfma_f32_32x32x16_bf16 v[176:191], a[200:203], v[72:75], v[176:191]\n"
"ds_read_b128 a[216:219], v22 offset:16384\n"
"s_waitcnt lgkmcnt(3)\n"
"v_mfma_f32_32x32x16_bf16 v[176:191], a[204:207], v[76:79], v[176:191]\n"
"ds_read_b128 a[220:223], v23 offset:16384\n"
"s_add_u32 m0, s60, 86016\n"
"s_mov_b32 s87, 213248\n"
"buffer_load_dwordx4 v4, s[44:47], s87 offen nt lds\n"
"s_waitcnt lgkmcnt(3)\n"
"v_mfma_f32_32x32x16_bf16 v[192:207], a[208:211], v[64:67], v[192:207]\n"
"ds_read_b128 a[192:195], v20 offset:20480\n"
"s_waitcnt lgkmcnt(3)\n"
"v_mfma_f32_32x32x16_bf16 v[192:207], a[212:215], v[68:71], v[192:207]\n"
"ds_read_b128 a[196:199], v21 offset:20480\n"
"s_waitcnt lgkmcnt(3)\n"
"v_mfma_f32_32x32x16_bf16 v[192:207], a[216:219], v[72:75], v[192:207]\n"
"ds_read_b128 a[200:203], v22 offset:20480\n"
"s_add_u32 m0, s60, 90112\n"
"s_mov_b32 s87, 229632\n"
"buffer_load_dwordx4 v4, s[44:47], s87 offen nt lds\n"
"s_waitcnt lgkmcnt(3)\n"
"v_mfma_f32_32x32x16_bf16 v[192:207], a[220:223], v[76:79], v[192:207]\n"
"ds_read_b128 a[204:207], v23 offset:20480\n"
"s_waitcnt lgkmcnt(3)\n"
"v_mfma_f32_32x32x16_bf16 v[208:223], a[192:195], v[64:67], v[208:223]\n"
"ds_read_b128 a[208:211], v20 offset:24576\n"
"s_waitcnt lgkmcnt(3)\n"
"v_mfma_f32_32x32x16_bf16 v[208:223], a[196:199], v[68:71], v[208:223]\n"
"ds_read_b128 a[212:215], v21 offset:24576\n"
"s_add_u32 m0, s60, 94208\n"
"s_mov_b32 s87, 246016\n"
"buffer_load_dwordx4 v4, s[44:47], s87 offen nt lds\n"
"s_waitcnt lgkmcnt(3)\n"
"v_mfma_f32_32x32x16_bf16 v[208:223], a[200:203], v[72:75], v[208:223]\n"
"ds_read_b128 a[216:219], v22 offset:24576\n"
"s_waitcnt lgkmcnt(3)\n"
"v_mfma_f32_32x32x16_bf16 v[208:223], a[204:207], v[76:79], v[208:223]\n"
"ds_read_b128 a[220:223], v23 offset:24576\n"
"s_waitcnt lgkmcnt(3)\n"
"v_mfma_f32_32x32x16_bf16 v[224:239], a[208:211], v[64:67], v[224:239]\n"
"ds_read_b128 a[192:195], v20 offset:28672\n"
"s_waitcnt lgkmcnt(3)\n"
"v_mfma_f32_32x32x16_bf16 v[224:239], a[212:215], v[68:71], v[224:239]\n"
"ds_read_b128 a[196:199], v21 offset:28672\n"
"s_waitcnt lgkmcnt(3)\n"
"v_mfma_f32_32x32x16_bf16 v[224:239], a[216:219], v[72:75], v[224:239]\n"
"ds_read_b128 a[200:203], v22 offset:28672\n"
"s_waitcnt lgkmcnt(3)\n"
"v_mfma_f32_32x32x16_bf16 v[224:239], a[220:223], v[76:79], v[224:239]\n"
"ds_read_b128 a[204:207], v23 offset:28672\n"
"v_add_u32 v20, 131072, v7\n"
"v_xor_b32 v21, 64, v20\n"
"s_waitcnt lgkmcnt(3)\n"
"v_mfma_f32_32x32x16_bf16 v[240:255], a[192:195], v[64:67], v[240:255]\n"
"ds_read_b64_tr_b16 a[208:209], v20 offset:0\n"
"ds_read_b64_tr_b16 a[210:211], v20 offset:512\n"
"s_waitcnt lgkmcnt(4)\n"
"v_mfma_f32_32x32x16_bf16 v[240:255], a[196:199], v[68:71], v[240:255]\n"
"ds_read_b64_tr_b16 a[212:213], v21 offset:0\n"
"ds_read_b64_tr_b16 a[214:215], v21 offset:512\n"
"s_waitcnt lgkmcnt(5)\n"
"v_mfma_f32_32x32x16_bf16 v[240:255], a[200:203], v[72:75], v[240:255]\n"
"ds_read_b64_tr_b16 a[216:217], v20 offset:2048\n"
"ds_read_b64_tr_b16 a[218:219], v20 offset:2560\n"
"s_waitcnt lgkmcnt(6)\n"
"v_mfma_f32_32x32x16_bf16 v[240:255], a[204:207], v[76:79], v[240:255]\n"
"ds_read_b64_tr_b16 a[220:221], v21 offset:2048\n"
"ds_read_b64_tr_b16 a[222:223], v21 offset:2560\n"
"s_waitcnt lgkmcnt(6)\n"
"v_mfma_f32_32x32x16_bf16 v[32:47], a[208:211], a[136:139], v[32:47]\n"
"s_waitcnt vmcnt(22)\n"
"s_barrier\n"
"ds_read_b64_tr_b16 a[192:193], v20 offset:4096\n"
"ds_read_b64_tr_b16 a[194:195], v20 offset:4608\n"
"s_add_u32 m0, s60, 98304\n"
"s_add_u32 s87, s83, 262528\n"
"buffer_load_dwordx4 v3, s[40:43], s87 offen lds\n"
"s_waitcnt lgkmcnt(6)\n"
"v_mfma_f32_32x32x16_bf16 v[48:63], a[212:215], a[136:139], v[48:63]\n"
"ds_read_b64_tr_b16 a[196:197], v21 offset:4096\n"
"ds_read_b64_tr_b16 a[198:199], v21 offset:4608\n"
"s_waitcnt lgkmcnt(6)\n"
"v_mfma_f32_32x32x16_bf16 v[32:47], a[216:219], a[140:143], v[32:47]\n"
"ds_read_b64_tr_b16 a[200:201], v20 offset:6144\n"
"ds_read_b64_tr_b16 a[202:203], v20 offset:6656\n"
"s_add_u32 m0, s60, 102400\n"
"s_add_u32 s87, s84, 384\n"
"buffer_load_dwordx4 v3, s[40:43], s87 offen lds\n"
"s_waitcnt lgkmcnt(6)\n"
"v_mfma_f32_32x32x16_bf16 v[48:63], a[220:223], a[140:143], v[48:63]\n"
"ds_read_b64_tr_b16 a[204:205], v21 offset:6144\n"
"ds_read_b64_tr_b16 a[206:207], v21 offset:6656\n"
"s_waitcnt lgkmcnt(6)\n"
"v_mfma_f32_32x32x16_bf16 v[32:47], a[192:195], a[144:147], v[32:47]\n"
"ds_read_b64_tr_b16 a[208:209], v20 offset:8192\n"
"ds_read_b64_tr_b16 a[210:211], v20 offset:8704\n"
"s_add_u32 m0, s60, 106496\n"
"s_add_u32 s87, s84, 262528\n"
"buffer_load_dwordx4 v3, s[40:43], s87 offen lds\n"
"s_waitcnt lgkmcnt(6)\n"
"v_mfma_f32_32x32x16_bf16 v[48:63], a[196:199], a[144:147], v[48:63]\n"
"ds_read_b64_tr_b16 a[212:213], v21 offset:8192\n"
"ds_read_b64_tr_b16 a[214:215], v21 offset:8704\n"
"s_waitcnt lgkmcnt(6)\n"
"v_mfma_f32_32x32x16_bf16 v[32:47], a[200:203], a[148:151], v[32:47]\n"
"ds_read_b64_tr_b16 a[216:217], v20 offset:10240\n"
"ds_read_b64_tr_b16 a[218:219], v20 offset:10752\n"
"s_add_u32 m0, s60, 110592\n"
"s_add_u32 s87, s85, 384\n"
"buffer_load_dwordx4 v3, s[40:43], s87 offen lds\n"
"s_waitcnt lgkmcnt(6)\n"
"v_mfma_f32_32x32x16_bf16 v[48:63], a[204:207], a[148:151], v[48:63]\n"
"ds_read_b64_tr_b16 a[220:221], v21 offset:10240\n"
"ds_read_b64_tr_b16 a[222:223], v21 offset:10752\n"
"s_waitcnt lgkmcnt(6)\n"
"v_mfma_f32_32x32x16_bf16 v[32:47], a[208:211], a[152:155], v[32:47]\n"
"ds_read_b64_tr_b16 a[192:193], v20 offset:12288\n"
"ds_read_b64_tr_b16 a[194:195], v20 offset:12800\n"
"s_add_u32 m0, s60, 114688\n"
"s_add_u32 s87, s85, 262528\n"
"buffer_load_dwordx4 v3, s[40:43], s87 offen lds\n"
"s_waitcnt lgkmcnt(6)\n"
"v_mfma_f32_32x32x16_bf16 v[48:63], a[212:215], a[152:155], v[48:63]\n"
"ds_read_b64_tr_b16 a[196:197], v21 offset:12288\n"
"ds_read_b64_tr_b16 a[198:199], v21 offset:12800\n"
"s_waitcnt lgkmcnt(6)\n"
"v_mfma_f32_32x32x16_bf16 v[32:47], a[216:219], a[156:159], v[32:47]\n"
"ds_read_b64_tr_b16 a[200:201], v20 offset:14336\n"
"ds_read_b64_tr_b16 a[202:203], v20 offset:14848\n"
"s_add_u32 m0, s60, 118784\n"
"s_add_u32 s87, s86, 384\n"
"buffer_load_dwordx4 v3, s[40:43], s87 offen lds\n"
"s_waitcnt lgkmcnt(6)\n"
"v_mfma_f32_32x32x16_bf16 v[48:63], a[220:223], a[156:159], v[48:63]\n"
"ds_read_b64_tr_b16 a[204:205], v21 offset:14336\n"
"ds_read_b64_tr_b16 a[206:207], v21 offset:14848\n"
"s_waitcnt lgkmcnt(6)\n"
"v_mfma_f32_32x32x16_bf16 v[32:47], a[192:195], a[160:163], v[32:47]\n"
"ds_read_b64_tr_b16 a[208:209], v20 offset:16384\n"
"ds_read_b64_tr_b16 a[210:211], v20 offset:16896\n"
"s_waitcnt lgkmcnt(6)\n"
"v_mfma_f32_32x32x16_bf16 v[48:63], a[196:199], a[160:163], v[48:63]\n"
"ds_read_b64_tr_b16 a[212:213], v21 offset:16384\n"
"ds_read_b64_tr_b16 a[214:215], v21 offset:16896\n"
"s_add_u32 m0, s60, 122880\n"
"s_add_u32 s87, s86, 262528\n"
"buffer_load_dwordx4 v3, s[40:43], s87 offen lds\n"
"s_waitcnt lgkmcnt(6)\n"
"v_mfma_f32_32x32x16_bf16 v[32:47], a[200:203], a[164:167], v[32:47]\n"
"ds_read_b64_tr_b16 a[216:217], v20 offset:18432\n"
"ds_read_b64_tr_b16 a[218:219], v20 offset:18944\n"
"s_waitcnt lgkmcnt(6)\n"
"v_mfma_f32_32x32x16_bf16 v[48:63], a[204:207], a[164:167], v[48:63]\n"
"ds_read_b64_tr_b16 a[220:221], v21 offset:18432\n"
"ds_read_b64_tr_b16 a[222:223], v21 offset:18944\n"
"s_waitcnt lgkmcnt(6)\n"
"v_mfma_f32_32x32x16_bf16 v[32:47], a[208:211], a[168:171], v[32:47]\n"
"ds_read_b64_tr_b16 a[192:193], v20 offset:20480\n"
"ds_read_b64_tr_b16 a[194:195], v20 offset:20992\n"
"s_waitcnt lgkmcnt(6)\n"
"v_mfma_f32_32x32x16_bf16 v[48:63], a[212:215], a[168:171], v[48:63]\n"
"ds_read_b64_tr_b16 a[196:197], v21 offset:20480\n"
"ds_read_b64_tr_b16 a[198:199], v21 offset:20992\n"
"s_waitcnt lgkmcnt(6)\n"
"v_mfma_f32_32x32x16_bf16 v[32:47], a[216:219], a[172:175], v[32:47]\n"
"ds_read_b64_tr_b16 a[200:201], v20 offset:22528\n"
"ds_read_b64_tr_b16 a[202:203], v20 offset:23040\n"
"s_waitcnt lgkmcnt(6)\n"
"v_mfma_f32_32x32x16_bf16 v[48:63], a[220:223], a[172:175], v[48:63]\n"
"ds_read_b64_tr_b16 a[204:205], v21 offset:22528\n"
"ds_read_b64_tr_b16 a[206:207], v21 offset:23040\n"
"s_waitcnt lgkmcnt(6)\n"
"v_mfma_f32_32x32x16_bf16 v[32:47], a[192:195], a[176:179], v[32:47]\n"
"ds_read_b64_tr_b16 a[208:209], v20 offset:24576\n"
"ds_read_b64_tr_b16 a[210:211], v20 offset:25088\n"
"s_waitcnt lgkmcnt(6)\n"
"v_mfma_f32_32x32x16_bf16 v[48:63], a[196:199], a[176:179], v[48:63]\n"
"ds_read_b64_tr_b16 a[216:217], v20 offset:26624\n"
"ds_read_b64_tr_b16 a[218:219], v20 offset:27136\n"
"s_waitcnt lgkmcnt(6)\n"
"v_mfma_f32_32x32x16_bf16 v[32:47], a[200:203], a[180:183], v[32:47]\n"
"ds_read_b64_tr_b16 a[212:213], v21 offset:24576\n"
"ds_read_b64_tr_b16 a[214:215], v21 offset:25088\n"
"s_waitcnt lgkmcnt(6)\n"
"v_mfma_f32_32x32x16_bf16 v[48:63], a[204:207], a[180:183], v[48:63]\n"
"ds_read_b64_tr_b16 a[220:221], v21 offset:26624\n"
"ds_read_b64_tr_b16 a[222:223], v21 offset:27136\n"
"v_mov_b32 v20, v8\n"
"v_xor_b32 v21, 32, v20\n"
"v_xor_b32 v22, 64, v20\n"
"v_xor_b32 v23, 0x60, v20\n"
"s_waitcnt lgkmcnt(6)\n"
"v_mfma_f32_32x32x16_bf16 v[32:47], a[208:211], a[184:187], v[32:47]\n"
"ds_read_b128 a[192:195], v20 offset:0\n"
"s_waitcnt lgkmcnt(5)\n"
"v_mfma_f32_32x32x16_bf16 v[32:47], a[216:219], a[188:191], v[32:47]\n"
"ds_read_b128 a[196:199], v21 offset:0\n"
"s_waitcnt lgkmcnt(4)\n"
"v_mfma_f32_32x32x16_bf16 v[48:63], a[212:215], a[184:187], v[48:63]\n"
"ds_read_b128 a[200:203], v22 offset:0\n"
"s_waitcnt lgkmcnt(3)\n"
"v_mfma_f32_32x32x16_bf16 v[48:63], a[220:223], a[188:191], v[48:63]\n"
"ds_read_b128 a[204:207], v23 offset:0\n"
"s_nop 7\n"
"v_cvt_pk_bf16_f32 v64, v32, v33\n"
"v_cvt_pk_bf16_f32 v65, v34, v35\n"
"v_cvt_pk_bf16_f32 v66, v36, v37\n"
"v_cvt_pk_bf16_f32 v67, v38, v39\n"
"s_nop 1\n"
"s_waitcnt lgkmcnt(3)\n"
"v_mfma_f32_32x32x16_bf16 v[80:95], a[192:195], v[64:67], v[80:95]\n"
"s_waitcnt vmcnt(21)\n"
"s_barrier\n"
"ds_read_b128 a[208:211], v20 offset:4096\n"
"v_cvt_pk_bf16_f32 v68, v40, v41\n"
"v_cvt_pk_bf16_f32 v69, v42, v43\n"
"v_cvt_pk_bf16_f32 v70, v44, v45\n"
"v_cvt_pk_bf16_f32 v71, v46, v47\n"
"s_nop 1\n"
"s_add_u32 m0, s60, 131072\n"
"s_mov_b32 s87, 384\n"
"buffer_load_dwordx4 v4, s[44:47], s87 offen nt lds\n"
"s_waitcnt lgkmcnt(3)\n"
"v_mfma_f32_32x32x16_bf16 v[80:95], a[196:199], v[68:71], v[80:95]\n"
"ds_read_b128 a[212:215], v21 offset:4096\n"
"v_cvt_pk_bf16_f32 v72, v48, v49\n"
"v_cvt_pk_bf16_f32 v73, v50, v51\n"
"v_cvt_pk_bf16_f32 v74, v52, v53\n"
"v_cvt_pk_bf16_f32 v75, v54, v55\n"
"s_nop 1\n"
"s_waitcnt lgkmcnt(3)\n"
"v_mfma_f32_32x32x16_bf16 v[80:95], a[200:203], v[72:75], v[80:95]\n"
"ds_read_b128 a[216:219], v22 offset:4096\n"
"v_cvt_pk_bf16_f32 v76, v56, v57\n"
"v_cvt_pk_bf16_f32 v77, v58, v59\n"
"v_cvt_pk_bf16_f32 v78, v60, v61\n"
"v_cvt_pk_bf16_f32 v79, v62, v63\n"
"s_nop 1\n"
"s_waitcnt lgkmcnt(3)\n"
"v_mfma_f32_32x32x16_bf16 v[80:95], a[204:207], v[76:79], v[80:95]\n"
"ds_read_b128 a[220:223], v23 offset:4096\n"
"s_add_u32 m0, s60, 135168\n"
"s_mov_b32 s87, 16768\n"
"buffer_load_dwordx4 v4, s[44:47], s87 offen nt lds\n"
"s_waitcnt lgkmcnt(3)\n"
"v_mfma_f32_32x32x16_bf16 v[96:111], a[208:211], v[64:67], v[96:111]\n"
"ds_read_b128 a[192:195], v20 offset:8192\n"
"s_waitcnt lgkmcnt(3)\n"
"v_mfma_f32_32x32x16_bf16 v[96:111], a[212:215], v[68:71], v[96:111]\n"
"ds_read_b128 a[196:199], v21 offset:8192\n"
"s_waitcnt lgkmcnt(3)\n"
"v_mfma_f32_32x32x16_bf16 v[96:111], a[216:219], v[72:75], v[96:111]\n"
"ds_read_b128 a[200:203], v22 offset:8192\n"
"s_add_u32 m0, s60, 139264\n"
"s_mov_b32 s87, 33152\n"
"buffer_load_dwordx4 v4, s[44:47], s87 offen nt lds\n"
"s_waitcnt lgkmcnt(3)\n"
"v_mfma_f32_32x32x16_bf16 v[96:111], a[220:223], v[76:79], v[96:111]\n"
"ds_read_b128 a[204:207], v23 offset:8192\n"
"s_waitcnt lgkmcnt(3)\n"
"v_mfma_f32_32x32x16_bf16 v[112:127], a[192:195], v[64:67], v[112:127]\n"
"ds_read_b128 a[208:211], v20 offset:12288\n"
"s_waitcnt lgkmcnt(3)\n"
"v_mfma_f32_32x32x16_bf16 v[112:127], a[196:199], v[68:71], v[112:127]\n"
"ds_read_b128 a[212:215], v21 offset:12288\n"
"s_add_u32 m0, s60, 143360\n"
"s_mov_b32 s87, 49536\n"
"buffer_load_dwordx4 v4, s[44:47], s87 offen nt lds\n"
"s_waitcnt lgkmcnt(3)\n"
"v_mfma_f32_32x32x16_bf16 v[112:127], a[200:203], v[72:75], v[112:127]\n"
"ds_read_b128 a[216:219], v22 offset:12288\n"
"s_waitcnt lgkmcnt(3)\n"
"v_mfma_f32_32x32x16_bf16 v[112:127], a[204:207], v[76:79], v[112:127]\n"
"ds_read_b128 a[220:223], v23 offset:12288\n"
"s_waitcnt lgkmcnt(3)\n"
"v_mfma_f32_32x32x16_bf16 a[0:15], a[208:211], v[64:67], a[0:15]\n"
"ds_read_b128 a[192:195], v20 offset:16384\n"
"s_add_u32 m0, s60, 147456\n"
"s_mov_b32 s87, 65920\n"
"buffer_load_dwordx4 v4, s[44:47], s87 offen nt lds\n"
"s_waitcnt lgkmcnt(3)\n"
"v_mfma_f32_32x32x16_bf16 a[0:15], a[212:215], v[68:71], a[0:15]\n"
"ds_read_b128 a[196:199], v21 offset:16384\n"
"s_waitcnt lgkmcnt(3)\n"
"v_mfma_f32_32x32x16_bf16 a[0:15], a[216:219], v[72:75], a[0:15]\n"
"ds_read_b128 a[200:203], v22 offset:16384\n"
"s_waitcnt lgkmcnt(3)\n"
"v_mfma_f32_32x32x16_bf16 a[0:15], a[220:223], v[76:79], a[0:15]\n"
"ds_read_b128 a[204:207], v23 offset:16384\n"
"s_add_u32 m0, s60, 151552\n"
"s_mov_b32 s87, 82304\n"
"buffer_load_dwordx4 v4, s[44:47], s87 offen nt lds\n"
"s_waitcnt lgkmcnt(3)\n"
"v_mfma_f32_32x32x16_bf16 a[16:31], a[192:195], v[64:67], a[16:31]\n"
"ds_read_b128 a[208:211], v20 offset:20480\n"
"s_waitcnt lgkmcnt(3)\n"
"v_mfma_f32_32x32x16_bf16 a[16:31], a[196:199], v[68:71], a[16:31]\n"
"ds_read_b128 a[212:215], v21 offset:20480\n"
"s_waitcnt lgkmcnt(3)\n"
"v_mfma_f32_32x32x16_bf16 a[16:31], a[200:203], v[72:75], a[16:31]\n"
"ds_read_b128 a[216:219], v22 offset:20480\n"
"s_add_u32 m0, s60, 155648\n"
"s_mov_b32 s87, 98688\n"
"buffer_load_dwordx4 v4, s[44:47], s87 offen nt lds\n"
"s_waitcnt lgkmcnt(3)\n"
"v_mfma_f32_32x32x16_bf16 a[16:31], a[204:207], v[76:79], a[16:31]\n"
"ds_read_b128 a[220:223], v23 offset:20480\n"
"s_waitcnt lgkmcnt(3)\n"
"v_mfma_f32_32x32x16_bf16 a[32:47], a[208:211], v[64:67], a[32:47]\n"
"ds_read_b128 a[192:195], v20 offset:24576\n"
"s_waitcnt lgkmcnt(3)\n"
"v_mfma_f32_32x32x16_bf16 a[32:47], a[212:215], v[68:71], a[32:47]\n"
"ds_read_b128 a[196:199], v21 offset:24576\n"
"s_add_u32 m0, s60, 159744\n"
"s_mov_b32 s87, 115072\n"
"buffer_load_dwordx4 v4, s[44:47], s87 offen nt lds\n"
"s_waitcnt lgkmcnt(3)\n"
"v_mfma_f32_32x32x16_bf16 a[32:47], a[216:219], v[72:75], a[32:47]\n"
"ds_read_b128 a[200:203], v22 offset:24576\n"
"s_waitcnt lgkmcnt(3)\n"
"v_mfma_f32_32x32x16_bf16 a[32:47], a[220:223], v[76:79], a[32:47]\n"
"ds_read_b128 a[204:207], v23 offset:24576\n"
"s_waitcnt lgkmcnt(3)\n"
"v_mfma_f32_32x32x16_bf16 a[48:63], a[192:195], v[64:67], a[48:63]\n"
"ds_read_b128 a[208:211], v20 offset:28672\n"
"s_waitcnt lgkmcnt(3)\n"
"v_mfma_f32_32x32x16_bf16 a[48:63], a[196:199], v[68:71], a[48:63]\n"
"ds_read_b128 a[212:215], v21 offset:28672\n"
"s_waitcnt lgkmcnt(3)\n"
"v_mfma_f32_32x32x16_bf16 a[48:63], a[200:203], v[72:75], a[48:63]\n"
"ds_read_b128 a[216:219], v22 offset:28672\n"
"s_waitcnt lgkmcnt(3)\n"
"v_mfma_f32_32x32x16_bf16 a[48:63], a[204:207], v[76:79], a[48:63]\n"
"ds_read_b128 a[220:223], v23 offset:28672\n"
"v_add_u32 v20, 32768, v7\n"
"v_xor_b32 v21, 64, v20\n"
"s_waitcnt lgkmcnt(3)\n"
"v_mfma_f32_32x32x16_bf16 a[64:79], a[208:211], v[64:67], a[64:79]\n"
"ds_read_b64_tr_b16 a[192:193], v20 offset:0\n"
"ds_read_b64_tr_b16 a[194:195], v20 offset:512\n"
"s_waitcnt lgkmcnt(4)\n"
"v_mfma_f32_32x32x16_bf16 a[64:79], a[212:215], v[68:71], a[64:79]\n"
"ds_read_b64_tr_b16 a[196:197], v21 offset:0\n"
"ds_read_b64_tr_b16 a[198:199], v21 offset:512\n"
"s_waitcnt lgkmcnt(5)\n"
"v_mfma_f32_32x32x16_bf16 a[64:79], a[216:219], v[72:75], a[64:79]\n"
"ds_read_b64_tr_b16 a[200:201], v20 offset:2048\n"
"ds_read_b64_tr_b16 a[202:203], v20 offset:2560\n"
"s_waitcnt lgkmcnt(6)\n"
"v_mfma_f32_32x32x16_bf16 a[64:79], a[220:223], v[76:79], a[64:79]\n"
"ds_read_b64_tr_b16 a[204:205], v21 offset:2048\n"
"ds_read_b64_tr_b16 a[206:207], v21 offset:2560\n"
"s_waitcnt lgkmcnt(6)\n"
"v_mfma_f32_32x32x16_bf16 v[32:47], a[192:195], a[80:83], 0\n"
"s_waitcnt vmcnt(22)\n"
"s_barrier\n"
"ds_read_b64_tr_b16 a[208:209], v20 offset:4096\n"
"ds_read_b64_tr_b16 a[210:211], v20 offset:4608\n"
"s_add_u32 m0, s60, 0\n"
"s_mov_b32 s87, 131456\n"
"buffer_load_dwordx4 v4, s[44:47], s87 offen nt lds\n"
"s_waitcnt lgkmcnt(6)\n"
"v_mfma_f32_32x32x16_bf16 v[48:63], a[196:199], a[80:83], 0\n"
"ds_read_b64_tr_b16 a[212:213], v21 offset:4096\n"
"ds_read_b64_tr_b16 a[214:215], v21 offset:4608\n"
"s_waitcnt lgkmcnt(6)\n"
"v_mfma_f32_32x32x16_bf16 v[32:47], a[200:203], a[84:87], v[32:47]\n"
"ds_read_b64_tr_b16 a[216:217], v20 offset:6144\n"
"ds_read_b64_tr_b16 a[218:219], v20 offset:6656\n"
"s_waitcnt lgkmcnt(6)\n"
"v_mfma_f32_32x32x16_bf16 v[48:63], a[204:207], a[84:87], v[48:63]\n"
"ds_read_b64_tr_b16 a[220:221], v21 offset:6144\n"
"ds_read_b64_tr_b16 a[222:223], v21 offset:6656\n"
"s_add_u32 m0, s60, 4096\n"
"s_mov_b32 s87, 147840\n"
"buffer_load_dwordx4 v4, s[44:47], s87 offen nt lds\n"
"s_waitcnt lgkmcnt(6)\n"
"v_mfma_f32_32x32x16_bf16 v[32:47], a[208:211], a[88:91], v[32:47]\n"
"ds_read_b64_tr_b16 a[192:193], v20 offset:8192\n"
"ds_read_b64_tr_b16 a[194:195], v20 offset:8704\n"
"s_waitcnt lgkmcnt(6)\n"
"v_mfma_f32_32x32x16_bf16 v[48:63], a[212:215], a[88:91], v[48:63]\n"
"ds_read_b64_tr_b16 a[196:197], v21 offset:8192\n"
"ds_read_b64_tr_b16 a[198:199], v21 offset:8704\n"
"s_waitcnt lgkmcnt(6)\n"
"v_mfma_f32_32x32x16_bf16 v[32:47], a[216:219], a[92:95], v[32:47]\n"
"ds_read_b64_tr_b16 a[200:201], v20 offset:10240\n"
"ds_read_b64_tr_b16 a[202:203], v20 offset:10752\n"
"s_add_u32 m0, s60, 8192\n"
"s_mov_b32 s87, 164224\n"
"buffer_load_dwordx4 v4, s[44:47], s87 offen nt lds\n"
"s_waitcnt lgkmcnt(6)\n"
"v_mfma_f32_32x32x16_bf16 v[48:63], a[220:223], a[92:95], v[48:63]\n"
"ds_read_b64_tr_b16 a[204:205], v21 offset:10240\n"
"ds_read_b64_tr_b16 a[206:207], v21 offset:10752\n"
"s_waitcnt lgkmcnt(6)\n"
"v_mfma_f32_32x32x16_bf16 v[32:47], a[192:195], a[96:99], v[32:47]\n"
"ds_read_b64_tr_b16 a[208:209], v20 offset:12288\n"
"ds_read_b64_tr_b16 a[210:211], v20 offset:12800\n"
"s_waitcnt lgkmcnt(6)\n"
"v_mfma_f32_32x32x16_bf16 v[48:63], a[196:199], a[96:99], v[48:63]\n"
"ds_read_b64_tr_b16 a[212:213], v21 offset:12288\n"
"ds_read_b64_tr_b16 a[214:215], v21 offset:12800\n"
"s_waitcnt lgkmcnt(6)\n"
"v_mfma_f32_32x32x16_bf16 v[32:47], a[200:203], a[100:103], v[32:47]\n"
"ds_read_b64_tr_b16 a[216:217], v20 offset:14336\n"
"ds_read_b64_tr_b16 a[218:219], v20 offset:14848\n"
"s_waitcnt lgkmcnt(6)\n"
"v_mfma_f32_32x32x16_bf16 v[48:63], a[204:207], a[100:103], v[48:63]\n"
"ds_read_b64_tr_b16 a[220:221], v21 offset:14336\n"
"ds_read_b64_tr_b16 a[222:223], v21 offset:14848\n"
"s_waitcnt lgkmcnt(6)\n"
"v_mfma_f32_32x32x16_bf16 v[32:47], a[208:211], a[104:107], v[32:47]\n"
"ds_read_b64_tr_b16 a[192:193], v20 offset:16384\n"
"ds_read_b64_tr_b16 a[194:195], v20 offset:16896\n"
"s_add_u32 m0, s60, 12288\n"
"s_mov_b32 s87, 180608\n"
"buffer_load_dwordx4 v4, s[44:47], s87 offen nt lds\n"
"s_waitcnt lgkmcnt(6)\n"
"v_mfma_f32_32x32x16_bf16 v[48:63], a[212:215], a[104:107], v[48:63]\n"
"ds_read_b64_tr_b16 a[196:197], v21 offset:16384\n"
"ds_read_b64_tr_b16 a[198:199], v21 offset:16896\n"
"s_waitcnt lgkmcnt(6)\n"
"v_mfma_f32_32x32x16_bf16 v[32:47], a[216:219], a[108:111], v[32:47]\n"
"ds_read_b64_tr_b16 a[200:201], v20 offset:18432\n"
"ds_read_b64_tr_b16 a[202:203], v20 offset:18944\n"
"s_waitcnt lgkmcnt(6)\n"
"v_mfma_f32_32x32x16_bf16 v[48:63], a[220:223], a[108:111], v[48:63]\n"
"ds_read_b64_tr_b16 a[204:205], v21 offset:18432\n"
"ds_read_b64_tr_b16 a[206:207], v21 offset:18944\n"
"s_waitcnt lgkmcnt(6)\n"
"v_mfma_f32_32x32x16_bf16 v[32:47], a[192:195], a[112:115], v[32:47]\n"
"ds_read_b64_tr_b16 a[208:209], v20 offset:20480\n"
"ds_read_b64_tr_b16 a[210:211], v20 offset:20992\n"
"s_waitcnt lgkmcnt(6)\n"
"v_mfma_f32_32x32x16_bf16 v[48:63], a[196:199], a[112:115], v[48:63]\n"
"ds_read_b64_tr_b16 a[212:213], v21 offset:20480\n"
"ds_read_b64_tr_b16 a[214:215], v21 offset:20992\n"
"s_waitcnt lgkmcnt(6)\n"
"v_mfma_f32_32x32x16_bf16 v[32:47], a[200:203], a[116:119], v[32:47]\n"
"ds_read_b64_tr_b16 a[216:217], v20 offset:22528\n"
"ds_read_b64_tr_b16 a[218:219], v20 offset:23040\n"
"s_add_u32 m0, s60, 16384\n"
"s_mov_b32 s87, 196992\n"
"buffer_load_dwordx4 v4, s[44:47], s87 offen nt lds\n"
"s_waitcnt lgkmcnt(6)\n"
"v_mfma_f32_32x32x16_bf16 v[48:63], a[204:207], a[116:119], v[48:63]\n"
"ds_read_b64_tr_b16 a[220:221], v21 offset:22528\n"
"ds_read_b64_tr_b16 a[222:223], v21 offset:23040\n"
"s_waitcnt lgkmcnt(6)\n"
"v_mfma_f32_32x32x16_bf16 v[32:47], a[208:211], a[120:123], v[32:47]\n"
"ds_read_b64_tr_b16 a[192:193], v20 offset:24576\n"
"ds_read_b64_tr_b16 a[194:195], v20 offset:25088\n"
"s_waitcnt lgkmcnt(6)\n"
"v_mfma_f32_32x32x16_bf16 v[48:63], a[212:215], a[120:123], v[48:63]\n"
"ds_read_b64_tr_b16 a[196:197], v21 offset:24576\n"
"ds_read_b64_tr_b16 a[198:199], v21 offset:25088\n"
"s_waitcnt lgkmcnt(6)\n"
"v_mfma_f32_32x32x16_bf16 v[32:47], a[216:219], a[124:127], v[32:47]\n"
"ds_read_b64_tr_b16 a[200:201], v20 offset:26624\n"
"ds_read_b64_tr_b16 a[202:203], v20 offset:27136\n"
"s_waitcnt lgkmcnt(6)\n"
"v_mfma_f32_32x32x16_bf16 v[48:63], a[220:223], a[124:127], v[48:63]\n"
"ds_read_b64_tr_b16 a[204:205], v21 offset:26624\n"
"ds_read_b64_tr_b16 a[206:207], v21 offset:27136\n"
"v_add_u32 v20, 65536, v8\n"
"v_xor_b32 v21, 32, v20\n"
"v_xor_b32 v22, 64, v20\n"
"v_xor_b32 v23, 0x60, v20\n"
"s_waitcnt lgkmcnt(6)\n"
"v_mfma_f32_32x32x16_bf16 v[32:47], a[192:195], a[128:131], v[32:47]\n"
"ds_read_b128 a[208:211], v20 offset:0\n"
"s_waitcnt lgkmcnt(5)\n"
"v_mfma_f32_32x32x16_bf16 v[48:63], a[196:199], a[128:131], v[48:63]\n"
"ds_read_b128 a[212:215], v21 offset:0\n"
"s_add_u32 m0, s60, 20480\n"
"s_mov_b32 s87, 213376\n"
"buffer_load_dwordx4 v4, s[44:47], s87 offen nt lds\n"
"s_waitcnt lgkmcnt(4)\n"
"v_mfma_f32_32x32x16_bf16 v[32:47], a[200:203], a[132:135], v[32:47]\n"
"ds_read_b128 a[216:219], v22 offset:0\n"
"s_waitcnt lgkmcnt(3)\n"
"v_mfma_f32_32x32x16_bf16 v[48:63], a[204:207], a[132:135], v[48:63]\n"
"ds_read_b128 a[220:223], v23 offset:0\n"
"s_waitcnt lgkmcnt(3)\n"
"v_mfma_f32_32x32x16_bf16 v[128:143], a[208:211], v[64:67], v[128:143]\n"
"s_waitcnt vmcnt(20)\n"
"s_barrier\n"
"ds_read_b128 a[192:195], v20 offset:4096\n"
"s_waitcnt lgkmcnt(3)\n"
"v_mfma_f32_32x32x16_bf16 v[128:143], a[212:215], v[68:71], v[128:143]\n"
"ds_read_b128 a[196:199], v21 offset:4096\n"
"s_waitcnt lgkmcnt(3)\n"
"v_mfma_f32_32x32x16_bf16 v[128:143], a[216:219], v[72:75], v[128:143]\n"
"ds_read_b128 a[200:203], v22 offset:4096\n"
"s_waitcnt lgkmcnt(3)\n"
"v_mfma_f32_32x32x16_bf16 v[128:143], a[220:223], v[76:79], v[128:143]\n"
"ds_read_b128 a[204:207], v23 offset:4096\n"
"s_add_u32 m0, s60, 24576\n"
"s_mov_b32 s87, 229760\n"
"buffer_load_dwordx4 v4, s[44:47], s87 offen nt lds\n"
"s_waitcnt lgkmcnt(3)\n"
"v_mfma_f32_32x32x16_bf16 v[144:159], a[192:195], v[64:67], v[144:159]\n"
"ds_read_b128 a[208:211], v20 offset:8192\n"
"s_waitcnt lgkmcnt(3)\n"
"v_mfma_f32_32x32x16_bf16 v[144:159], a[196:199], v[68:71], v[144:159]\n"
"ds_read_b128 a[212:215], v21 offset:8192\n"
"s_waitcnt lgkmcnt(3)\n"
"v_mfma_f32_32x32x16_bf16 v[144:159], a[200:203], v[72:75], v[144:159]\n"
"ds_read_b128 a[216:219], v22 offset:8192\n"
"s_waitcnt lgkmcnt(3)\n"
"v_mfma_f32_32x32x16_bf16 v[144:159], a[204:207], v[76:79], v[144:159]\n"
"ds_read_b128 a[220:223], v23 offset:8192\n"
"s_waitcnt lgkmcnt(3)\n"
"v_mfma_f32_32x32x16_bf16 v[160:175], a[208:211], v[64:67], v[160:175]\n"
"ds_read_b128 a[192:195], v20 offset:12288\n"
"s_waitcnt lgkmcnt(3)\n"
"v_mfma_f32_32x32x16_bf16 v[160:175], a[212:215], v[68:71], v[160:175]\n"
"ds_read_b128 a[196:199], v21 offset:12288\n"
"s_add_u32 m0, s60, 28672\n"
"s_mov_b32 s87, 246144\n"
"buffer_load_dwordx4 v4, s[44:47], s87 offen nt lds\n"
"s_waitcnt lgkmcnt(3)\n"
"v_mfma_f32_32x32x16_bf16 v[160:175], a[216:219], v[72:75], v[160:175]\n"
"ds_read_b128 a[200:203], v22 offset:12288\n"
"s_waitcnt lgkmcnt(3)\n"
"v_mfma_f32_32x32x16_bf16 v[160:175], a[220:223], v[76:79], v[160:175]\n"
"ds_read_b128 a[204:207], v23 offset:12288\n"
"s_waitcnt lgkmcnt(3)\n"
"v_mfma_f32_32x32x16_bf16 v[176:191], a[192:195], v[64:67], v[176:191]\n"
"ds_read_b128 a[208:211], v20 offset:16384\n"
"s_waitcnt lgkmcnt(3)\n"
"v_mfma_f32_32x32x16_bf16 v[176:191], a[196:199], v[68:71], v[176:191]\n"
"ds_read_b128 a[212:215], v21 offset:16384\n"
"s_waitcnt lgkmcnt(3)\n"
"v_mfma_f32_32x32x16_bf16 v[176:191], a[200:203], v[72:75], v[176:191]\n"
"ds_read_b128 a[216:219], v22 offset:16384\n"
"s_waitcnt lgkmcnt(3)\n"
"v_mfma_f32_32x32x16_bf16 v[176:191], a[204:207], v[76:79], v[176:191]\n"
"ds_read_b128 a[220:223], v23 offset:16384\n"
"s_add_u32 m0, s60, 32768\n"
"s_mov_b32 s87, 0\n"
"buffer_load_dwordx4 v5, s[48:51], s87 offen nt lds\n"
"s_waitcnt lgkmcnt(3)\n"
"v_mfma_f32_32x32x16_bf16 v[192:207], a[208:211], v[64:67], v[192:207]\n"
"ds_read_b128 a[192:195], v20 offset:20480\n"
"s_waitcnt lgkmcnt(3)\n"
"v_mfma_f32_32x32x16_bf16 v[192:207], a[212:215], v[68:71], v[192:207]\n"
"ds_read_b128 a[196:199], v21 offset:20480\n"
"s_waitcnt lgkmcnt(3)\n"
"v_mfma_f32_32x32x16_bf16 v[192:207], a[216:219], v[72:75], v[192:207]\n"
"ds_read_b128 a[200:203], v22 offset:20480\n"
"s_waitcnt lgkmcnt(3)\n"
"v_mfma_f32_32x32x16_bf16 v[192:207], a[220:223], v[76:79], v[192:207]\n"
"ds_read_b128 a[204:207], v23 offset:20480\n"
"s_waitcnt lgkmcnt(3)\n"
"v_mfma_f32_32x32x16_bf16 v[208:223], a[192:195], v[64:67], v[208:223]\n"
"ds_read_b128 a[208:211], v20 offset:24576\n"
"s_waitcnt lgkmcnt(3)\n"
"v_mfma_f32_32x32x16_bf16 v[208:223], a[196:199], v[68:71], v[208:223]\n"
"ds_read_b128 a[212:215], v21 offset:24576\n"
"s_add_u32 m0, s60, 36864\n"
"s_mov_b32 s87, 128\n"
"buffer_load_dwordx4 v5, s[48:51], s87 offen nt lds\n"
"s_waitcnt lgkmcnt(3)\n"
"v_mfma_f32_32x32x16_bf16 v[208:223], a[200:203], v[72:75], v[208:223]\n"
"ds_read_b128 a[216:219], v22 offset:24576\n"
"s_waitcnt lgkmcnt(3)\n"
"v_mfma_f32_32x32x16_bf16 v[208:223], a[204:207], v[76:79], v[208:223]\n"
"ds_read_b128 a[220:223], v23 offset:24576\n"
"s_waitcnt lgkmcnt(3)\n"
"v_mfma_f32_32x32x16_bf16 v[224:239], a[208:211], v[64:67], v[224:239]\n"
"ds_read_b128 a[192:195], v20 offset:28672\n"
"s_waitcnt lgkmcnt(3)\n"
"v_mfma_f32_32x32x16_bf16 v[224:239], a[212:215], v[68:71], v[224:239]\n"
"ds_read_b128 a[196:199], v21 offset:28672\n"
"s_waitcnt lgkmcnt(3)\n"
"v_mfma_f32_32x32x16_bf16 v[224:239], a[216:219], v[72:75], v[224:239]\n"
"ds_read_b128 a[200:203], v22 offset:28672\n"
"s_waitcnt lgkmcnt(3)\n"
"v_mfma_f32_32x32x16_bf16 v[224:239], a[220:223], v[76:79], v[224:239]\n"
"ds_read_b128 a[204:207], v23 offset:28672\n"
"v_add_u32 v20, 98304, v7\n"
"v_xor_b32 v21, 64, v20\n"
"s_waitcnt lgkmcnt(3)\n"
"v_mfma_f32_32x32x16_bf16 v[240:255], a[192:195], v[64:67], v[240:255]\n"
"ds_read_b64_tr_b16 a[208:209], v20 offset:0\n"
"ds_read_b64_tr_b16 a[210:211], v20 offset:512\n"
"s_add_u32 m0, s60, 40960\n"
"s_mov_b32 s87, 256\n"
"buffer_load_dwordx4 v5, s[48:51], s87 offen nt lds\n"
"s_waitcnt lgkmcnt(4)\n"
"v_mfma_f32_32x32x16_bf16 v[240:255], a[196:199], v[68:71], v[240:255]\n"
"ds_read_b64_tr_b16 a[212:213], v21 offset:0\n"
"ds_read_b64_tr_b16 a[214:215], v21 offset:512\n"
"s_waitcnt lgkmcnt(5)\n"
"v_mfma_f32_32x32x16_bf16 v[240:255], a[200:203], v[72:75], v[240:255]\n"
"ds_read_b64_tr_b16 a[216:217], v20 offset:2048\n"
"ds_read_b64_tr_b16 a[218:219], v20 offset:2560\n"
"s_waitcnt lgkmcnt(6)\n"
"v_mfma_f32_32x32x16_bf16 v[240:255], a[204:207], v[76:79], v[240:255]\n"
"ds_read_b64_tr_b16 a[220:221], v21 offset:2048\n"
"ds_read_b64_tr_b16 a[222:223], v21 offset:2560\n"
"s_waitcnt lgkmcnt(6)\n"
"v_mfma_f32_32x32x16_bf16 v[32:47], a[208:211], a[136:139], v[32:47]\n"
"s_waitcnt vmcnt(18)\n"
"s_barrier\n"
"ds_read_b64_tr_b16 a[192:193], v20 offset:4096\n"
"ds_read_b64_tr_b16 a[194:195], v20 offset:4608\n"
"s_waitcnt lgkmcnt(6)\n"
"v_mfma_f32_32x32x16_bf16 v[48:63], a[212:215], a[136:139], v[48:63]\n"
"ds_read_b64_tr_b16 a[196:197], v21 offset:4096\n"
"ds_read_b64_tr_b16 a[198:199], v21 offset:4608\n"
"s_waitcnt lgkmcnt(6)\n"
"v_mfma_f32_32x32x16_bf16 v[32:47], a[216:219], a[140:143], v[32:47]\n"
"ds_read_b64_tr_b16 a[200:201], v20 offset:6144\n"
"ds_read_b64_tr_b16 a[202:203], v20 offset:6656\n"
"s_add_u32 m0, s60, 45056\n"
"s_mov_b32 s87, 384\n"
"buffer_load_dwordx4 v5, s[48:51], s87 offen nt lds\n"
"s_waitcnt lgkmcnt(6)\n"
"v_mfma_f32_32x32x16_bf16 v[48:63], a[220:223], a[140:143], v[48:63]\n"
"ds_read_b64_tr_b16 a[204:205], v21 offset:6144\n"
"ds_read_b64_tr_b16 a[206:207], v21 offset:6656\n"
"s_waitcnt lgkmcnt(6)\n"
"v_mfma_f32_32x32x16_bf16 v[32:47], a[192:195], a[144:147], v[32:47]\n"
"ds_read_b64_tr_b16 a[208:209], v20 offset:8192\n"
"ds_read_b64_tr_b16 a[210:211], v20 offset:8704\n"
"s_waitcnt lgkmcnt(6)\n"
"v_mfma_f32_32x32x16_bf16 v[48:63], a[196:199], a[144:147], v[48:63]\n"
"ds_read_b64_tr_b16 a[212:213], v21 offset:8192\n"
"ds_read_b64_tr_b16 a[214:215], v21 offset:8704\n"
"s_waitcnt lgkmcnt(6)\n"
"v_mfma_f32_32x32x16_bf16 v[32:47], a[200:203], a[148:151], v[32:47]\n"
"ds_read_b64_tr_b16 a[216:217], v20 offset:10240\n"
"ds_read_b64_tr_b16 a[218:219], v20 offset:10752\n"
"s_waitcnt lgkmcnt(6)\n"
"v_mfma_f32_32x32x16_bf16 v[48:63], a[204:207], a[148:151], v[48:63]\n"
"ds_read_b64_tr_b16 a[220:221], v21 offset:10240\n"
"ds_read_b64_tr_b16 a[222:223], v21 offset:10752\n"
"s_waitcnt lgkmcnt(6)\n"
"v_mfma_f32_32x32x16_bf16 v[32:47], a[208:211], a[152:155], v[32:47]\n"
"ds_read_b64_tr_b16 a[192:193], v20 offset:12288\n"
"ds_read_b64_tr_b16 a[194:195], v20 offset:12800\n"
"s_add_u32 m0, s60, 49152\n"
"s_mov_b32 s87, 16384\n"
"buffer_load_dwordx4 v5, s[48:51], s87 offen nt lds\n"
"s_waitcnt lgkmcnt(6)\n"
"v_mfma_f32_32x32x16_bf16 v[48:63], a[212:215], a[152:155], v[48:63]\n"
"ds_read_b64_tr_b16 a[196:197], v21 offset:12288\n"
"ds_read_b64_tr_b16 a[198:199], v21 offset:12800\n"
"s_waitcnt lgkmcnt(6)\n"
"v_mfma_f32_32x32x16_bf16 v[32:47], a[216:219], a[156:159], v[32:47]\n"
"ds_read_b64_tr_b16 a[200:201], v20 offset:14336\n"
"ds_read_b64_tr_b16 a[202:203], v20 offset:14848\n"
"s_waitcnt lgkmcnt(6)\n"
"v_mfma_f32_32x32x16_bf16 v[48:63], a[220:223], a[156:159], v[48:63]\n"
"ds_read_b64_tr_b16 a[204:205], v21 offset:14336\n"
"ds_read_b64_tr_b16 a[206:207], v21 offset:14848\n"
"s_waitcnt lgkmcnt(6)\n"
"v_mfma_f32_32x32x16_bf16 v[32:47], a[192:195], a[160:163], v[32:47]\n"
"ds_read_b64_tr_b16 a[208:209], v20 offset:16384\n"
"ds_read_b64_tr_b16 a[210:211], v20 offset:16896\n"
"s_waitcnt lgkmcnt(6)\n"
"v_mfma_f32_32x32x16_bf16 v[48:63], a[196:199], a[160:163], v[48:63]\n"
"ds_read_b64_tr_b16 a[212:213], v21 offset:16384\n"
"ds_read_b64_tr_b16 a[214:215], v21 offset:16896\n"
"s_waitcnt lgkmcnt(6)\n"
"v_mfma_f32_32x32x16_bf16 v[32:47], a[200:203], a[164:167], v[32:47]\n"
"ds_read_b64_tr_b16 a[216:217], v20 offset:18432\n"
"ds_read_b64_tr_b16 a[218:219], v20 offset:18944\n"
"s_add_u32 m0, s60, 53248\n"
"s_mov_b32 s87, 16512\n"
"buffer_load_dwordx4 v5, s[48:51], s87 offen nt lds\n"
"s_waitcnt lgkmcnt(6)\n"
"v_mfma_f32_32x32x16_bf16 v[48:63], a[204:207], a[164:167], v[48:63]\n"
"ds_read_b64_tr_b16 a[220:221], v21 offset:18432\n"
"ds_read_b64_tr_b16 a[222:223], v21 offset:18944\n"
"s_waitcnt lgkmcnt(6)\n"
"v_mfma_f32_32x32x16_bf16 v[32:47], a[208:211], a[168:171], v[32:47]\n"
"ds_read_b64_tr_b16 a[192:193], v20 offset:20480\n"
"ds_read_b64_tr_b16 a[194:195], v20 offset:20992\n"
"s_waitcnt lgkmcnt(6)\n"
"v_mfma_f32_32x32x16_bf16 v[48:63], a[212:215], a[168:171], v[48:63]\n"
"ds_read_b64_tr_b16 a[196:197], v21 offset:20480\n"
"ds_read_b64_tr_b16 a[198:199], v21 offset:20992\n"
"s_waitcnt lgkmcnt(6)\n"
"v_mfma_f32_32x32x16_bf16 v[32:47], a[216:219], a[172:175], v[32:47]\n"
"ds_read_b64_tr_b16 a[200:201], v20 offset:22528\n"
"ds_read_b64_tr_b16 a[202:203], v20 offset:23040\n"
"s_waitcnt lgkmcnt(6)\n"
"v_mfma_f32_32x32x16_bf16 v[48:63], a[220:223], a[172:175], v[48:63]\n"
"ds_read_b64_tr_b16 a[204:205], v21 offset:22528\n"
"ds_read_b64_tr_b16 a[206:207], v21 offset:23040\n"
"s_waitcnt lgkmcnt(6)\n"
"v_mfma_f32_32x32x16_bf16 v[32:47], a[192:195], a[176:179], v[32:47]\n"
"ds_read_b64_tr_b16 a[208:209], v20 offset:24576\n"
"ds_read_b64_tr_b16 a[210:211], v20 offset:25088\n"
"s_waitcnt lgkmcnt(6)\n"
"v_mfma_f32_32x32x16_bf16 v[48:63], a[196:199], a[176:179], v[48:63]\n"
"ds_read_b64_tr_b16 a[216:217], v20 offset:26624\n"
"ds_read_b64_tr_b16 a[218:219], v20 offset:27136\n"
"s_add_u32 m0, s60, 57344\n"
"s_mov_b32 s87, 16640\n"
"buffer_load_dwordx4 v5, s[48:51], s87 offen nt lds\n"
"s_waitcnt lgkmcnt(6)\n"
"v_mfma_f32_32x32x16_bf16 v[32:47], a[200:203], a[180:183], v[32:47]\n"
"ds_read_b64_tr_b16 a[212:213], v21 offset:24576\n"
"ds_read_b64_tr_b16 a[214:215], v21 offset:25088\n"
"s_waitcnt lgkmcnt(6)\n"
"v_mfma_f32_32x32x16_bf16 v[48:63], a[204:207], a[180:183], v[48:63]\n"
"ds_read_b64_tr_b16 a[220:221], v21 offset:26624\n"
"ds_read_b64_tr_b16 a[222:223], v21 offset:27136\n"
"v_add_u32 v20, 131072, v8\n"
"v_xor_b32 v21, 32, v20\n"
"v_xor_b32 v22, 64, v20\n"
"v_xor_b32 v23, 0x60, v20\n"
"s_waitcnt lgkmcnt(6)\n"
"v_mfma_f32_32x32x16_bf16 v[32:47], a[208:211], a[184:187], v[32:47]\n"
"ds_read_b128 a[192:195], v20 offset:0\n"
"s_waitcnt lgkmcnt(5)\n"
"v_mfma_f32_32x32x16_bf16 v[32:47], a[216:219], a[188:191], v[32:47]\n"
"ds_read_b128 a[196:199], v21 offset:0\n"
"s_waitcnt lgkmcnt(4)\n"
"v_mfma_f32_32x32x16_bf16 v[48:63], a[212:215], a[184:187], v[48:63]\n"
"ds_read_b128 a[200:203], v22 offset:0\n"
"s_waitcnt lgkmcnt(3)\n"
"v_mfma_f32_32x32x16_bf16 v[48:63], a[220:223], a[188:191], v[48:63]\n"
"ds_read_b128 a[204:207], v23 offset:0\n"
"s_nop 7\n"
"v_cvt_pk_bf16_f32 v64, v32, v33\n"
"v_cvt_pk_bf16_f32 v65, v34, v35\n"
"v_cvt_pk_bf16_f32 v66, v36, v37\n"
"v_cvt_pk_bf16_f32 v67, v38, v39\n"
"s_nop 1\n"
"s_add_u32 m0, s60, 61440\n"
"s_mov_b32 s87, 16768\n"
"buffer_load_dwordx4 v5, s[48:51], s87 offen nt lds\n"
"s_waitcnt lgkmcnt(3)\n"
"v_mfma_f32_32x32x16_bf16 v[80:95], a[192:195], v[64:67], v[80:95]\n"
"s_waitcnt vmcnt(15)\n"
"s_barrier\n"
"ds_read_b128 a[208:211], v20 offset:4096\n"
"v_cvt_pk_bf16_f32 v68, v40, v41\n"
"v_cvt_pk_bf16_f32 v69, v42, v43\n"
"v_cvt_pk_bf16_f32 v70, v44, v45\n"
"v_cvt_pk_bf16_f32 v71, v46, v47\n"
"s_nop 1\n"
"s_waitcnt lgkmcnt(3)\n"
"v_mfma_f32_32x32x16_bf16 v[80:95], a[196:199], v[68:71], v[80:95]\n"
"ds_read_b128 a[212:215], v21 offset:4096\n"
"v_cvt_pk_bf16_f32 v72, v48, v49\n"
"v_cvt_pk_bf16_f32 v73, v50, v51\n"
"v_cvt_pk_bf16_f32 v74, v52, v53\n"
"v_cvt_pk_bf16_f32 v75, v54, v55\n"
"s_nop 1\n"
"s_waitcnt lgkmcnt(3)\n"
"v_mfma_f32_32x32x16_bf16 v[80:95], a[200:203], v[72:75], v[80:95]\n"
"ds_read_b128 a[216:219], v22 offset:4096\n"
"v_cvt_pk_bf16_f32 v76, v56, v57\n"
"v_cvt_pk_bf16_f32 v77, v58, v59\n"
"v_cvt_pk_bf16_f32 v78, v60, v61\n"
"v_cvt_pk_bf16_f32 v79, v62, v63\n"
"s_nop 1\n"
"s_waitcnt lgkmcnt(3)\n"
"v_mfma_f32_32x32x16_bf16 v[80:95], a[204:207], v[76:79], v[80:95]\n"
"ds_read_b128 a[220:223], v23 offset:4096\n"
"s_waitcnt lgkmcnt(3)\n"
"v_mfma_f32_32x32x16_bf16 v[96:111], a[208:211], v[64:67], v[96:111]\n"
"ds_read_b128 a[192:195], v20 offset:8192\n"
"s_add_u32 m0, s60, 65536\n"
"s_mov_b32 s87, 32768\n"
"buffer_load_dwordx4 v5, s[48:51], s87 offen nt lds\n"
"s_waitcnt lgkmcnt(3)\n"
"v_mfma_f32_32x32x16_bf16 v[96:111], a[212:215], v[68:71], v[96:111]\n"
"ds_read_b128 a[196:199], v21 offset:8192\n"
"s_waitcnt lgkmcnt(3)\n"
"v_mfma_f32_32x32x16_bf16 v[96:111], a[216:219], v[72:75], v[96:111]\n"
"ds_read_b128 a[200:203], v22 offset:8192\n"
"s_waitcnt lgkmcnt(3)\n"
"v_mfma_f32_32x32x16_bf16 v[96:111], a[220:223], v[76:79], v[96:111]\n"
"ds_read_b128 a[204:207], v23 offset:8192\n"
"s_waitcnt lgkmcnt(3)\n"
"v_mfma_f32_32x32x16_bf16 v[112:127], a[192:195], v[64:67], v[112:127]\n"
"ds_read_b128 a[208:211], v20 offset:12288\n"
"v_max_f32 v24, v80, v81\n"
"v_max_f32 v25, v82, v83\n"
"s_waitcnt lgkmcnt(3)\n"
"v_mfma_f32_32x32x16_bf16 v[112:127], a[196:199], v[68:71], v[112:127]\n"
"ds_read_b128 a[212:215], v21 offset:12288\n"
"v_max3_f32 v24, v24, v84, v85\n"
"v_max3_f32 v25, v25, v86, v87\n"
"s_waitcnt lgkmcnt(3)\n"
"v_mfma_f32_32x32x16_bf16 v[112:127], a[200:203], v[72:75], v[112:127]\n"
"ds_read_b128 a[216:219], v22 offset:12288\n"
"v_max3_f32 v24, v24, v88, v89\n"
"v_max3_f32 v25, v25, v90, v91\n"
"s_add_u32 m0, s60, 69632\n"
"s_mov_b32 s87, 32896\n"
"buffer_load_dwordx4 v5, s[48:51], s87 offen nt lds\n"
"s_waitcnt lgkmcnt(3)\n"
"v_mfma_f32_32x32x16_bf16 v[112:127], a[204:207], v[76:79], v[112:127]\n"
"ds_read_b128 a[220:223], v23 offset:12288\n"
"v_max3_f32 v24, v24, v92, v93\n"
"v_max3_f32 v25, v25, v94, v95\n"
"s_waitcnt lgkmcnt(3)\n"
"v_mfma_f32_32x32x16_bf16 a[0:15], a[208:211], v[64:67], a[0:15]\n"
"ds_read_b128 a[192:195], v20 offset:16384\n"
"v_max3_f32 v24, v24, v96, v97\n"
"v_max3_f32 v25, v25, v98, v99\n"
"s_waitcnt lgkmcnt(3)\n"
"v_mfma_f32_32x32x16_bf16 a[0:15], a[212:215], v[68:71], a[0:15]\n"
"ds_read_b128 a[196:199], v21 offset:16384\n"
"v_max3_f32 v24, v24, v100, v101\n"
"v_max3_f32 v25, v25, v102, v103\n"
"s_waitcnt lgkmcnt(3)\n"
"v_mfma_f32_32x32x16_bf16 a[0:15], a[216:219], v[72:75], a[0:15]\n"
"ds_read_b128 a[200:203], v22 offset:16384\n"
"v_max3_f32 v24, v24, v104, v105\n"
"v_max3_f32 v25, v25, v106, v107\n"
"s_waitcnt lgkmcnt(3)\n"
"v_mfma_f32_32x32x16_bf16 a[0:15], a[220:223], v[76:79], a[0:15]\n"
"ds_read_b128 a[204:207], v23 offset:16384\n"
"v_max3_f32 v24, v24, v108, v109\n"
"v_max3_f32 v25, v25, v110, v111\n"
"s_waitcnt lgkmcnt(3)\n"
"v_mfma_f32_32x32x16_bf16 a[16:31], a[192:195], v[64:67], a[16:31]\n"
"ds_read_b128 a[208:211], v20 offset:20480\n"
"v_max3_f32 v24, v24, v112, v113\n"
"v_max3_f32 v25, v25, v114, v115\n"
"s_add_u32 m0, s60, 73728\n"
"s_mov_b32 s87, 33024\n"
"buffer_load_dwordx4 v5, s[48:51], s87 offen nt lds\n"
"s_waitcnt lgkmcnt(3)\n"
"v_mfma_f32_32x32x16_bf16 a[16:31], a[196:199], v[68:71], a[16:31]\n"
"ds_read_b128 a[212:215], v21 offset:20480\n"
"v_max3_f32 v24, v24, v116, v117\n"
"v_max3_f32 v25, v25, v118, v119\n"
"s_waitcnt lgkmcnt(3)\n"
"v_mfma_f32_32x32x16_bf16 a[16:31], a[200:203], v[72:75], a[16:31]\n"
"ds_read_b128 a[216:219], v22 offset:20480\n"
"v_max3_f32 v24, v24, v120, v121\n"
"v_max3_f32 v25, v25, v122, v123\n"
"s_waitcnt lgkmcnt(3)\n"
"v_mfma_f32_32x32x16_bf16 a[16:31], a[204:207], v[76:79], a[16:31]\n"
"ds_read_b128 a[220:223], v23 offset:20480\n"
"v_max3_f32 v24, v24, v124, v125\n"
"v_max3_f32 v25, v25, v126, v127\n"
"s_waitcnt lgkmcnt(3)\n"
"v_mfma_f32_32x32x16_bf16 a[32:47], a[208:211], v[64:67], a[32:47]\n"
"ds_read_b128 a[192:195], v20 offset:24576\n"
"v_accvgpr_read_b32 v32, a0\n"
"v_accvgpr_read_b32 v33, a1\n"
"v_accvgpr_read_b32 v34, a2\n"
"v_accvgpr_read_b32 v35, a3\n"
"v_accvgpr_read_b32 v36, a4\n"
"v_accvgpr_read_b32 v37, a5\n"
"s_waitcnt lgkmcnt(3)\n"
"v_mfma_f32_32x32x16_bf16 a[32:47], a[212:215], v[68:71], a[32:47]\n"
"ds_read_b128 a[196:199], v21 offset:24576\n"
"v_accvgpr_read_b32 v38, a6\n"
"v_accvgpr_read_b32 v39, a7\n"
"v_accvgpr_read_b32 v40, a8\n"
"v_accvgpr_read_b32 v41, a9\n"
"v_accvgpr_read_b32 v42, a10\n"
"v_accvgpr_read_b32 v43, a11\n"
"s_add_u32 m0, s60, 77824\n"
"s_mov_b32 s87, 33152\n"
"buffer_load_dwordx4 v5, s[48:51], s87 offen nt lds\n"
"s_waitcnt lgkmcnt(3)\n"
"v_mfma_f32_32x32x16_bf16 a[32:47], a[216:219], v[72:75], a[32:47]\n"
"ds_read_b128 a[200:203], v22 offset:24576\n"
"v_accvgpr_read_b32 v44, a12\n"
"v_accvgpr_read_b32 v45, a13\n"
"v_accvgpr_read_b32 v46, a14\n"
"v_accvgpr_read_b32 v47, a15\n"
"v_max3_f32 v24, v24, v32, v33\n"
"v_max3_f32 v25, v25, v34, v35\n"
"s_waitcnt lgkmcnt(3)\n"
"v_mfma_f32_32x32x16_bf16 a[32:47], a[220:223], v[76:79], a[32:47]\n"
"ds_read_b128 a[204:207], v23 offset:24576\n"
"v_max3_f32 v24, v24, v36, v37\n"
"v_max3_f32 v25, v25, v38, v39\n"
"v_max3_f32 v24, v24, v40, v41\n"
"v_max3_f32 v25, v25, v42, v43\n"
"v_max3_f32 v24, v24, v44, v45\n"
"v_max3_f32 v25, v25, v46, v47\n"
"s_waitcnt lgkmcnt(3)\n"
"v_mfma_f32_32x32x16_bf16 a[48:63], a[192:195], v[64:67], a[48:63]\n"
"ds_read_b128 a[208:211], v20 offset:28672\n"
"v_accvgpr_read_b32 v48, a16\n"
"v_accvgpr_read_b32 v49, a17\n"
"v_accvgpr_read_b32 v50, a18\n"
"v_accvgpr_read_b32 v51, a19\n"
"v_accvgpr_read_b32 v52, a20\n"
"v_accvgpr_read_b32 v53, a21\n"
"s_waitcnt lgkmcnt(3)\n"
"v_mfma_f32_32x32x16_bf16 a[48:63], a[196:199], v[68:71], a[48:63]\n"
"ds_read_b128 a[212:215], v21 offset:28672\n"
"v_accvgpr_read_b32 v54, a22\n"
"v_accvgpr_read_b32 v55, a23\n"
"v_accvgpr_read_b32 v56, a24\n"
"v_accvgpr_read_b32 v57, a25\n"
"v_accvgpr_read_b32 v58, a26\n"
"v_accvgpr_read_b32 v59, a27\n"
"s_waitcnt lgkmcnt(3)\n"
"v_mfma_f32_32x32x16_bf16 a[48:63], a[200:203], v[72:75], a[48:63]\n"
"ds_read_b128 a[216:219], v22 offset:28672\n"
"v_accvgpr_read_b32 v60, a28\n"
"v_accvgpr_read_b32 v61, a29\n"
"v_accvgpr_read_b32 v62, a30\n"
"v_accvgpr_read_b32 v63, a31\n"
"v_max3_f32 v24, v24, v48, v49\n"
"v_max3_f32 v25, v25, v50, v51\n"
"s_add_u32 m0, s60, 81920\n"
"s_mov_b32 s87, 49152\n"
"buffer_load_dwordx4 v5, s[48:51], s87 offen nt lds\n"
"s_waitcnt lgkmcnt(3)\n"
"v_mfma_f32_32x32x16_bf16 a[48:63], a[204:207], v[76:79], a[48:63]\n"
"ds_read_b128 a[220:223], v23 offset:28672\n"
"v_mov_b32 v20, v8\n"
"v_xor_b32 v21, 32, v20\n"
"v_xor_b32 v22, 64, v20\n"
"v_xor_b32 v23, 0x60, v20\n"
"v_max3_f32 v24, v24, v52, v53\n"
"v_max3_f32 v25, v25, v54, v55\n"
"v_max3_f32 v24, v24, v56, v57\n"
"v_max3_f32 v25, v25, v58, v59\n"
"v_max3_f32 v24, v24, v60, v61\n"
"v_max3_f32 v25, v25, v62, v63\n"
"s_waitcnt lgkmcnt(3)\n"
"v_mfma_f32_32x32x16_bf16 a[64:79], a[208:211], v[64:67], a[64:79]\n"
"ds_read_b128 a[192:195], v20 offset:0\n"
"v_accvgpr_read_b32 v28, a32\n"
"v_accvgpr_read_b32 v29, a33\n"
"v_accvgpr_read_b32 v30, a34\n"
"v_accvgpr_read_b32 v31, a35\n"
"v_max3_f32 v24, v24, v28, v29\n"
"v_max3_f32 v25, v25, v30, v31\n"
"s_waitcnt lgkmcnt(3)\n"
"v_mfma_f32_32x32x16_bf16 a[64:79], a[212:215], v[68:71], a[64:79]\n"
"ds_read_b128 a[196:199], v21 offset:0\n"
"v_accvgpr_read_b32 v28, a36\n"
"v_accvgpr_read_b32 v29, a37\n"
"v_accvgpr_read_b32 v30, a38\n"
"v_accvgpr_read_b32 v31, a39\n"
"v_max3_f32 v24, v24, v28, v29\n"
"v_max3_f32 v25, v25, v30, v31\n"
"s_waitcnt lgkmcnt(3)\n"
"v_mfma_f32_32x32x16_bf16 a[64:79], a[216:219], v[72:75], a[64:79]\n"
"ds_read_b128 a[200:203], v22 offset:0\n"
"v_accvgpr_read_b32 v28, a40\n"
"v_accvgpr_read_b32 v29, a41\n"
"v_accvgpr_read_b32 v30, a42\n"
"v_accvgpr_read_b32 v31, a43\n"
"v_max3_f32 v24, v24, v28, v29\n"
"v_max3_f32 v25, v25, v30, v31\n"
"s_add_u32 m0, s60, 86016\n"
"s_mov_b32 s87, 49280\n"
"buffer_load_dwordx4 v5, s[48:51], s87 offen nt lds\n"
"s_waitcnt lgkmcnt(3)\n"
"v_mfma_f32_32x32x16_bf16 a[64:79], a[220:223], v[76:79], a[64:79]\n"
"ds_read_b128 a[204:207], v23 offset:0\n"
"v_accvgpr_read_b32 v28, a44\n"
"v_accvgpr_read_b32 v29, a45\n"
"v_accvgpr_read_b32 v30, a46\n"
"v_accvgpr_read_b32 v31, a47\n"
"v_max3_f32 v24, v24, v28, v29\n"
"v_max3_f32 v25, v25, v30, v31\n"
"s_waitcnt lgkmcnt(3)\n"
"v_mfma_f32_32x32x16_bf16 v[128:143], a[192:195], v[64:67], v[128:143]\n"
"s_waitcnt vmcnt(13)\n"
"s_barrier\n"
"ds_read_b128 a[208:211], v20 offset:4096\n"
"v_accvgpr_read_b32 v28, a48\n"
"v_accvgpr_read_b32 v29, a49\n"
"v_accvgpr_read_b32 v30, a50\n"
"v_accvgpr_read_b32 v31, a51\n"
"v_max3_f32 v24, v24, v28, v29\n"
"v_max3_f32 v25, v25, v30, v31\n"
"s_waitcnt lgkmcnt(3)\n"
"v_mfma_f32_32x32x16_bf16 v[128:143], a[196:199], v[68:71], v[128:143]\n"
"ds_read_b128 a[212:215], v21 offset:4096\n"
"v_accvgpr_read_b32 v28, a52\n"
"v_accvgpr_read_b32 v29, a53\n"
"v_accvgpr_read_b32 v30, a54\n"
"v_accvgpr_read_b32 v31, a55\n"
"v_max3_f32 v24, v24, v28, v29\n"
"v_max3_f32 v25, v25, v30, v31\n"
"s_waitcnt lgkmcnt(3)\n"
"v_mfma_f32_32x32x16_bf16 v[128:143], a[200:203], v[72:75], v[128:143]\n"
"ds_read_b128 a[216:219], v22 offset:4096\n"
"v_accvgpr_read_b32 v28, a56\n"
"v_accvgpr_read_b32 v29, a57\n"
"v_accvgpr_read_b32 v30, a58\n"
"v_accvgpr_read_b32 v31, a59\n"
"v_max3_f32 v24, v24, v28, v29\n"
"v_max3_f32 v25, v25, v30, v31\n"
"s_waitcnt lgkmcnt(3)\n"
"v_mfma_f32_32x32x16_bf16 v[128:143], a[204:207], v[76:79], v[128:143]\n"
"ds_read_b128 a[220:223], v23 offset:4096\n"
"v_accvgpr_read_b32 v28, a60\n"
"v_accvgpr_read_b32 v29, a61\n"
"v_accvgpr_read_b32 v30, a62\n"
"v_accvgpr_read_b32 v31, a63\n"
"v_max3_f32 v24, v24, v28, v29\n"
"v_max3_f32 v25, v25, v30, v31\n"
"s_add_u32 m0, s60, 90112\n"
"s_mov_b32 s87, 49408\n"
"buffer_load_dwordx4 v5, s[48:51], s87 offen nt lds\n"
"s_waitcnt lgkmcnt(3)\n"
"v_mfma_f32_32x32x16_bf16 v[144:159], a[208:211], v[64:67], v[144:159]\n"
"ds_read_b128 a[192:195], v20 offset:8192\n"
"v_accvgpr_read_b32 v28, a64\n"
"v_accvgpr_read_b32 v29, a65\n"
"v_accvgpr_read_b32 v30, a66\n"
"v_accvgpr_read_b32 v31, a67\n"
"v_max3_f32 v24, v24, v28, v29\n"
"v_max3_f32 v25, v25, v30, v31\n"
"s_waitcnt lgkmcnt(3)\n"
"v_mfma_f32_32x32x16_bf16 v[144:159], a[212:215], v[68:71], v[144:159]\n"
"ds_read_b128 a[196:199], v21 offset:8192\n"
"v_accvgpr_read_b32 v28, a68\n"
"v_accvgpr_read_b32 v29, a69\n"
"v_accvgpr_read_b32 v30, a70\n"
"v_accvgpr_read_b32 v31, a71\n"
"v_max3_f32 v24, v24, v28, v29\n"
"v_max3_f32 v25, v25, v30, v31\n"
"s_waitcnt lgkmcnt(3)\n"
"v_mfma_f32_32x32x16_bf16 v[144:159], a[216:219], v[72:75], v[144:159]\n"
"ds_read_b128 a[200:203], v22 offset:8192\n"
"v_accvgpr_read_b32 v28, a72\n"
"v_accvgpr_read_b32 v29, a73\n"
"v_accvgpr_read_b32 v30, a74\n"
"v_accvgpr_read_b32 v31, a75\n"
"v_max3_f32 v24, v24, v28, v29\n"
"v_max3_f32 v25, v25, v30, v31\n"
"s_waitcnt lgkmcnt(3)\n"
"v_mfma_f32_32x32x16_bf16 v[144:159], a[220:223], v[76:79], v[144:159]\n"
"ds_read_b128 a[204:207], v23 offset:8192\n"
"v_accvgpr_read_b32 v28, a76\n"
"v_accvgpr_read_b32 v29, a77\n"
"v_accvgpr_read_b32 v30, a78\n"
"v_accvgpr_read_b32 v31, a79\n"
"v_max3_f32 v24, v24, v28, v29\n"
"v_max3_f32 v25, v25, v30, v31\n"
"s_add_u32 m0, s60, 94208\n"
"s_mov_b32 s87, 49536\n"
"buffer_load_dwordx4 v5, s[48:51], s87 offen nt lds\n"
"s_waitcnt lgkmcnt(3)\n"
"v_mfma_f32_32x32x16_bf16 v[160:175], a[192:195], v[64:67], v[160:175]\n"
"ds_read_b128 a[208:211], v20 offset:12288\n"
"v_max3_f32 v24, v24, v128, v129\n"
"v_max3_f32 v25, v25, v130, v131\n"
"s_waitcnt lgkmcnt(3)\n"
"v_mfma_f32_32x32x16_bf16 v[160:175], a[196:199], v[68:71], v[160:175]\n"
"ds_read_b128 a[212:215], v21 offset:12288\n"
"v_max3_f32 v24, v24, v132, v133\n"
"v_max3_f32 v25, v25, v134, v135\n"
"s_waitcnt lgkmcnt(3)\n"
"v_mfma_f32_32x32x16_bf16 v[160:175], a[200:203], v[72:75], v[160:175]\n"
"ds_read_b128 a[216:219], v22 offset:12288\n"
"v_max3_f32 v24, v24, v136, v137\n"
"v_max3_f32 v25, v25, v138, v139\n"
"s_waitcnt lgkmcnt(3)\n"
"v_mfma_f32_32x32x16_bf16 v[160:175], a[204:207], v[76:79], v[160:175]\n"
"ds_read_b128 a[220:223], v23 offset:12288\n"
"v_max3_f32 v24, v24, v140, v141\n"
"v_max3_f32 v25, v25, v142, v143\n"
"s_waitcnt lgkmcnt(3)\n"
"v_mfma_f32_32x32x16_bf16 v[176:191], a[208:211], v[64:67], v[176:191]\n"
"ds_read_b128 a[192:195], v20 offset:16384\n"
"v_max3_f32 v24, v24, v144, v145\n"
"v_max3_f32 v25, v25, v146, v147\n"
"s_waitcnt lgkmcnt(3)\n"
"v_mfma_f32_32x32x16_bf16 v[176:191], a[212:215], v[68:71], v[176:191]\n"
"ds_read_b128 a[196:199], v21 offset:16384\n"
"v_max3_f32 v24, v24, v148, v149\n"
"v_max3_f32 v25, v25, v150, v151\n"
"s_add_u32 m0, s60, 98304\n"
"s_mov_b32 s87, 65536\n"
"buffer_load_dwordx4 v5, s[48:51], s87 offen nt lds\n"
"s_waitcnt lgkmcnt(3)\n"
"v_mfma_f32_32x32x16_bf16 v[176:191], a[216:219], v[72:75], v[176:191]\n"
"ds_read_b128 a[200:203], v22 offset:16384\n"
"v_max3_f32 v24, v24, v152, v153\n"
"v_max3_f32 v25, v25, v154, v155\n"
"s_waitcnt lgkmcnt(3)\n"
"v_mfma_f32_32x32x16_bf16 v[176:191], a[220:223], v[76:79], v[176:191]\n"
"ds_read_b128 a[204:207], v23 offset:16384\n"
"v_max3_f32 v24, v24, v156, v157\n"
"v_max3_f32 v25, v25, v158, v159\n"
"s_waitcnt lgkmcnt(3)\n"
"v_mfma_f32_32x32x16_bf16 v[192:207], a[192:195], v[64:67], v[192:207]\n"
"ds_read_b128 a[208:211], v20 offset:20480\n"
"v_max3_f32 v24, v24, v160, v161\n"
"v_max3_f32 v25, v25, v162, v163\n"
"s_waitcnt lgkmcnt(3)\n"
"v_mfma_f32_32x32x16_bf16 v[192:207], a[196:199], v[68:71], v[192:207]\n"
"ds_read_b128 a[212:215], v21 offset:20480\n"
"v_max3_f32 v24, v24, v164, v165\n"
"v_max3_f32 v25, v25, v166, v167\n"
"s_waitcnt lgkmcnt(3)\n"
"v_mfma_f32_32x32x16_bf16 v[192:207], a[200:203], v[72:75], v[192:207]\n"
"ds_read_b128 a[216:219], v22 offset:20480\n"
"v_max3_f32 v24, v24, v168, v169\n"
"v_max3_f32 v25, v25, v170, v171\n"
"s_waitcnt lgkmcnt(3)\n"
"v_mfma_f32_32x32x16_bf16 v[192:207], a[204:207], v[76:79], v[192:207]\n"
"ds_read_b128 a[220:223], v23 offset:20480\n"
"v_max3_f32 v24, v24, v172, v173\n"
"v_max3_f32 v25, v25, v174, v175\n"
"s_add_u32 m0, s60, 102400\n"
"s_mov_b32 s87, 65664\n"
"buffer_load_dwordx4 v5, s[48:51], s87 offen nt lds\n"
"s_waitcnt lgkmcnt(3)\n"
"v_mfma_f32_32x32x16_bf16 v[208:223], a[208:211], v[64:67], v[208:223]\n"
"ds_read_b128 a[192:195], v20 offset:24576\n"
"v_max3_f32 v24, v24, v176, v177\n"
"v_max3_f32 v25, v25, v178, v179\n"
"s_waitcnt lgkmcnt(3)\n"
"v_mfma_f32_32x32x16_bf16 v[208:223], a[212:215], v[68:71], v[208:223]\n"
"ds_read_b128 a[196:199], v21 offset:24576\n"
"v_max3_f32 v24, v24, v180, v181\n"
"v_max3_f32 v25, v25, v182, v183\n"
"s_waitcnt lgkmcnt(3)\n"
"v_mfma_f32_32x32x16_bf16 v[208:223], a[216:219], v[72:75], v[208:223]\n"
"ds_read_b128 a[200:203], v22 offset:24576\n"
"v_max3_f32 v24, v24, v184, v185\n"
"v_max3_f32 v25, v25, v186, v187\n"
"s_waitcnt lgkmcnt(3)\n"
"v_mfma_f32_32x32x16_bf16 v[208:223], a[220:223], v[76:79], v[208:223]\n"
"ds_read_b128 a[204:207], v23 offset:24576\n"
"v_max3_f32 v24, v24, v188, v189\n"
"v_max3_f32 v25, v25, v190, v191\n"
"s_waitcnt lgkmcnt(3)\n"
"v_mfma_f32_32x32x16_bf16 v[224:239], a[192:195], v[64:67], v[224:239]\n"
"ds_read_b128 a[208:211], v20 offset:28672\n"
"v_max3_f32 v24, v24, v192, v193\n"
"v_max3_f32 v25, v25, v194, v195\n"
"s_waitcnt lgkmcnt(3)\n"
"v_mfma_f32_32x32x16_bf16 v[224:239], a[196:199], v[68:71], v[224:239]\n"
"ds_read_b128 a[212:215], v21 offset:28672\n"
"v_max3_f32 v24, v24, v196, v197\n"
"v_max3_f32 v25, v25, v198, v199\n"
"s_waitcnt lgkmcnt(3)\n"
"v_mfma_f32_32x32x16_bf16 v[224:239], a[200:203], v[72:75], v[224:239]\n"
"ds_read_b128 a[216:219], v22 offset:28672\n"
"v_max3_f32 v24, v24, v200, v201\n"
"v_max3_f32 v25, v25, v202, v203\n"
"s_add_u32 m0, s60, 106496\n"
"s_mov_b32 s87, 65792\n"
"buffer_load_dwordx4 v5, s[48:51], s87 offen nt lds\n"
"s_waitcnt lgkmcnt(3)\n"
"v_mfma_f32_32x32x16_bf16 v[224:239], a[204:207], v[76:79], v[224:239]\n"
"ds_read_b128 a[220:223], v23 offset:28672\n"
"v_add_u32 v20, 32768, v7\n"
"v_xor_b32 v21, 64, v20\n"
"v_max3_f32 v24, v24, v204, v205\n"
"v_max3_f32 v25, v25, v206, v207\n"
"s_waitcnt lgkmcnt(3)\n"
"v_mfma_f32_32x32x16_bf16 v[240:255], a[208:211], v[64:67], v[240:255]\n"
"ds_read_b64_tr_b16 a[192:193], v20 offset:0\n"
"ds_read_b64_tr_b16 a[194:195], v20 offset:512\n"
"v_max3_f32 v24, v24, v208, v209\n"
"v_max3_f32 v25, v25, v210, v211\n"
"s_waitcnt lgkmcnt(4)\n"
"v_mfma_f32_32x32x16_bf16 v[240:255], a[212:215], v[68:71], v[240:255]\n"
"ds_read_b64_tr_b16 a[196:197], v21 offset:0\n"
"ds_read_b64_tr_b16 a[198:199], v21 offset:512\n"
"v_max3_f32 v24, v24, v212, v213\n"
"v_max3_f32 v25, v25, v214, v215\n"
"s_waitcnt lgkmcnt(5)\n"
"v_mfma_f32_32x32x16_bf16 v[240:255], a[216:219], v[72:75], v[240:255]\n"
"ds_read_b64_tr_b16 a[200:201], v20 offset:2048\n"
"ds_read_b64_tr_b16 a[202:203], v20 offset:2560\n"
"v_max3_f32 v24, v24, v216, v217\n"
"v_max3_f32 v25, v25, v218, v219\n"
"s_waitcnt lgkmcnt(6)\n"
"v_mfma_f32_32x32x16_bf16 v[240:255], a[220:223], v[76:79], v[240:255]\n"
"ds_read_b64_tr_b16 a[204:205], v21 offset:2048\n"
"ds_read_b64_tr_b16 a[206:207], v21 offset:2560\n"
"v_max3_f32 v24, v24, v220, v221\n"
"v_max3_f32 v25, v25, v222, v223\n"
"v_max3_f32 v24, v24, v224, v225\n"
"v_max3_f32 v25, v25, v226, v227\n"
"v_max3_f32 v24, v24, v228, v229\n"
"v_max3_f32 v25, v25, v230, v231\n"
"v_max3_f32 v24, v24, v232, v233\n"
"v_max3_f32 v25, v25, v234, v235\n"
"v_max3_f32 v24, v24, v236, v237\n"
"v_max3_f32 v25, v25, v238, v239\n"
"s_nop 3\n"
"v_max3_f32 v24, v24, v240, v241\n"
"v_max3_f32 v25, v25, v242, v243\n"
"v_max3_f32 v24, v24, v244, v245\n"
"v_max3_f32 v25, v25, v246, v247\n"
"v_max3_f32 v24, v24, v248, v249\n"
"v_max3_f32 v25, v25, v250, v251\n"
"v_max3_f32 v24, v24, v252, v253\n"
"v_max3_f32 v25, v25, v254, v255\n"
"v_max_f32 v24, v24, v25\n"
"v_mov_b32 v28, v24\n"
"s_nop 1\n"
"v_permlane32_swap_b32 v24, v28\n"
"s_nop 1\n"
"v_max_f32 v24, v24, v28\n"
"v_mul_f32 v2, s88, v24\n"
"v_mul_f32 v3, s88, v24\n"
"v_mov_b32 v8, 0\n"
"v_mov_b32 v9, 0\n"
"v_mov_b32 v10, 0\n"
"v_mov_b32 v11, 0\n"
"v_pk_fma_f32 v[80:81], v[80:81], s[88:89], v[2:3] neg_lo:[0,0,1] neg_hi:[0,0,1]\n"
"v_pk_fma_f32 v[82:83], v[82:83], s[88:89], v[2:3] neg_lo:[0,0,1] neg_hi:[0,0,1]\n"
"v_pk_fma_f32 v[84:85], v[84:85], s[88:89], v[2:3] neg_lo:[0,0,1] neg_hi:[0,0,1]\n"
"v_pk_fma_f32 v[86:87], v[86:87], s[88:89], v[2:3] neg_lo:[0,0,1] neg_hi:[0,0,1]\n"
"v_pk_fma_f32 v[88:89], v[88:89], s[88:89], v[2:3] neg_lo:[0,0,1] neg_hi:[0,0,1]\n"
"v_pk_fma_f32 v[90:91], v[90:91], s[88:89], v[2:3] neg_lo:[0,0,1] neg_hi:[0,0,1]\n"
"v_pk_fma_f32 v[92:93], v[92:93], s[88:89], v[2:3] neg_lo:[0,0,1] neg_hi:[0,0,1]\n"
"v_pk_fma_f32 v[94:95], v[94:95], s[88:89], v[2:3] neg_lo:[0,0,1] neg_hi:[0,0,1]\n"
"v_exp_f32 v80, v80\n"
"v_exp_f32 v81, v81\n"
"v_exp_f32 v82, v82\n"
"v_exp_f32 v83, v83\n"
"v_exp_f32 v84, v84\n"
"v_exp_f32 v85, v85\n"
"v_exp_f32 v86, v86\n"
"v_exp_f32 v87, v87\n"
"v_exp_f32 v88, v88\n"
"v_exp_f32 v89, v89\n"
"v_exp_f32 v90, v90\n"
"v_exp_f32 v91, v91\n"
"v_exp_f32 v92, v92\n"
"v_exp_f32 v93, v93\n"
"v_exp_f32 v94, v94\n"
"v_exp_f32 v95, v95\n"
"v_pk_add_f32 v[8:9], v[8:9], v[80:81]\n"
"v_pk_add_f32 v[10:11], v[10:11], v[82:83]\n"
"v_pk_add_f32 v[8:9], v[8:9], v[84:85]\n"
"v_pk_add_f32 v[10:11], v[10:11], v[86:87]\n"
"v_pk_add_f32 v[8:9], v[8:9], v[88:89]\n"
"v_pk_add_f32 v[10:11], v[10:11], v[90:91]\n"
"v_pk_add_f32 v[8:9], v[8:9], v[92:93]\n"
"v_pk_add_f32 v[10:11], v[10:11], v[94:95]\n"
"v_cvt_pk_bf16_f32 v80, v80, v81\n"
"v_cvt_pk_bf16_f32 v81, v82, v83\n"
"v_cvt_pk_bf16_f32 v82, v84, v85\n"
"v_cvt_pk_bf16_f32 v83, v86, v87\n"
"v_cvt_pk_bf16_f32 v84, v88, v89\n"
"v_cvt_pk_bf16_f32 v85, v90, v91\n"
"v_cvt_pk_bf16_f32 v86, v92, v93\n"
"v_cvt_pk_bf16_f32 v87, v94, v95\n"
"s_nop 1\n"
"s_waitcnt lgkmcnt(6)\n"
"v_mfma_f32_32x32x16_bf16 a[80:95], a[192:195], v[80:83], 0\n"
"s_waitcnt vmcnt(10)\n"
"s_barrier\n"
"ds_read_b64_tr_b16 a[208:209], v20 offset:4096\n"
"ds_read_b64_tr_b16 a[210:211], v20 offset:4608\n"
"v_fma_f32 v96, v96, s88, -v2\n"
"v_fma_f32 v97, v97, s88, -v2\n"
"v_exp_f32 v96, v96\n"
"v_fma_f32 v98, v98, s88, -v2\n"
"v_exp_f32 v97, v97\n"
"s_add_u32 m0, s60, 110592\n"
"s_mov_b32 s87, 65920\n"
"buffer_load_dwordx4 v5, s[48:51], s87 offen nt lds\n"
"s_waitcnt lgkmcnt(6)\n"
"v_mfma_f32_32x32x16_bf16 a[96:111], a[196:199], v[80:83], 0\n"
"ds_read_b64_tr_b16 a[212:213], v21 offset:4096\n"
"ds_read_b64_tr_b16 a[214:215], v21 offset:4608\n"
"v_add_f32 v8, v8, v96\n"
"v_fma_f32 v99, v99, s88, -v2\n"
"v_exp_f32 v98, v98\n"
"v_add_f32 v9, v9, v97\n"
"v_cvt_pk_bf16_f32 v96, v96, v97\n"
"s_waitcnt lgkmcnt(6)\n"
"v_mfma_f32_32x32x16_bf16 a[80:95], a[200:203], v[84:87], a[80:95]\n"
"ds_read_b64_tr_b16 a[216:217], v20 offset:6144\n"
"ds_read_b64_tr_b16 a[218:219], v20 offset:6656\n"
"v_fma_f32 v100, v100, s88, -v2\n"
"v_exp_f32 v99, v99\n"
"v_add_f32 v10, v10, v98\n"
"v_fma_f32 v101, v101, s88, -v2\n"
"v_exp_f32 v100, v100\n"
"s_waitcnt lgkmcnt(6)\n"
"v_mfma_f32_32x32x16_bf16 a[96:111], a[204:207], v[84:87], a[96:111]\n"
"ds_read_b64_tr_b16 a[220:221], v21 offset:6144\n"
"ds_read_b64_tr_b16 a[222:223], v21 offset:6656\n"
"v_add_f32 v11, v11, v99\n"
"v_cvt_pk_bf16_f32 v97, v98, v99\n"
"v_fma_f32 v102, v102, s88, -v2\n"
"v_exp_f32 v101, v101\n"
"v_add_f32 v8, v8, v100\n"
"v_fma_f32 v103, v103, s88, -v2\n"
"s_waitcnt lgkmcnt(6)\n"
"v_mfma_f32_32x32x16_bf16 a[112:127], a[208:211], v[80:83], 0\n"
"ds_read_b64_tr_b16 a[192:193], v20 offset:8192\n"
"ds_read_b64_tr_b16 a[194:195], v20 offset:8704\n"
"v_exp_f32 v102, v102\n"
"v_add_f32 v9, v9, v101\n"
"v_cvt_pk_bf16_f32 v98, v100, v101\n"
"v_fma_f32 v104, v104, s88, -v2\n"
"v_exp_f32 v103, v103\n"
"s_add_u32 m0, s60, 114688\n"
"s_mov_b32 s87, 81920\n"
"buffer_load_dwordx4 v5, s[48:51], s87 offen nt lds\n"
"s_waitcnt lgkmcnt(6)\n"
"v_mfma_f32_32x32x16_bf16 a[128:143], a[212:215], v[80:83], 0\n"
"ds_read_b64_tr_b16 a[196:197], v21 offset:8192\n"
"ds_read_b64_tr_b16 a[198:199], v21 offset:8704\n"
"v_add_f32 v10, v10, v102\n"
"v_fma_f32 v105, v105, s88, -v2\n"
"v_exp_f32 v104, v104\n"
"v_add_f32 v11, v11, v103\n"
"v_cvt_pk_bf16_f32 v99, v102, v103\n"
"s_waitcnt lgkmcnt(6)\n"
"v_mfma_f32_32x32x16_bf16 a[112:127], a[216:219], v[84:87], a[112:127]\n"
"ds_read_b64_tr_b16 a[200:201], v20 offset:10240\n"
"ds_read_b64_tr_b16 a[202:203], v20 offset:10752\n"
"v_fma_f32 v106, v106, s88, -v2\n"
"v_exp_f32 v105, v105\n"
"v_add_f32 v8, v8, v104\n"
"v_fma_f32 v107, v107, s88, -v2\n"
"v_exp_f32 v106, v106\n"
"s_waitcnt lgkmcnt(6)\n"
"v_mfma_f32_32x32x16_bf16 a[128:143], a[220:223], v[84:87], a[128:143]\n"
"ds_read_b64_tr_b16 a[204:205], v21 offset:10240\n"
"ds_read_b64_tr_b16 a[206:207], v21 offset:10752\n"
"v_add_f32 v9, v9, v105\n"
"v_cvt_pk_bf16_f32 v100, v104, v105\n"
"v_fma_f32 v108, v108, s88, -v2\n"
"v_exp_f32 v107, v107\n"
"v_add_f32 v10, v10, v106\n"
"v_fma_f32 v109, v109, s88, -v2\n"
"s_waitcnt lgkmcnt(6)\n"
"v_mfma_f32_32x32x16_bf16 a[144:159], a[192:195], v[80:83], 0\n"
"ds_read_b64_tr_b16 a[208:209], v20 offset:12288\n"
"ds_read_b64_tr_b16 a[210:211], v20 offset:12800\n"
"v_exp_f32 v108, v108\n"
"v_add_f32 v11, v11, v107\n"
"v_cvt_pk_bf16_f32 v101, v106, v107\n"
"v_fma_f32 v110, v110, s88, -v2\n"
"v_exp_f32 v109, v109\n"
"s_waitcnt lgkmcnt(6)\n"
"v_mfma_f32_32x32x16_bf16 a[160:175], a[196:199], v[80:83], 0\n"
"ds_read_b64_tr_b16 a[212:213], v21 offset:12288\n"
"ds_read_b64_tr_b16 a[214:215], v21 offset:12800\n"
"v_add_f32 v8, v8, v108\n"
"v_fma_f32 v111, v111, s88, -v2\n"
"v_exp_f32 v110, v110\n"
"v_add_f32 v9, v9, v109\n"
"v_cvt_pk_bf16_f32 v102, v108, v109\n"
"s_add_u32 m0, s60, 118784\n"
"s_mov_b32 s87, 82048\n"
"buffer_load_dwordx4 v5, s[48:51], s87 offen nt lds\n"
"s_waitcnt lgkmcnt(6)\n"
"v_mfma_f32_32x32x16_bf16 a[144:159], a[200:203], v[84:87], a[144:159]\n"
"ds_read_b64_tr_b16 a[216:217], v20 offset:14336\n"
"ds_read_b64_tr_b16 a[218:219], v20 offset:14848\n"
"v_exp_f32 v111, v111\n"
"v_add_f32 v10, v10, v110\n"
"v_add_f32 v11, v11, v111\n"
"v_cvt_pk_bf16_f32 v103, v110, v111\n"
"v_fma_f32 v112, v112, s88, -v2\n"
"v_fma_f32 v113, v113, s88, -v2\n"
"s_waitcnt lgkmcnt(6)\n"
"v_mfma_f32_32x32x16_bf16 a[160:175], a[204:207], v[84:87], a[160:175]\n"
"ds_read_b64_tr_b16 a[220:221], v21 offset:14336\n"
"ds_read_b64_tr_b16 a[222:223], v21 offset:14848\n"
"v_exp_f32 v112, v112\n"
"v_fma_f32 v114, v114, s88, -v2\n"
"v_exp_f32 v113, v113\n"
"v_add_f32 v8, v8, v112\n"
"v_fma_f32 v115, v115, s88, -v2\n"
"s_waitcnt lgkmcnt(6)\n"
"v_mfma_f32_32x32x16_bf16 a[176:191], a[208:211], v[80:83], 0\n"
"ds_read_b64_tr_b16 a[192:193], v20 offset:16384\n"
"ds_read_b64_tr_b16 a[194:195], v20 offset:16896\n"
"v_exp_f32 v114, v114\n"
"v_add_f32 v9, v9, v113\n"
"v_cvt_pk_bf16_f32 v112, v112, v113\n"
"v_fma_f32 v116, v116, s88, -v2\n"
"v_exp_f32 v115, v115\n"
"s_waitcnt lgkmcnt(6)\n"
"v_mfma_f32_32x32x16_bf16 a[224:239], a[212:215], v[80:83], 0\n"
"ds_read_b64_tr_b16 a[196:197], v21 offset:16384\n"
"ds_read_b64_tr_b16 a[198:199], v21 offset:16896\n"
"v_add_f32 v10, v10, v114\n"
"v_fma_f32 v117, v117, s88, -v2\n"
"v_exp_f32 v116, v116\n"
"v_add_f32 v11, v11, v115\n"
"v_cvt_pk_bf16_f32 v113, v114, v115\n"
"s_waitcnt lgkmcnt(6)\n"
"v_mfma_f32_32x32x16_bf16 a[176:191], a[216:219], v[84:87], a[176:191]\n"
"ds_read_b64_tr_b16 a[200:201], v20 offset:18432\n"
"ds_read_b64_tr_b16 a[202:203], v20 offset:18944\n"
"v_fma_f32 v118, v118, s88, -v2\n"
"v_exp_f32 v117, v117\n"
"v_add_f32 v8, v8, v116\n"
"v_fma_f32 v119, v119, s88, -v2\n"
"v_exp_f32 v118, v118\n"
"v_add_f32 v9, v9, v117\n"
"s_add_u32 m0, s60, 122880\n"
"s_mov_b32 s87, 82176\n"
"buffer_load_dwordx4 v5, s[48:51], s87 offen nt lds\n"
"s_waitcnt lgkmcnt(6)\n"
"v_mfma_f32_32x32x16_bf16 a[224:239], a[220:223], v[84:87], a[224:239]\n"
"ds_read_b64_tr_b16 a[204:205], v21 offset:18432\n"
"ds_read_b64_tr_b16 a[206:207], v21 offset:18944\n"
"v_cvt_pk_bf16_f32 v114, v116, v117\n"
"v_fma_f32 v120, v120, s88, -v2\n"
"v_exp_f32 v119, v119\n"
"v_add_f32 v10, v10, v118\n"
"v_fma_f32 v121, v121, s88, -v2\n"
"s_waitcnt lgkmcnt(6)\n"
"v_mfma_f32_32x32x16_bf16 a[80:95], a[192:195], v[96:99], a[80:95]\n"
"ds_read_b64_tr_b16 a[208:209], v20 offset:20480\n"
"ds_read_b64_tr_b16 a[210:211], v20 offset:20992\n"
"v_exp_f32 v120, v120\n"
"v_add_f32 v11, v11, v119\n"
"v_cvt_pk_bf16_f32 v115, v118, v119\n"
"v_fma_f32 v122, v122, s88, -v2\n"
"v_exp_f32 v121, v121\n"
"s_waitcnt lgkmcnt(6)\n"
"v_mfma_f32_32x32x16_bf16 a[96:111], a[196:199], v[96:99], a[96:111]\n"
"ds_read_b64_tr_b16 a[212:213], v21 offset:20480\n"
"ds_read_b64_tr_b16 a[214:215], v21 offset:20992\n"
"v_add_f32 v8, v8, v120\n"
"v_fma_f32 v123, v123, s88, -v2\n"
"v_exp_f32 v122, v122\n"
"v_add_f32 v9, v9, v121\n"
"v_cvt_pk_bf16_f32 v116, v120, v121\n"
"v_fma_f32 v124, v124, s88, -v2\n"
"s_waitcnt lgkmcnt(6)\n"
"v_mfma_f32_32x32x16_bf16 a[80:95], a[200:203], v[100:103], a[80:95]\n"
"ds_read_b64_tr_b16 a[216:217], v20 offset:22528\n"
"ds_read_b64_tr_b16 a[218:219], v20 offset:23040\n"
"v_exp_f32 v123, v123\n"
"v_add_f32 v10, v10, v122\n"
"v_fma_f32 v125, v125, s88, -v2\n"
"v_exp_f32 v124, v124\n"
"v_add_f32 v11, v11, v123\n"
"s_add_u32 m0, s60, 126976\n"
"s_mov_b32 s87, 82304\n"
"buffer_load_dwordx4 v5, s[48:51], s87 offen nt lds\n"
"s_waitcnt lgkmcnt(6)\n"
"v_mfma_f32_32x32x16_bf16 a[96:111], a[204:207], v[100:103], a[96:111]\n"
"ds_read_b64_tr_b16 a[220:221], v21 offset:22528\n"
"ds_read_b64_tr_b16 a[222:223], v21 offset:23040\n"
"v_cvt_pk_bf16_f32 v117, v122, v123\n"
"v_fma_f32 v126, v126, s88, -v2\n"
"v_exp_f32 v125, v125\n"
"v_add_f32 v8, v8, v124\n"
"v_fma_f32 v127, v127, s88, -v2\n"
"s_waitcnt lgkmcnt(6)\n"
"v_mfma_f32_32x32x16_bf16 a[112:127], a[208:211], v[96:99], a[112:127]\n"
"ds_read_b64_tr_b16 a[192:193], v20 offset:24576\n"
"ds_read_b64_tr_b16 a[194:195], v20 offset:25088\n"
"v_exp_f32 v126, v126\n"
"v_add_f32 v9, v9, v125\n"
"v_cvt_pk_bf16_f32 v118, v124, v125\n"
"v_exp_f32 v127, v127\n"
"v_add_f32 v10, v10, v126\n"
"s_waitcnt lgkmcnt(6)\n"
"v_mfma_f32_32x32x16_bf16 a[128:143], a[212:215], v[96:99], a[128:143]\n"
"ds_read_b64_tr_b16 a[196:197], v21 offset:24576\n"
"ds_read_b64_tr_b16 a[198:199], v21 offset:25088\n"
"v_add_f32 v11, v11, v127\n"
"v_cvt_pk_bf16_f32 v119, v126, v127\n"
"v_fma_f32 v32, v32, s88, -v2\n"
"v_fma_f32 v33, v33, s88, -v2\n"
"v_exp_f32 v32, v32\n"
"v_fma_f32 v34, v34, s88, -v2\n"
"s_waitcnt lgkmcnt(6)\n"
"v_mfma_f32_32x32x16_bf16 a[112:127], a[216:219], v[100:103], a[112:127]\n"
"ds_read_b64_tr_b16 a[200:201], v20 offset:26624\n"
"ds_read_b64_tr_b16 a[202:203], v20 offset:27136\n"
"v_exp_f32 v33, v33\n"
"v_add_f32 v8, v8, v32\n"
"v_fma_f32 v35, v35, s88, -v2\n"
"v_exp_f32 v34, v34\n"
"v_add_f32 v9, v9, v33\n"
"s_waitcnt lgkmcnt(6)\n"
"v_mfma_f32_32x32x16_bf16 a[128:143], a[220:223], v[100:103], a[128:143]\n"
"ds_read_b64_tr_b16 a[204:205], v21 offset:26624\n"
"ds_read_b64_tr_b16 a[206:207], v21 offset:27136\n"
"v_cvt_pk_bf16_f32 v32, v32, v33\n"
"v_fma_f32 v36, v36, s88, -v2\n"
"v_exp_f32 v35, v35\n"
"v_add_f32 v10, v10, v34\n"
"v_fma_f32 v37, v37, s88, -v2\n"
"s_add_u32 m0, s60, 131072\n"
"s_mov_b32 s87, 98304\n"
"buffer_load_dwordx4 v5, s[48:51], s87 offen nt lds\n"
"s_waitcnt lgkmcnt(6)\n"
"v_mfma_f32_32x32x16_bf16 a[144:159], a[192:195], v[96:99], a[144:159]\n"
"ds_read_b64_tr_b16 a[208:209], v20 offset:28672\n"
"ds_read_b64_tr_b16 a[210:211], v20 offset:29184\n"
"v_exp_f32 v36, v36\n"
"v_add_f32 v11, v11, v35\n"
"v_cvt_pk_bf16_f32 v33, v34, v35\n"
"v_fma_f32 v38, v38, s88, -v2\n"
"v_exp_f32 v37, v37\n"
"v_add_f32 v8, v8, v36\n"
"s_waitcnt lgkmcnt(6)\n"
"v_mfma_f32_32x32x16_bf16 a[160:175], a[196:199], v[96:99], a[160:175]\n"
"ds_read_b64_tr_b16 a[212:213], v21 offset:28672\n"
"ds_read_b64_tr_b16 a[214:215], v21 offset:29184\n"
"v_fma_f32 v39, v39, s88, -v2\n"
"v_exp_f32 v38, v38\n"
"v_add_f32 v9, v9, v37\n"
"v_cvt_pk_bf16_f32 v34, v36, v37\n"
"v_fma_f32 v40, v40, s88, -v2\n"
"s_waitcnt lgkmcnt(6)\n"
"v_mfma_f32_32x32x16_bf16 a[144:159], a[200:203], v[100:103], a[144:159]\n"
"ds_read_b64_tr_b16 a[216:217], v20 offset:30720\n"
"ds_read_b64_tr_b16 a[218:219], v20 offset:31232\n"
"v_exp_f32 v39, v39\n"
"v_add_f32 v10, v10, v38\n"
"v_fma_f32 v41, v41, s88, -v2\n"
"v_exp_f32 v40, v40\n"
"v_add_f32 v11, v11, v39\n"
"s_waitcnt lgkmcnt(6)\n"
"v_mfma_f32_32x32x16_bf16 a[160:175], a[204:207], v[100:103], a[160:175]\n"
"ds_read_b64_tr_b16 a[220:221], v21 offset:30720\n"
"ds_read_b64_tr_b16 a[222:223], v21 offset:31232\n"
"v_add_u32 v20, 65536, v7\n"
"v_xor_b32 v21, 64, v20\n"
"v_cvt_pk_bf16_f32 v35, v38, v39\n"
"v_fma_f32 v42, v42, s88, -v2\n"
"v_exp_f32 v41, v41\n"
"v_add_f32 v8, v8, v40\n"
"v_fma_f32 v43, v43, s88, -v2\n"
"s_waitcnt lgkmcnt(6)\n"
"v_mfma_f32_32x32x16_bf16 a[176:191], a[208:211], v[96:99], a[176:191]\n"
"ds_read_b64_tr_b16 a[192:193], v20 offset:0\n"
"ds_read_b64_tr_b16 a[194:195], v20 offset:512\n"
"v_exp_f32 v42, v42\n"
"v_add_f32 v9, v9, v41\n"
"v_cvt_pk_bf16_f32 v36, v40, v41\n"
"v_fma_f32 v44, v44, s88, -v2\n"
"v_exp_f32 v43, v43\n"
"v_add_f32 v10, v10, v42\n"
"s_add_u32 m0, s60, 135168\n"
"s_mov_b32 s87, 98432\n"
"buffer_load_dwordx4 v5, s[48:51], s87 offen nt lds\n"
"s_waitcnt lgkmcnt(6)\n"
"v_mfma_f32_32x32x16_bf16 a[224:239], a[212:215], v[96:99], a[224:239]\n"
"ds_read_b64_tr_b16 a[196:197], v21 offset:0\n"
"ds_read_b64_tr_b16 a[198:199], v21 offset:512\n"
"v_fma_f32 v45, v45, s88, -v2\n"
"v_exp_f32 v44, v44\n"
"v_add_f32 v11, v11, v43\n"
"v_cvt_pk_bf16_f32 v37, v42, v43\n"
"v_fma_f32 v46, v46, s88, -v2\n"
"s_waitcnt lgkmcnt(6)\n"
"v_mfma_f32_32x32x16_bf16 a[176:191], a[216:219], v[100:103], a[176:191]\n"
"ds_read_b64_tr_b16 a[200:201], v20 offset:2048\n"
"ds_read_b64_tr_b16 a[202:203], v20 offset:2560\n"
"v_exp_f32 v45, v45\n"
"v_add_f32 v8, v8, v44\n"
"v_fma_f32 v47, v47, s88, -v2\n"
"v_exp_f32 v46, v46\n"
"v_add_f32 v9, v9, v45\n"
"s_waitcnt lgkmcnt(6)\n"
"v_mfma_f32_32x32x16_bf16 a[224:239], a[220:223], v[100:103], a[224:239]\n"
"ds_read_b64_tr_b16 a[204:205], v21 offset:2048\n"
"ds_read_b64_tr_b16 a[206:207], v21 offset:2560\n"
"v_cvt_pk_bf16_f32 v38, v44, v45\n"
"v_exp_f32 v47, v47\n"
"v_add_f32 v10, v10, v46\n"
"v_add_f32 v11, v11, v47\n"
"v_cvt_pk_bf16_f32 v39, v46, v47\n"
"s_nop 1\n"
"s_waitcnt lgkmcnt(6)\n"
"v_mfma_f32_32x32x16_bf16 a[80:95], a[192:195], v[112:115], a[80:95]\n"
"s_waitcnt vmcnt(9)\n"
"s_barrier\n"
"ds_read_b64_tr_b16 a[208:209], v20 offset:4096\n"
"ds_read_b64_tr_b16 a[210:211], v20 offset:4608\n"
"v_fma_f32 v48, v48, s88, -v2\n"
"v_fma_f32 v49, v49, s88, -v2\n"
"v_exp_f32 v48, v48\n"
"v_fma_f32 v50, v50, s88, -v2\n"
"v_exp_f32 v49, v49\n"
"v_add_f32 v8, v8, v48\n"
"s_add_u32 m0, s60, 139264\n"
"s_mov_b32 s87, 98560\n"
"buffer_load_dwordx4 v5, s[48:51], s87 offen nt lds\n"
"s_waitcnt lgkmcnt(6)\n"
"v_mfma_f32_32x32x16_bf16 a[96:111], a[196:199], v[112:115], a[96:111]\n"
"ds_read_b64_tr_b16 a[212:213], v21 offset:4096\n"
"ds_read_b64_tr_b16 a[214:215], v21 offset:4608\n"
"v_fma_f32 v51, v51, s88, -v2\n"
"v_exp_f32 v50, v50\n"
"v_add_f32 v9, v9, v49\n"
"v_cvt_pk_bf16_f32 v48, v48, v49\n"
"v_fma_f32 v52, v52, s88, -v2\n"
"v_exp_f32 v51, v51\n"
"s_waitcnt lgkmcnt(6)\n"
"v_mfma_f32_32x32x16_bf16 a[80:95], a[200:203], v[116:119], a[80:95]\n"
"ds_read_b64_tr_b16 a[216:217], v20 offset:6144\n"
"ds_read_b64_tr_b16 a[218:219], v20 offset:6656\n"
"v_add_f32 v10, v10, v50\n"
"v_fma_f32 v53, v53, s88, -v2\n"
"v_exp_f32 v52, v52\n"
"v_add_f32 v11, v11, v51\n"
"v_cvt_pk_bf16_f32 v49, v50, v51\n"
"v_fma_f32 v54, v54, s88, -v2\n"
"s_waitcnt lgkmcnt(6)\n"
"v_mfma_f32_32x32x16_bf16 a[96:111], a[204:207], v[116:119], a[96:111]\n"
"ds_read_b64_tr_b16 a[220:221], v21 offset:6144\n"
"ds_read_b64_tr_b16 a[222:223], v21 offset:6656\n"
"v_exp_f32 v53, v53\n"
"v_add_f32 v8, v8, v52\n"
"v_fma_f32 v55, v55, s88, -v2\n"
"v_exp_f32 v54, v54\n"
"v_add_f32 v9, v9, v53\n"
"v_cvt_pk_bf16_f32 v50, v52, v53\n"
"v_fma_f32 v56, v56, s88, -v2\n"
"s_waitcnt lgkmcnt(6)\n"
"v_mfma_f32_32x32x16_bf16 a[112:127], a[208:211], v[112:115], a[112:127]\n"
"ds_read_b64_tr_b16 a[192:193], v20 offset:8192\n"
"ds_read_b64_tr_b16 a[194:195], v20 offset:8704\n"
"v_exp_f32 v55, v55\n"
"v_add_f32 v10, v10, v54\n"
"v_fma_f32 v57, v57, s88, -v2\n"
"v_exp_f32 v56, v56\n"
"v_add_f32 v11, v11, v55\n"
"v_cvt_pk_bf16_f32 v51, v54, v55\n"
"s_waitcnt lgkmcnt(6)\n"
"v_mfma_f32_32x32x16_bf16 a[128:143], a[212:215], v[112:115], a[128:143]\n"
"ds_read_b64_tr_b16 a[196:197], v21 offset:8192\n"
"ds_read_b64_tr_b16 a[198:199], v21 offset:8704\n"
"v_fma_f32 v58, v58, s88, -v2\n"
"v_exp_f32 v57, v57\n"
"v_add_f32 v8, v8, v56\n"
"v_fma_f32 v59, v59, s88, -v2\n"
"v_exp_f32 v58, v58\n"
"v_add_f32 v9, v9, v57\n"
"s_add_u32 m0, s60, 143360\n"
"s_mov_b32 s87, 98688\n"
"buffer_load_dwordx4 v5, s[48:51], s87 offen nt lds\n"
"s_waitcnt lgkmcnt(6)\n"
"v_mfma_f32_32x32x16_bf16 a[112:127], a[216:219], v[116:119], a[112:127]\n"
"ds_read_b64_tr_b16 a[200:201], v20 offset:10240\n"
"ds_read_b64_tr_b16 a[202:203], v20 offset:10752\n"
"v_cvt_pk_bf16_f32 v52, v56, v57\n"
"v_fma_f32 v60, v60, s88, -v2\n"
"v_exp_f32 v59, v59\n"
"v_add_f32 v10, v10, v58\n"
"v_fma_f32 v61, v61, s88, -v2\n"
"v_exp_f32 v60, v60\n"
"s_waitcnt lgkmcnt(6)\n"
"v_mfma_f32_32x32x16_bf16 a[128:143], a[220:223], v[116:119], a[128:143]\n"
"ds_read_b64_tr_b16 a[204:205], v21 offset:10240\n"
"ds_read_b64_tr_b16 a[206:207], v21 offset:10752\n"
"v_add_f32 v11, v11, v59\n"
"v_cvt_pk_bf16_f32 v53, v58, v59\n"
"v_fma_f32 v62, v62, s88, -v2\n"
"v_exp_f32 v61, v61\n"
"v_add_f32 v8, v8, v60\n"
"v_fma_f32 v63, v63, s88, -v2\n"
"v_exp_f32 v62, v62\n"
"s_waitcnt lgkmcnt(6)\n"
"v_mfma_f32_32x32x16_bf16 a[144:159], a[192:195], v[112:115], a[144:159]\n"
"ds_read_b64_tr_b16 a[208:209], v20 offset:12288\n"
"ds_read_b64_tr_b16 a[210:211], v20 offset:12800\n"
"v_add_f32 v9, v9, v61\n"
"v_cvt_pk_bf16_f32 v54, v60, v61\n"
"v_exp_f32 v63, v63\n"
"v_add_f32 v10, v10, v62\n"
"v_add_f32 v11, v11, v63\n"
"v_cvt_pk_bf16_f32 v55, v62, v63\n"
"s_waitcnt lgkmcnt(6)\n"
"v_mfma_f32_32x32x16_bf16 a[160:175], a[196:199], v[112:115], a[160:175]\n"
"ds_read_b64_tr_b16 a[212:213], v21 offset:12288\n"
"ds_read_b64_tr_b16 a[214:215], v21 offset:12800\n"
"v_accvgpr_read_b32 v64, a32\n"
"v_accvgpr_read_b32 v65, a33\n"
"v_accvgpr_read_b32 v66, a34\n"
"v_accvgpr_read_b32 v67, a35\n"
"v_accvgpr_read_b32 v68, a36\n"
"v_accvgpr_read_b32 v69, a37\n"
"s_add_u32 m0, s60, 147456\n"
"s_mov_b32 s87, 114688\n"
"buffer_load_dwordx4 v5, s[48:51], s87 offen nt lds\n"
"s_waitcnt lgkmcnt(6)\n"
"v_mfma_f32_32x32x16_bf16 a[144:159], a[200:203], v[116:119], a[144:159]\n"
"ds_read_b64_tr_b16 a[216:217], v20 offset:14336\n"
"ds_read_b64_tr_b16 a[218:219], v20 offset:14848\n"
"v_accvgpr_read_b32 v70, a38\n"
"v_accvgpr_read_b32 v71, a39\n"
"v_accvgpr_read_b32 v72, a40\n"
"v_accvgpr_read_b32 v73, a41\n"
"v_accvgpr_read_b32 v74, a42\n"
"v_accvgpr_read_b32 v75, a43\n"
"v_accvgpr_read_b32 v76, a44\n"
"s_waitcnt lgkmcnt(6)\n"
"v_mfma_f32_32x32x16_bf16 a[160:175], a[204:207], v[116:119], a[160:175]\n"
"ds_read_b64_tr_b16 a[220:221], v21 offset:14336\n"
"ds_read_b64_tr_b16 a[222:223], v21 offset:14848\n"
"v_accvgpr_read_b32 v77, a45\n"
"v_accvgpr_read_b32 v78, a46\n"
"v_accvgpr_read_b32 v79, a47\n"
"v_fma_f32 v64, v64, s88, -v2\n"
"v_fma_f32 v65, v65, s88, -v2\n"
"v_exp_f32 v64, v64\n"
"s_waitcnt lgkmcnt(6)\n"
"v_mfma_f32_32x32x16_bf16 a[176:191], a[208:211], v[112:115], a[176:191]\n"
"ds_read_b64_tr_b16 a[192:193], v20 offset:16384\n"
"ds_read_b64_tr_b16 a[194:195], v20 offset:16896\n"
"v_fma_f32 v66, v66, s88, -v2\n"
"v_exp_f32 v65, v65\n"
"v_add_f32 v8, v8, v64\n"
"v_fma_f32 v67, v67, s88, -v2\n"
"v_exp_f32 v66, v66\n"
"v_add_f32 v9, v9, v65\n"
"s_waitcnt lgkmcnt(6)\n"
"v_mfma_f32_32x32x16_bf16 a[224:239], a[212:215], v[112:115], a[224:239]\n"
"ds_read_b64_tr_b16 a[196:197], v21 offset:16384\n"
"ds_read_b64_tr_b16 a[198:199], v21 offset:16896\n"
"v_cvt_pk_bf16_f32 v64, v64, v65\n"
"v_fma_f32 v68, v68, s88, -v2\n"
"v_exp_f32 v67, v67\n"
"v_add_f32 v10, v10, v66\n"
"v_fma_f32 v69, v69, s88, -v2\n"
"v_exp_f32 v68, v68\n"
"s_add_u32 m0, s60, 151552\n"
"s_mov_b32 s87, 114816\n"
"buffer_load_dwordx4 v5, s[48:51], s87 offen nt lds\n"
"s_waitcnt lgkmcnt(6)\n"
"v_mfma_f32_32x32x16_bf16 a[176:191], a[216:219], v[116:119], a[176:191]\n"
"ds_read_b64_tr_b16 a[200:201], v20 offset:18432\n"
"ds_read_b64_tr_b16 a[202:203], v20 offset:18944\n"
"v_add_f32 v11, v11, v67\n"
"v_cvt_pk_bf16_f32 v65, v66, v67\n"
"v_fma_f32 v70, v70, s88, -v2\n"
"v_exp_f32 v69, v69\n"
"v_add_f32 v8, v8, v68\n"
"v_fma_f32 v71, v71, s88, -v2\n"
"v_exp_f32 v70, v70\n"
"s_waitcnt lgkmcnt(6)\n"
"v_mfma_f32_32x32x16_bf16 a[224:239], a[220:223], v[116:119], a[224:239]\n"
"ds_read_b64_tr_b16 a[204:205], v21 offset:18432\n"
"ds_read_b64_tr_b16 a[206:207], v21 offset:18944\n"
"v_add_f32 v9, v9, v69\n"
"v_cvt_pk_bf16_f32 v66, v68, v69\n"
"v_fma_f32 v72, v72, s88, -v2\n"
"v_exp_f32 v71, v71\n"
"v_add_f32 v10, v10, v70\n"
"v_fma_f32 v73, v73, s88, -v2\n"
"s_waitcnt lgkmcnt(6)\n"
"v_mfma_f32_32x32x16_bf16 a[80:95], a[192:195], v[32:35], a[80:95]\n"
"ds_read_b64_tr_b16 a[208:209], v20 offset:20480\n"
"ds_read_b64_tr_b16 a[210:211], v20 offset:20992\n"
"v_exp_f32 v72, v72\n"
"v_add_f32 v11, v11, v71\n"
"v_cvt_pk_bf16_f32 v67, v70, v71\n"
"v_fma_f32 v74, v74, s88, -v2\n"
"v_exp_f32 v73, v73\n"
"v_add_f32 v8, v8, v72\n"
"s_waitcnt lgkmcnt(6)\n"
"v_mfma_f32_32x32x16_bf16 a[96:111], a[196:199], v[32:35], a[96:111]\n"
"ds_read_b64_tr_b16 a[212:213], v21 offset:20480\n"
"ds_read_b64_tr_b16 a[214:215], v21 offset:20992\n"
"v_fma_f32 v75, v75, s88, -v2\n"
"v_exp_f32 v74, v74\n"
"v_add_f32 v9, v9, v73\n"
"v_cvt_pk_bf16_f32 v68, v72, v73\n"
"v_fma_f32 v76, v76, s88, -v2\n"
"v_exp_f32 v75, v75\n"
"v_add_f32 v10, v10, v74\n"
"s_waitcnt lgkmcnt(6)\n"
"v_mfma_f32_32x32x16_bf16 a[80:95], a[200:203], v[36:39], a[80:95]\n"
"ds_read_b64_tr_b16 a[216:217], v20 offset:22528\n"
"ds_read_b64_tr_b16 a[218:219], v20 offset:23040\n"
"v_fma_f32 v77, v77, s88, -v2\n"
"v_exp_f32 v76, v76\n"
"v_add_f32 v11, v11, v75\n"
"v_cvt_pk_bf16_f32 v69, v74, v75\n"
"v_fma_f32 v78, v78, s88, -v2\n"
"v_exp_f32 v77, v77\n"
"s_add_u32 m0, s60, 155648\n"
"s_mov_b32 s87, 114944\n"
"buffer_load_dwordx4 v5, s[48:51], s87 offen nt lds\n"
"s_waitcnt lgkmcnt(6)\n"
"v_mfma_f32_32x32x16_bf16 a[96:111], a[204:207], v[36:39], a[96:111]\n"
"ds_read_b64_tr_b16 a[220:221], v21 offset:22528\n"
"ds_read_b64_tr_b16 a[222:223], v21 offset:23040\n"
"v_add_f32 v8, v8, v76\n"
"v_fma_f32 v79, v79, s88, -v2\n"
"v_exp_f32 v78, v78\n"
"v_add_f32 v9, v9, v77\n"
"v_cvt_pk_bf16_f32 v70, v76, v77\n"
"v_exp_f32 v79, v79\n"
"s_waitcnt lgkmcnt(6)\n"
"v_mfma_f32_32x32x16_bf16 a[112:127], a[208:211], v[32:35], a[112:127]\n"
"ds_read_b64_tr_b16 a[192:193], v20 offset:24576\n"
"ds_read_b64_tr_b16 a[194:195], v20 offset:25088\n"
"v_add_f32 v10, v10, v78\n"
"v_add_f32 v11, v11, v79\n"
"v_cvt_pk_bf16_f32 v71, v78, v79\n"
"v_accvgpr_read_b32 v80, a48\n"
"v_accvgpr_read_b32 v81, a49\n"
"v_accvgpr_read_b32 v82, a50\n"
"s_waitcnt lgkmcnt(6)\n"
"v_mfma_f32_32x32x16_bf16 a[128:143], a[212:215], v[32:35], a[128:143]\n"
"ds_read_b64_tr_b16 a[196:197], v21 offset:24576\n"
"ds_read_b64_tr_b16 a[198:199], v21 offset:25088\n"
"v_accvgpr_read_b32 v83, a51\n"
"v_accvgpr_read_b32 v84, a52\n"
"v_accvgpr_read_b32 v85, a53\n"
"v_accvgpr_read_b32 v86, a54\n"
"v_accvgpr_read_b32 v87, a55\n"
"v_accvgpr_read_b32 v88, a56\n"
"v_accvgpr_read_b32 v89, a57\n"
"s_waitcnt lgkmcnt(6)\n"
"v_mfma_f32_32x32x16_bf16 a[112:127], a[216:219], v[36:39], a[112:127]\n"
"ds_read_b64_tr_b16 a[200:201], v20 offset:26624\n"
"ds_read_b64_tr_b16 a[202:203], v20 offset:27136\n"
"v_accvgpr_read_b32 v90, a58\n"
"v_accvgpr_read_b32 v91, a59\n"
"v_accvgpr_read_b32 v92, a60\n"
"v_accvgpr_read_b32 v93, a61\n"
"v_accvgpr_read_b32 v94, a62\n"
"v_accvgpr_read_b32 v95, a63\n"
"s_add_u32 m0, s60, 159744\n"
"s_mov_b32 s87, 115072\n"
"buffer_load_dwordx4 v5, s[48:51], s87 offen nt lds\n"
"s_waitcnt lgkmcnt(6)\n"
"v_mfma_f32_32x32x16_bf16 a[128:143], a[220:223], v[36:39], a[128:143]\n"
"ds_read_b64_tr_b16 a[204:205], v21 offset:26624\n"
"ds_read_b64_tr_b16 a[206:207], v21 offset:27136\n"
"v_fma_f32 v80, v80, s88, -v2\n"
"v_fma_f32 v81, v81, s88, -v2\n"
"v_exp_f32 v80, v80\n"
"v_fma_f32 v82, v82, s88, -v2\n"
"v_exp_f32 v81, v81\n"
"v_add_f32 v8, v8, v80\n"
"s_waitcnt lgkmcnt(6)\n"
"v_mfma_f32_32x32x16_bf16 a[144:159], a[192:195], v[32:35], a[144:159]\n"
"ds_read_b64_tr_b16 a[208:209], v20 offset:28672\n"
"ds_read_b64_tr_b16 a[210:211], v20 offset:29184\n"
"v_fma_f32 v83, v83, s88, -v2\n"
"v_exp_f32 v82, v82\n"
"v_add_f32 v9, v9, v81\n"
"v_cvt_pk_bf16_f32 v80, v80, v81\n"
"v_fma_f32 v84, v84, s88, -v2\n"
"v_exp_f32 v83, v83\n"
"v_add_f32 v10, v10, v82\n"
"s_waitcnt lgkmcnt(6)\n"
"v_mfma_f32_32x32x16_bf16 a[160:175], a[196:199], v[32:35], a[160:175]\n"
"ds_read_b64_tr_b16 a[212:213], v21 offset:28672\n"
"ds_read_b64_tr_b16 a[214:215], v21 offset:29184\n"
"v_fma_f32 v85, v85, s88, -v2\n"
"v_exp_f32 v84, v84\n"
"v_add_f32 v11, v11, v83\n"
"v_cvt_pk_bf16_f32 v81, v82, v83\n"
"v_fma_f32 v86, v86, s88, -v2\n"
"v_exp_f32 v85, v85\n"
"s_waitcnt lgkmcnt(6)\n"
"v_mfma_f32_32x32x16_bf16 a[144:159], a[200:203], v[36:39], a[144:159]\n"
"ds_read_b64_tr_b16 a[216:217], v20 offset:30720\n"
"ds_read_b64_tr_b16 a[218:219], v20 offset:31232\n"
"v_add_f32 v8, v8, v84\n"
"v_fma_f32 v87, v87, s88, -v2\n"
"v_exp_f32 v86, v86\n"
"v_add_f32 v9, v9, v85\n"
"v_cvt_pk_bf16_f32 v82, v84, v85\n"
"v_fma_f32 v88, v88, s88, -v2\n"
"s_add_u32 m0, s60, 0\n"
"s_mov_b32 s87, 131072\n"
"buffer_load_dwordx4 v5, s[48:51], s87 offen nt lds\n"
"s_waitcnt lgkmcnt(6)\n"
"v_mfma_f32_32x32x16_bf16 a[160:175], a[204:207], v[36:39], a[160:175]\n"
"ds_read_b64_tr_b16 a[220:221], v21 offset:30720\n"
"ds_read_b64_tr_b16 a[222:223], v21 offset:31232\n"
"v_add_u32 v20, 98304, v7\n"
"v_xor_b32 v21, 64, v20\n"
"v_exp_f32 v87, v87\n"
"v_add_f32 v10, v10, v86\n"
"v_fma_f32 v89, v89, s88, -v2\n"
"v_exp_f32 v88, v88\n"
"v_add_f32 v11, v11, v87\n"
"v_cvt_pk_bf16_f32 v83, v86, v87\n"
"s_waitcnt lgkmcnt(6)\n"
"v_mfma_f32_32x32x16_bf16 a[176:191], a[208:211], v[32:35], a[176:191]\n"
"ds_read_b64_tr_b16 a[192:193], v20 offset:0\n"
"ds_read_b64_tr_b16 a[194:195], v20 offset:512\n"
"v_fma_f32 v90, v90, s88, -v2\n"
"v_exp_f32 v89, v89\n"
"v_add_f32 v8, v8, v88\n"
"v_fma_f32 v91, v91, s88, -v2\n"
"v_exp_f32 v90, v90\n"
"v_add_f32 v9, v9, v89\n"
"v_cvt_pk_bf16_f32 v84, v88, v89\n"
"s_waitcnt lgkmcnt(6)\n"
"v_mfma_f32_32x32x16_bf16 a[224:239], a[212:215], v[32:35], a[224:239]\n"
"ds_read_b64_tr_b16 a[196:197], v21 offset:0\n"
"ds_read_b64_tr_b16 a[198:199], v21 offset:512\n"
"v_fma_f32 v92, v92, s88, -v2\n"
"v_exp_f32 v91, v91\n"
"v_add_f32 v10, v10, v90\n"
"v_fma_f32 v93, v93, s88, -v2\n"
"v_exp_f32 v92, v92\n"
"v_add_f32 v11, v11, v91\n"
"s_waitcnt lgkmcnt(6)\n"
"v_mfma_f32_32x32x16_bf16 a[176:191], a[216:219], v[36:39], a[176:191]\n"
"ds_read_b64_tr_b16 a[200:201], v20 offset:2048\n"
"ds_read_b64_tr_b16 a[202:203], v20 offset:2560\n"
"v_cvt_pk_bf16_f32 v85, v90, v91\n"
"v_fma_f32 v94, v94, s88, -v2\n"
"v_exp_f32 v93, v93\n"
"v_add_f32 v8, v8, v92\n"
"v_fma_f32 v95, v95, s88, -v2\n"
"v_exp_f32 v94, v94\n"
"s_waitcnt lgkmcnt(6)\n"
"v_mfma_f32_32x32x16_bf16 a[224:239], a[220:223], v[36:39], a[224:239]\n"
"ds_read_b64_tr_b16 a[204:205], v21 offset:2048\n"
"ds_read_b64_tr_b16 a[206:207], v21 offset:2560\n"
"v_add_f32 v9, v9, v93\n"
"v_cvt_pk_bf16_f32 v86, v92, v93\n"
"v_exp_f32 v95, v95\n"
"v_add_f32 v10, v10, v94\n"
"v_add_f32 v11, v11, v95\n"
"v_cvt_pk_bf16_f32 v87, v94, v95\n"
"s_nop 1\n"
"s_add_u32 m0, s60, 4096\n"
"s_mov_b32 s87, 131200\n"
"buffer_load_dwordx4 v5, s[48:51], s87 offen nt lds\n"
"s_waitcnt lgkmcnt(6)\n"
"v_mfma_f32_32x32x16_bf16 a[80:95], a[192:195], v[48:51], a[80:95]\n"
"s_waitcnt vmcnt(9)\n"
"s_barrier\n"
"ds_read_b64_tr_b16 a[208:209], v20 offset:4096\n"
"ds_read_b64_tr_b16 a[210:211], v20 offset:4608\n"
"v_accvgpr_read_b32 v96, a64\n"
"v_accvgpr_read_b32 v97, a65\n"
"v_accvgpr_read_b32 v98, a66\n"
"v_accvgpr_read_b32 v99, a67\n"
"v_accvgpr_read_b32 v100, a68\n"
"s_waitcnt lgkmcnt(6)\n"
"v_mfma_f32_32x32x16_bf16 a[96:111], a[196:199], v[48:51], a[96:111]\n"
"ds_read_b64_tr_b16 a[212:213], v21 offset:4096\n"
"ds_read_b64_tr_b16 a[214:215], v21 offset:4608\n"
"v_accvgpr_read_b32 v101, a69\n"
"v_accvgpr_read_b32 v102, a70\n"
"v_accvgpr_read_b32 v103, a71\n"
"v_accvgpr_read_b32 v104, a72\n"
"v_accvgpr_read_b32 v105, a73\n"
"v_accvgpr_read_b32 v106, a74\n"
"s_waitcnt lgkmcnt(6)\n"
"v_mfma_f32_32x32x16_bf16 a[80:95], a[200:203], v[52:55], a[80:95]\n"
"ds_read_b64_tr_b16 a[216:217], v20 offset:6144\n"
"ds_read_b64_tr_b16 a[218:219], v20 offset:6656\n"
"v_accvgpr_read_b32 v107, a75\n"
"v_accvgpr_read_b32 v108, a76\n"
"v_accvgpr_read_b32 v109, a77\n"
"v_accvgpr_read_b32 v110, a78\n"
"v_accvgpr_read_b32 v111, a79\n"
"v_fma_f32 v96, v96, s88, -v2\n"
"s_waitcnt lgkmcnt(6)\n"
"v_mfma_f32_32x32x16_bf16 a[96:111], a[204:207], v[52:55], a[96:111]\n"
"ds_read_b64_tr_b16 a[220:221], v21 offset:6144\n"
"ds_read_b64_tr_b16 a[222:223], v21 offset:6656\n"
"v_fma_f32 v97, v97, s88, -v2\n"
"v_exp_f32 v96, v96\n"
"v_fma_f32 v98, v98, s88, -v2\n"
"v_exp_f32 v97, v97\n"
"v_add_f32 v8, v8, v96\n"
"v_fma_f32 v99, v99, s88, -v2\n"
"s_add_u32 m0, s60, 8192\n"
"s_mov_b32 s87, 131328\n"
"buffer_load_dwordx4 v5, s[48:51], s87 offen nt lds\n"
"s_waitcnt lgkmcnt(6)\n"
"v_mfma_f32_32x32x16_bf16 a[112:127], a[208:211], v[48:51], a[112:127]\n"
"ds_read_b64_tr_b16 a[192:193], v20 offset:8192\n"
"ds_read_b64_tr_b16 a[194:195], v20 offset:8704\n"
"v_exp_f32 v98, v98\n"
"v_add_f32 v9, v9, v97\n"
"v_cvt_pk_bf16_f32 v96, v96, v97\n"
"v_fma_f32 v100, v100, s88, -v2\n"
"v_exp_f32 v99, v99\n"
"s_waitcnt lgkmcnt(6)\n"
"v_mfma_f32_32x32x16_bf16 a[128:143], a[212:215], v[48:51], a[128:143]\n"
"ds_read_b64_tr_b16 a[196:197], v21 offset:8192\n"
"ds_read_b64_tr_b16 a[198:199], v21 offset:8704\n"
"v_add_f32 v10, v10, v98\n"
"v_fma_f32 v101, v101, s88, -v2\n"
"v_exp_f32 v100, v100\n"
"v_add_f32 v11, v11, v99\n"
"v_cvt_pk_bf16_f32 v97, v98, v99\n"
"v_fma_f32 v102, v102, s88, -v2\n"
"s_waitcnt lgkmcnt(6)\n"
"v_mfma_f32_32x32x16_bf16 a[112:127], a[216:219], v[52:55], a[112:127]\n"
"ds_read_b64_tr_b16 a[200:201], v20 offset:10240\n"
"ds_read_b64_tr_b16 a[202:203], v20 offset:10752\n"
"v_exp_f32 v101, v101\n"
"v_add_f32 v8, v8, v100\n"
"v_fma_f32 v103, v103, s88, -v2\n"
"v_exp_f32 v102, v102\n"
"v_add_f32 v9, v9, v101\n"
"v_cvt_pk_bf16_f32 v98, v100, v101\n"
"s_waitcnt lgkmcnt(6)\n"
"v_mfma_f32_32x32x16_bf16 a[128:143], a[220:223], v[52:55], a[128:143]\n"
"ds_read_b64_tr_b16 a[204:205], v21 offset:10240\n"
"ds_read_b64_tr_b16 a[206:207], v21 offset:10752\n"
"v_fma_f32 v104, v104, s88, -v2\n"
"v_exp_f32 v103, v103\n"
"v_add_f32 v10, v10, v102\n"
"v_fma_f32 v105, v105, s88, -v2\n"
"v_exp_f32 v104, v104\n"
"v_add_f32 v11, v11, v103\n"
"s_waitcnt lgkmcnt(6)\n"
"v_mfma_f32_32x32x16_bf16 a[144:159], a[192:195], v[48:51], a[144:159]\n"
"ds_read_b64_tr_b16 a[208:209], v20 offset:12288\n"
"ds_read_b64_tr_b16 a[210:211], v20 offset:12800\n"
"v_cvt_pk_bf16_f32 v99, v102, v103\n"
"v_fma_f32 v106, v106, s88, -v2\n"
"v_exp_f32 v105, v105\n"
"v_add_f32 v8, v8, v104\n"
"v_fma_f32 v107, v107, s88, -v2\n"
"v_exp_f32 v106, v106\n"
"s_add_u32 m0, s60, 12288\n"
"s_mov_b32 s87, 131456\n"
"buffer_load_dwordx4 v5, s[48:51], s87 offen nt lds\n"
"s_waitcnt lgkmcnt(6)\n"
"v_mfma_f32_32x32x16_bf16 a[160:175], a[196:199], v[48:51], a[160:175]\n"
"ds_read_b64_tr_b16 a[212:213], v21 offset:12288\n"
"ds_read_b64_tr_b16 a[214:215], v21 offset:12800\n"
"v_add_f32 v9, v9, v105\n"
"v_cvt_pk_bf16_f32 v100, v104, v105\n"
"v_fma_f32 v108, v108, s88, -v2\n"
"v_exp_f32 v107, v107\n"
"v_add_f32 v10, v10, v106\n"
"s_waitcnt lgkmcnt(6)\n"
"v_mfma_f32_32x32x16_bf16 a[144:159], a[200:203], v[52:55], a[144:159]\n"
"ds_read_b64_tr_b16 a[216:217], v20 offset:14336\n"
"ds_read_b64_tr_b16 a[218:219], v20 offset:14848\n"
"v_fma_f32 v109, v109, s88, -v2\n"
"v_exp_f32 v108, v108\n"
"v_add_f32 v11, v11, v107\n"
"v_cvt_pk_bf16_f32 v101, v106, v107\n"
"v_fma_f32 v110, v110, s88, -v2\n"
"v_exp_f32 v109, v109\n"
"s_waitcnt lgkmcnt(6)\n"
"v_mfma_f32_32x32x16_bf16 a[160:175], a[204:207], v[52:55], a[160:175]\n"
"ds_read_b64_tr_b16 a[220:221], v21 offset:14336\n"
"ds_read_b64_tr_b16 a[222:223], v21 offset:14848\n"
"v_add_f32 v8, v8, v108\n"
"v_fma_f32 v111, v111, s88, -v2\n"
"v_exp_f32 v110, v110\n"
"v_add_f32 v9, v9, v109\n"
"v_cvt_pk_bf16_f32 v102, v108, v109\n"
"v_exp_f32 v111, v111\n"
"s_waitcnt lgkmcnt(6)\n"
"v_mfma_f32_32x32x16_bf16 a[176:191], a[208:211], v[48:51], a[176:191]\n"
"ds_read_b64_tr_b16 a[192:193], v20 offset:16384\n"
"ds_read_b64_tr_b16 a[194:195], v20 offset:16896\n"
"v_add_f32 v10, v10, v110\n"
"v_add_f32 v11, v11, v111\n"
"v_cvt_pk_bf16_f32 v103, v110, v111\n"
"v_fma_f32 v128, v128, s88, -v2\n"
"v_fma_f32 v129, v129, s88, -v2\n"
"v_exp_f32 v128, v128\n"
"s_add_u32 m0, s60, 16384\n"
"s_mov_b32 s87, 147456\n"
"buffer_load_dwordx4 v5, s[48:51], s87 offen nt lds\n"
"s_waitcnt lgkmcnt(6)\n"
"v_mfma_f32_32x32x16_bf16 a[224:239], a[212:215], v[48:51], a[224:239]\n"
"ds_read_b64_tr_b16 a[196:197], v21 offset:16384\n"
"ds_read_b64_tr_b16 a[198:199], v21 offset:16896\n"
"v_fma_f32 v130, v130, s88, -v2\n"
"v_exp_f32 v129, v129\n"
"v_add_f32 v8, v8, v128\n"
"v_fma_f32 v131, v131, s88, -v2\n"
"v_exp_f32 v130, v130\n"
"s_waitcnt lgkmcnt(6)\n"
"v_mfma_f32_32x32x16_bf16 a[176:191], a[216:219], v[52:55], a[176:191]\n"
"ds_read_b64_tr_b16 a[200:201], v20 offset:18432\n"
"ds_read_b64_tr_b16 a[202:203], v20 offset:18944\n"
"v_add_f32 v9, v9, v129\n"
"v_cvt_pk_bf16_f32 v128, v128, v129\n"
"v_fma_f32 v132, v132, s88, -v2\n"
"v_exp_f32 v131, v131\n"
"v_add_f32 v10, v10, v130\n"
"v_fma_f32 v133, v133, s88, -v2\n"
"s_waitcnt lgkmcnt(6)\n"
"v_mfma_f32_32x32x16_bf16 a[224:239], a[220:223], v[52:55], a[224:239]\n"
"ds_read_b64_tr_b16 a[204:205], v21 offset:18432\n"
"ds_read_b64_tr_b16 a[206:207], v21 offset:18944\n"
"v_exp_f32 v132, v132\n"
"v_add_f32 v11, v11, v131\n"
"v_cvt_pk_bf16_f32 v129, v130, v131\n"
"v_fma_f32 v134, v134, s88, -v2\n"
"v_exp_f32 v133, v133\n"
"v_add_f32 v8, v8, v132\n"
"s_waitcnt lgkmcnt(6)\n"
"v_mfma_f32_32x32x16_bf16 a[80:95], a[192:195], v[64:67], a[80:95]\n"
"ds_read_b64_tr_b16 a[208:209], v20 offset:20480\n"
"ds_read_b64_tr_b16 a[210:211], v20 offset:20992\n"
"v_fma_f32 v135, v135, s88, -v2\n"
"v_exp_f32 v134, v134\n"
"v_add_f32 v9, v9, v133\n"
"v_cvt_pk_bf16_f32 v130, v132, v133\n"
"v_fma_f32 v136, v136, s88, -v2\n"
"v_exp_f32 v135, v135\n"
"s_waitcnt lgkmcnt(6)\n"
"v_mfma_f32_32x32x16_bf16 a[96:111], a[196:199], v[64:67], a[96:111]\n"
"ds_read_b64_tr_b16 a[212:213], v21 offset:20480\n"
"ds_read_b64_tr_b16 a[214:215], v21 offset:20992\n"
"v_add_f32 v10, v10, v134\n"
"v_fma_f32 v137, v137, s88, -v2\n"
"v_exp_f32 v136, v136\n"
"v_add_f32 v11, v11, v135\n"
"v_cvt_pk_bf16_f32 v131, v134, v135\n"
"v_fma_f32 v138, v138, s88, -v2\n"
"s_add_u32 m0, s60, 20480\n"
"s_mov_b32 s87, 147584\n"
"buffer_load_dwordx4 v5, s[48:51], s87 offen nt lds\n"
"s_waitcnt lgkmcnt(6)\n"
"v_mfma_f32_32x32x16_bf16 a[80:95], a[200:203], v[68:71], a[80:95]\n"
"ds_read_b64_tr_b16 a[216:217], v20 offset:22528\n"
"ds_read_b64_tr_b16 a[218:219], v20 offset:23040\n"
"v_exp_f32 v137, v137\n"
"v_add_f32 v8, v8, v136\n"
"v_fma_f32 v139, v139, s88, -v2\n"
"v_exp_f32 v138, v138\n"
"v_add_f32 v9, v9, v137\n"
"s_waitcnt lgkmcnt(6)\n"
"v_mfma_f32_32x32x16_bf16 a[96:111], a[204:207], v[68:71], a[96:111]\n"
"ds_read_b64_tr_b16 a[220:221], v21 offset:22528\n"
"ds_read_b64_tr_b16 a[222:223], v21 offset:23040\n"
"v_cvt_pk_bf16_f32 v132, v136, v137\n"
"v_fma_f32 v140, v140, s88, -v2\n"
"v_exp_f32 v139, v139\n"
"v_add_f32 v10, v10, v138\n"
"v_fma_f32 v141, v141, s88, -v2\n"
"v_exp_f32 v140, v140\n"
"s_waitcnt lgkmcnt(6)\n"
"v_mfma_f32_32x32x16_bf16 a[112:127], a[208:211], v[64:67], a[112:127]\n"
"ds_read_b64_tr_b16 a[192:193], v20 offset:24576\n"
"ds_read_b64_tr_b16 a[194:195], v20 offset:25088\n"
"v_add_f32 v11, v11, v139\n"
"v_cvt_pk_bf16_f32 v133, v138, v139\n"
"v_fma_f32 v142, v142, s88, -v2\n"
"v_exp_f32 v141, v141\n"
"v_add_f32 v8, v8, v140\n"
"v_fma_f32 v143, v143, s88, -v2\n"
"s_waitcnt lgkmcnt(6)\n"
"v_mfma_f32_32x32x16_bf16 a[128:143], a[212:215], v[64:67], a[128:143]\n"
"ds_read_b64_tr_b16 a[196:197], v21 offset:24576\n"
"ds_read_b64_tr_b16 a[198:199], v21 offset:25088\n"
"v_exp_f32 v142, v142\n"
"v_add_f32 v9, v9, v141\n"
"v_cvt_pk_bf16_f32 v134, v140, v141\n"
"v_exp_f32 v143, v143\n"
"v_add_f32 v10, v10, v142\n"
"v_add_f32 v11, v11, v143\n"
"s_add_u32 m0, s60, 24576\n"
"s_mov_b32 s87, 147712\n"
"buffer_load_dwordx4 v5, s[48:51], s87 offen nt lds\n"
"s_waitcnt lgkmcnt(6)\n"
"v_mfma_f32_32x32x16_bf16 a[112:127], a[216:219], v[68:71], a[112:127]\n"
"ds_read_b64_tr_b16 a[200:201], v20 offset:26624\n"
"ds_read_b64_tr_b16 a[202:203], v20 offset:27136\n"
"v_cvt_pk_bf16_f32 v135, v142, v143\n"
"v_fma_f32 v144, v144, s88, -v2\n"
"v_fma_f32 v145, v145, s88, -v2\n"
"v_exp_f32 v144, v144\n"
"v_fma_f32 v146, v146, s88, -v2\n"
"s_waitcnt lgkmcnt(6)\n"
"v_mfma_f32_32x32x16_bf16 a[128:143], a[220:223], v[68:71], a[128:143]\n"
"ds_read_b64_tr_b16 a[204:205], v21 offset:26624\n"
"ds_read_b64_tr_b16 a[206:207], v21 offset:27136\n"
"v_exp_f32 v145, v145\n"
"v_add_f32 v8, v8, v144\n"
"v_fma_f32 v147, v147, s88, -v2\n"
"v_exp_f32 v146, v146\n"
"v_add_f32 v9, v9, v145\n"
"v_cvt_pk_bf16_f32 v144, v144, v145\n"
"s_waitcnt lgkmcnt(6)\n"
"v_mfma_f32_32x32x16_bf16 a[144:159], a[192:195], v[64:67], a[144:159]\n"
"ds_read_b64_tr_b16 a[208:209], v20 offset:28672\n"
"ds_read_b64_tr_b16 a[210:211], v20 offset:29184\n"
"v_fma_f32 v148, v148, s88, -v2\n"
"v_exp_f32 v147, v147\n"
"v_add_f32 v10, v10, v146\n"
"v_fma_f32 v149, v149, s88, -v2\n"
"v_exp_f32 v148, v148\n"
"v_add_f32 v11, v11, v147\n"
"s_waitcnt lgkmcnt(6)\n"
"v_mfma_f32_32x32x16_bf16 a[160:175], a[196:199], v[64:67], a[160:175]\n"
"ds_read_b64_tr_b16 a[212:213], v21 offset:28672\n"
"ds_read_b64_tr_b16 a[214:215], v21 offset:29184\n"
"v_cvt_pk_bf16_f32 v145, v146, v147\n"
"v_fma_f32 v150, v150, s88, -v2\n"
"v_exp_f32 v149, v149\n"
"v_add_f32 v8, v8, v148\n"
"v_fma_f32 v151, v151, s88, -v2\n"
"v_exp_f32 v150, v150\n"
"s_waitcnt lgkmcnt(6)\n"
"v_mfma_f32_32x32x16_bf16 a[144:159], a[200:203], v[68:71], a[144:159]\n"
"ds_read_b64_tr_b16 a[216:217], v20 offset:30720\n"
"ds_read_b64_tr_b16 a[218:219], v20 offset:31232\n"
"v_add_f32 v9, v9, v149\n"
"v_cvt_pk_bf16_f32 v146, v148, v149\n"
"v_fma_f32 v152, v152, s88, -v2\n"
"v_exp_f32 v151, v151\n"
"v_add_f32 v10, v10, v150\n"
"v_fma_f32 v153, v153, s88, -v2\n"
"s_add_u32 m0, s60, 28672\n"
"s_mov_b32 s87, 147840\n"
"buffer_load_dwordx4 v5, s[48:51], s87 offen nt lds\n"
"s_waitcnt lgkmcnt(6)\n"
"v_mfma_f32_32x32x16_bf16 a[160:175], a[204:207], v[68:71], a[160:175]\n"
"ds_read_b64_tr_b16 a[220:221], v21 offset:30720\n"
"ds_read_b64_tr_b16 a[222:223], v21 offset:31232\n"
"v_add_u32 v20, 131072, v7\n"
"v_xor_b32 v21, 64, v20\n"
"v_exp_f32 v152, v152\n"
"v_add_f32 v11, v11, v151\n"
"v_cvt_pk_bf16_f32 v147, v150, v151\n"
"v_fma_f32 v154, v154, s88, -v2\n"
"v_exp_f32 v153, v153\n"
"s_waitcnt lgkmcnt(6)\n"
"v_mfma_f32_32x32x16_bf16 a[176:191], a[208:211], v[64:67], a[176:191]\n"
"ds_read_b64_tr_b16 a[192:193], v20 offset:0\n"
"ds_read_b64_tr_b16 a[194:195], v20 offset:512\n"
"v_add_f32 v8, v8, v152\n"
"v_fma_f32 v155, v155, s88, -v2\n"
"v_exp_f32 v154, v154\n"
"v_add_f32 v9, v9, v153\n"
"v_cvt_pk_bf16_f32 v148, v152, v153\n"
"v_fma_f32 v156, v156, s88, -v2\n"
"s_waitcnt lgkmcnt(6)\n"
"v_mfma_f32_32x32x16_bf16 a[224:239], a[212:215], v[64:67], a[224:239]\n"
"ds_read_b64_tr_b16 a[196:197], v21 offset:0\n"
"ds_read_b64_tr_b16 a[198:199], v21 offset:512\n"
"v_exp_f32 v155, v155\n"
"v_add_f32 v10, v10, v154\n"
"v_fma_f32 v157, v157, s88, -v2\n"
"v_exp_f32 v156, v156\n"
"v_add_f32 v11, v11, v155\n"
"v_cvt_pk_bf16_f32 v149, v154, v155\n"
"s_waitcnt lgkmcnt(6)\n"
"v_mfma_f32_32x32x16_bf16 a[176:191], a[216:219], v[68:71], a[176:191]\n"
"ds_read_b64_tr_b16 a[200:201], v20 offset:2048\n"
"ds_read_b64_tr_b16 a[202:203], v20 offset:2560\n"
"v_fma_f32 v158, v158, s88, -v2\n"
"v_exp_f32 v157, v157\n"
"v_add_f32 v8, v8, v156\n"
"v_fma_f32 v159, v159, s88, -v2\n"
"v_exp_f32 v158, v158\n"
"v_add_f32 v9, v9, v157\n"
"s_add_u32 m0, s60, 32768\n"
"s_mov_b32 s87, 163840\n"
"buffer_load_dwordx4 v5, s[48:51], s87 offen nt lds\n"
"s_waitcnt lgkmcnt(6)\n"
"v_mfma_f32_32x32x16_bf16 a[224:239], a[220:223], v[68:71], a[224:239]\n"
"ds_read_b64_tr_b16 a[204:205], v21 offset:2048\n"
"ds_read_b64_tr_b16 a[206:207], v21 offset:2560\n"
"v_cvt_pk_bf16_f32 v150, v156, v157\n"
"v_exp_f32 v159, v159\n"
"v_add_f32 v10, v10, v158\n"
"v_add_f32 v11, v11, v159\n"
"v_cvt_pk_bf16_f32 v151, v158, v159\n"
"s_nop 1\n"
"s_waitcnt lgkmcnt(6)\n"
"v_mfma_f32_32x32x16_bf16 a[80:95], a[192:195], v[80:83], a[80:95]\n"
"s_waitcnt vmcnt(8)\n"
"s_barrier\n"
"ds_read_b64_tr_b16 a[208:209], v20 offset:4096\n"
"ds_read_b64_tr_b16 a[210:211], v20 offset:4608\n"
"v_fma_f32 v160, v160, s88, -v2\n"
"v_fma_f32 v161, v161, s88, -v2\n"
"v_exp_f32 v160, v160\n"
"v_fma_f32 v162, v162, s88, -v2\n"
"v_exp_f32 v161, v161\n"
"s_add_u32 m0, s60, 36864\n"
"s_mov_b32 s87, 180224\n"
"buffer_load_dwordx4 v5, s[48:51], s87 offen nt lds\n"
"s_waitcnt lgkmcnt(6)\n"
"v_mfma_f32_32x32x16_bf16 a[96:111], a[196:199], v[80:83], a[96:111]\n"
"ds_read_b64_tr_b16 a[212:213], v21 offset:4096\n"
"ds_read_b64_tr_b16 a[214:215], v21 offset:4608\n"
"v_add_f32 v8, v8, v160\n"
"v_fma_f32 v163, v163, s88, -v2\n"
"v_exp_f32 v162, v162\n"
"v_add_f32 v9, v9, v161\n"
"v_cvt_pk_bf16_f32 v160, v160, v161\n"
"s_waitcnt lgkmcnt(6)\n"
"v_mfma_f32_32x32x16_bf16 a[80:95], a[200:203], v[84:87], a[80:95]\n"
"ds_read_b64_tr_b16 a[216:217], v20 offset:6144\n"
"ds_read_b64_tr_b16 a[218:219], v20 offset:6656\n"
"v_fma_f32 v164, v164, s88, -v2\n"
"v_exp_f32 v163, v163\n"
"v_add_f32 v10, v10, v162\n"
"v_fma_f32 v165, v165, s88, -v2\n"
"v_exp_f32 v164, v164\n"
"s_waitcnt lgkmcnt(6)\n"
"v_mfma_f32_32x32x16_bf16 a[96:111], a[204:207], v[84:87], a[96:111]\n"
"ds_read_b64_tr_b16 a[220:221], v21 offset:6144\n"
"ds_read_b64_tr_b16 a[222:223], v21 offset:6656\n"
"v_add_f32 v11, v11, v163\n"
"v_cvt_pk_bf16_f32 v161, v162, v163\n"
"v_fma_f32 v166, v166, s88, -v2\n"
"v_exp_f32 v165, v165\n"
"v_add_f32 v8, v8, v164\n"
"v_fma_f32 v167, v167, s88, -v2\n"
"s_waitcnt lgkmcnt(6)\n"
"v_mfma_f32_32x32x16_bf16 a[112:127], a[208:211], v[80:83], a[112:127]\n"
"ds_read_b64_tr_b16 a[192:193], v20 offset:8192\n"
"ds_read_b64_tr_b16 a[194:195], v20 offset:8704\n"
"v_exp_f32 v166, v166\n"
"v_add_f32 v9, v9, v165\n"
"v_cvt_pk_bf16_f32 v162, v164, v165\n"
"v_fma_f32 v168, v168, s88, -v2\n"
"v_exp_f32 v167, v167\n"
"s_waitcnt lgkmcnt(6)\n"
"v_mfma_f32_32x32x16_bf16 a[128:143], a[212:215], v[80:83], a[128:143]\n"
"ds_read_b64_tr_b16 a[196:197], v21 offset:8192\n"
"ds_read_b64_tr_b16 a[198:199], v21 offset:8704\n"
"v_add_f32 v10, v10, v166\n"
"v_fma_f32 v169, v169, s88, -v2\n"
"v_exp_f32 v168, v168\n"
"v_add_f32 v11, v11, v167\n"
"v_cvt_pk_bf16_f32 v163, v166, v167\n"
"s_add_u32 m0, s60, 40960\n"
"s_mov_b32 s87, 196608\n"
"buffer_load_dwordx4 v5, s[48:51], s87 offen nt lds\n"
"s_waitcnt lgkmcnt(6)\n"
"v_mfma_f32_32x32x16_bf16 a[112:127], a[216:219], v[84:87], a[112:127]\n"
"ds_read_b64_tr_b16 a[200:201], v20 offset:10240\n"
"ds_read_b64_tr_b16 a[202:203], v20 offset:10752\n"
"v_fma_f32 v170, v170, s88, -v2\n"
"v_exp_f32 v169, v169\n"
"v_add_f32 v8, v8, v168\n"
"v_fma_f32 v171, v171, s88, -v2\n"
"v_exp_f32 v170, v170\n"
"s_waitcnt lgkmcnt(6)\n"
"v_mfma_f32_32x32x16_bf16 a[128:143], a[220:223], v[84:87], a[128:143]\n"
"ds_read_b64_tr_b16 a[204:205], v21 offset:10240\n"
"ds_read_b64_tr_b16 a[206:207], v21 offset:10752\n"
"v_add_f32 v9, v9, v169\n"
"v_cvt_pk_bf16_f32 v164, v168, v169\n"
"v_fma_f32 v172, v172, s88, -v2\n"
"v_exp_f32 v171, v171\n"
"v_add_f32 v10, v10, v170\n"
"v_fma_f32 v173, v173, s88, -v2\n"
"s_waitcnt lgkmcnt(6)\n"
"v_mfma_f32_32x32x16_bf16 a[144:159], a[192:195], v[80:83], a[144:159]\n"
"ds_read_b64_tr_b16 a[208:209], v20 offset:12288\n"
"ds_read_b64_tr_b16 a[210:211], v20 offset:12800\n"
"v_exp_f32 v172, v172\n"
"v_add_f32 v11, v11, v171\n"
"v_cvt_pk_bf16_f32 v165, v170, v171\n"
"v_fma_f32 v174, v174, s88, -v2\n"
"v_exp_f32 v173, v173\n"
"s_waitcnt lgkmcnt(6)\n"
"v_mfma_f32_32x32x16_bf16 a[160:175], a[196:199], v[80:83], a[160:175]\n"
"ds_read_b64_tr_b16 a[212:213], v21 offset:12288\n"
"ds_read_b64_tr_b16 a[214:215], v21 offset:12800\n"
"v_add_f32 v8, v8, v172\n"
"v_fma_f32 v175, v175, s88, -v2\n"
"v_exp_f32 v174, v174\n"
"v_add_f32 v9, v9, v173\n"
"v_cvt_pk_bf16_f32 v166, v172, v173\n"
"s_waitcnt lgkmcnt(6)\n"
"v_mfma_f32_32x32x16_bf16 a[144:159], a[200:203], v[84:87], a[144:159]\n"
"ds_read_b64_tr_b16 a[216:217], v20 offset:14336\n"
"ds_read_b64_tr_b16 a[218:219], v20 offset:14848\n"
"v_exp_f32 v175, v175\n"
"v_add_f32 v10, v10, v174\n"
"v_add_f32 v11, v11, v175\n"
"v_cvt_pk_bf16_f32 v167, v174, v175\n"
"v_fma_f32 v176, v176, s88, -v2\n"
"v_fma_f32 v177, v177, s88, -v2\n"
"s_add_u32 m0, s60, 45056\n"
"s_mov_b32 s87, 212992\n"
"buffer_load_dwordx4 v5, s[48:51], s87 offen nt lds\n"
"s_waitcnt lgkmcnt(6)\n"
"v_mfma_f32_32x32x16_bf16 a[160:175], a[204:207], v[84:87], a[160:175]\n"
"ds_read_b64_tr_b16 a[220:221], v21 offset:14336\n"
"ds_read_b64_tr_b16 a[222:223], v21 offset:14848\n"
"v_exp_f32 v176, v176\n"
"v_fma_f32 v178, v178, s88, -v2\n"
"v_exp_f32 v177, v177\n"
"v_add_f32 v8, v8, v176\n"
"v_fma_f32 v179, v179, s88, -v2\n"
"s_waitcnt lgkmcnt(6)\n"
"v_mfma_f32_32x32x16_bf16 a[176:191], a[208:211], v[80:83], a[176:191]\n"
"ds_read_b64_tr_b16 a[192:193], v20 offset:16384\n"
"ds_read_b64_tr_b16 a[194:195], v20 offset:16896\n"
"v_exp_f32 v178, v178\n"
"v_add_f32 v9, v9, v177\n"
"v_cvt_pk_bf16_f32 v176, v176, v177\n"
"v_fma_f32 v180, v180, s88, -v2\n"
"v_exp_f32 v179, v179\n"
"s_waitcnt lgkmcnt(6)\n"
"v_mfma_f32_32x32x16_bf16 a[224:239], a[212:215], v[80:83], a[224:239]\n"
"ds_read_b64_tr_b16 a[196:197], v21 offset:16384\n"
"ds_read_b64_tr_b16 a[198:199], v21 offset:16896\n"
"v_add_f32 v10, v10, v178\n"
"v_fma_f32 v181, v181, s88, -v2\n"
"v_exp_f32 v180, v180\n"
"v_add_f32 v11, v11, v179\n"
"v_cvt_pk_bf16_f32 v177, v178, v179\n"
"s_waitcnt lgkmcnt(6)\n"
"v_mfma_f32_32x32x16_bf16 a[176:191], a[216:219], v[84:87], a[176:191]\n"
"ds_read_b64_tr_b16 a[200:201], v20 offset:18432\n"
"ds_read_b64_tr_b16 a[202:203], v20 offset:18944\n"
"v_fma_f32 v182, v182, s88, -v2\n"
"v_exp_f32 v181, v181\n"
"v_add_f32 v8, v8, v180\n"
"v_fma_f32 v183, v183, s88, -v2\n"
"v_exp_f32 v182, v182\n"
"v_add_f32 v9, v9, v181\n"
"s_waitcnt lgkmcnt(6)\n"
"v_mfma_f32_32x32x16_bf16 a[224:239], a[220:223], v[84:87], a[224:239]\n"
"ds_read_b64_tr_b16 a[204:205], v21 offset:18432\n"
"ds_read_b64_tr_b16 a[206:207], v21 offset:18944\n"
"v_cvt_pk_bf16_f32 v178, v180, v181\n"
"v_fma_f32 v184, v184, s88, -v2\n"
"v_exp_f32 v183, v183\n"
"v_add_f32 v10, v10, v182\n"
"v_fma_f32 v185, v185, s88, -v2\n"
"s_add_u32 m0, s60, 49152\n"
"s_mov_b32 s87, 229376\n"
"buffer_load_dwordx4 v5, s[48:51], s87 offen nt lds\n"
"s_waitcnt lgkmcnt(6)\n"
"v_mfma_f32_32x32x16_bf16 a[80:95], a[192:195], v[96:99], a[80:95]\n"
"ds_read_b64_tr_b16 a[208:209], v20 offset:20480\n"
"ds_read_b64_tr_b16 a[210:211], v20 offset:20992\n"
"v_exp_f32 v184, v184\n"
"v_add_f32 v11, v11, v183\n"
"v_cvt_pk_bf16_f32 v179, v182, v183\n"
"v_fma_f32 v186, v186, s88, -v2\n"
"v_exp_f32 v185, v185\n"
"s_waitcnt lgkmcnt(6)\n"
"v_mfma_f32_32x32x16_bf16 a[96:111], a[196:199], v[96:99], a[96:111]\n"
"ds_read_b64_tr_b16 a[212:213], v21 offset:20480\n"
"ds_read_b64_tr_b16 a[214:215], v21 offset:20992\n"
"v_add_f32 v8, v8, v184\n"
"v_fma_f32 v187, v187, s88, -v2\n"
"v_exp_f32 v186, v186\n"
"v_add_f32 v9, v9, v185\n"
"v_cvt_pk_bf16_f32 v180, v184, v185\n"
"v_fma_f32 v188, v188, s88, -v2\n"
"s_waitcnt lgkmcnt(6)\n"
"v_mfma_f32_32x32x16_bf16 a[80:95], a[200:203], v[100:103], a[80:95]\n"
"ds_read_b64_tr_b16 a[216:217], v20 offset:22528\n"
"ds_read_b64_tr_b16 a[218:219], v20 offset:23040\n"
"v_exp_f32 v187, v187\n"
"v_add_f32 v10, v10, v186\n"
"v_fma_f32 v189, v189, s88, -v2\n"
"v_exp_f32 v188, v188\n"
"v_add_f32 v11, v11, v187\n"
"s_waitcnt lgkmcnt(6)\n"
"v_mfma_f32_32x32x16_bf16 a[96:111], a[204:207], v[100:103], a[96:111]\n"
"ds_read_b64_tr_b16 a[220:221], v21 offset:22528\n"
"ds_read_b64_tr_b16 a[222:223], v21 offset:23040\n"
"v_cvt_pk_bf16_f32 v181, v186, v187\n"
"v_fma_f32 v190, v190, s88, -v2\n"
"v_exp_f32 v189, v189\n"
"v_add_f32 v8, v8, v188\n"
"v_fma_f32 v191, v191, s88, -v2\n"
"s_add_u32 m0, s60, 53248\n"
"s_mov_b32 s87, 245760\n"
"buffer_load_dwordx4 v5, s[48:51], s87 offen nt lds\n"
"s_waitcnt lgkmcnt(6)\n"
"v_mfma_f32_32x32x16_bf16 a[112:127], a[208:211], v[96:99], a[112:127]\n"
"ds_read_b64_tr_b16 a[192:193], v20 offset:24576\n"
"ds_read_b64_tr_b16 a[194:195], v20 offset:25088\n"
"v_exp_f32 v190, v190\n"
"v_add_f32 v9, v9, v189\n"
"v_cvt_pk_bf16_f32 v182, v188, v189\n"
"v_exp_f32 v191, v191\n"
"v_add_f32 v10, v10, v190\n"
"s_waitcnt lgkmcnt(6)\n"
"v_mfma_f32_32x32x16_bf16 a[128:143], a[212:215], v[96:99], a[128:143]\n"
"ds_read_b64_tr_b16 a[196:197], v21 offset:24576\n"
"ds_read_b64_tr_b16 a[198:199], v21 offset:25088\n"
"v_add_f32 v11, v11, v191\n"
"v_cvt_pk_bf16_f32 v183, v190, v191\n"
"v_fma_f32 v192, v192, s88, -v2\n"
"v_fma_f32 v193, v193, s88, -v2\n"
"v_exp_f32 v192, v192\n"
"v_fma_f32 v194, v194, s88, -v2\n"
"s_waitcnt lgkmcnt(6)\n"
"v_mfma_f32_32x32x16_bf16 a[112:127], a[216:219], v[100:103], a[112:127]\n"
"ds_read_b64_tr_b16 a[200:201], v20 offset:26624\n"
"ds_read_b64_tr_b16 a[202:203], v20 offset:27136\n"
"v_exp_f32 v193, v193\n"
"v_add_f32 v8, v8, v192\n"
"v_fma_f32 v195, v195, s88, -v2\n"
"v_exp_f32 v194, v194\n"
"v_add_f32 v9, v9, v193\n"
"s_waitcnt lgkmcnt(6)\n"
"v_mfma_f32_32x32x16_bf16 a[128:143], a[220:223], v[100:103], a[128:143]\n"
"ds_read_b64_tr_b16 a[204:205], v21 offset:26624\n"
"ds_read_b64_tr_b16 a[206:207], v21 offset:27136\n"
"v_cvt_pk_bf16_f32 v192, v192, v193\n"
"v_fma_f32 v196, v196, s88, -v2\n"
"v_exp_f32 v195, v195\n"
"v_add_f32 v10, v10, v194\n"
"v_fma_f32 v197, v197, s88, -v2\n"
"s_waitcnt lgkmcnt(6)\n"
"v_mfma_f32_32x32x16_bf16 a[144:159], a[192:195], v[96:99], a[144:159]\n"
"ds_read_b64_tr_b16 a[208:209], v20 offset:28672\n"
"ds_read_b64_tr_b16 a[210:211], v20 offset:29184\n"
"v_exp_f32 v196, v196\n"
"v_add_f32 v11, v11, v195\n"
"v_cvt_pk_bf16_f32 v193, v194, v195\n"
"v_fma_f32 v198, v198, s88, -v2\n"
"v_exp_f32 v197, v197\n"
"v_add_f32 v8, v8, v196\n"
"s_add_u32 m0, s60, 57344\n"
"s_mov_b32 s87, 163968\n"
"buffer_load_dwordx4 v5, s[48:51], s87 offen nt lds\n"
"s_waitcnt lgkmcnt(6)\n"
"v_mfma_f32_32x32x16_bf16 a[160:175], a[196:199], v[96:99], a[160:175]\n"
"ds_read_b64_tr_b16 a[212:213], v21 offset:28672\n"
"ds_read_b64_tr_b16 a[214:215], v21 offset:29184\n"
"v_fma_f32 v199, v199, s88, -v2\n"
"v_exp_f32 v198, v198\n"
"v_add_f32 v9, v9, v197\n"
"v_cvt_pk_bf16_f32 v194, v196, v197\n"
"v_fma_f32 v200, v200, s88, -v2\n"
"s_waitcnt lgkmcnt(6)\n"
"v_mfma_f32_32x32x16_bf16 a[144:159], a[200:203], v[100:103], a[144:159]\n"
"ds_read_b64_tr_b16 a[216:217], v20 offset:30720\n"
"ds_read_b64_tr_b16 a[218:219], v20 offset:31232\n"
"v_exp_f32 v199, v199\n"
"v_add_f32 v10, v10, v198\n"
"v_fma_f32 v201, v201, s88, -v2\n"
"v_exp_f32 v200, v200\n"
"v_add_f32 v11, v11, v199\n"
"s_waitcnt lgkmcnt(6)\n"
"v_mfma_f32_32x32x16_bf16 a[160:175], a[204:207], v[100:103], a[160:175]\n"
"ds_read_b64_tr_b16 a[220:221], v21 offset:30720\n"
"ds_read_b64_tr_b16 a[222:223], v21 offset:31232\n"
"v_mov_b32 v20, v7\n"
"v_xor_b32 v21, 64, v20\n"
"v_cvt_pk_bf16_f32 v195, v198, v199\n"
"v_fma_f32 v202, v202, s88, -v2\n"
"v_exp_f32 v201, v201\n"
"v_add_f32 v8, v8, v200\n"
"v_fma_f32 v203, v203, s88, -v2\n"
"s_waitcnt lgkmcnt(6)\n"
"v_mfma_f32_32x32x16_bf16 a[176:191], a[208:211], v[96:99], a[176:191]\n"
"ds_read_b64_tr_b16 a[192:193], v20 offset:0\n"
"ds_read_b64_tr_b16 a[194:195], v20 offset:512\n"
"v_exp_f32 v202, v202\n"
"v_add_f32 v9, v9, v201\n"
"v_cvt_pk_bf16_f32 v196, v200, v201\n"
"v_fma_f32 v204, v204, s88, -v2\n"
"v_exp_f32 v203, v203\n"
"v_add_f32 v10, v10, v202\n"
"s_waitcnt lgkmcnt(6)\n"
"v_mfma_f32_32x32x16_bf16 a[224:239], a[212:215], v[96:99], a[224:239]\n"
"ds_read_b64_tr_b16 a[196:197], v21 offset:0\n"
"ds_read_b64_tr_b16 a[198:199], v21 offset:512\n"
"v_fma_f32 v205, v205, s88, -v2\n"
"v_exp_f32 v204, v204\n"
"v_add_f32 v11, v11, v203\n"
"v_cvt_pk_bf16_f32 v197, v202, v203\n"
"v_fma_f32 v206, v206, s88, -v2\n"
"s_add_u32 m0, s60, 61440\n"
"s_mov_b32 s87, 180352\n"
"buffer_load_dwordx4 v5, s[48:51], s87 offen nt lds\n"
"s_waitcnt lgkmcnt(6)\n"
"v_mfma_f32_32x32x16_bf16 a[176:191], a[216:219], v[100:103], a[176:191]\n"
"ds_read_b64_tr_b16 a[200:201], v20 offset:2048\n"
"ds_read_b64_tr_b16 a[202:203], v20 offset:2560\n"
"v_exp_f32 v205, v205\n"
"v_add_f32 v8, v8, v204\n"
"v_fma_f32 v207, v207, s88, -v2\n"
"v_exp_f32 v206, v206\n"
"v_add_f32 v9, v9, v205\n"
"s_waitcnt lgkmcnt(6)\n"
"v_mfma_f32_32x32x16_bf16 a[224:239], a[220:223], v[100:103], a[224:239]\n"
"ds_read_b64_tr_b16 a[204:205], v21 offset:2048\n"
"ds_read_b64_tr_b16 a[206:207], v21 offset:2560\n"
"v_cvt_pk_bf16_f32 v198, v204, v205\n"
"v_exp_f32 v207, v207\n"
"v_add_f32 v10, v10, v206\n"
"v_add_f32 v11, v11, v207\n"
"v_cvt_pk_bf16_f32 v199, v206, v207\n"
"s_nop 1\n"
"s_waitcnt lgkmcnt(6)\n"
"v_mfma_f32_32x32x16_bf16 a[80:95], a[192:195], v[128:131], a[80:95]\n"
"s_waitcnt vmcnt(7)\n"
"s_barrier\n"
"ds_read_b64_tr_b16 a[208:209], v20 offset:4096\n"
"ds_read_b64_tr_b16 a[210:211], v20 offset:4608\n"
"v_fma_f32 v208, v208, s88, -v2\n"
"v_fma_f32 v209, v209, s88, -v2\n"
"v_exp_f32 v208, v208\n"
"v_fma_f32 v210, v210, s88, -v2\n"
"v_exp_f32 v209, v209\n"
"s_add_u32 m0, s60, 65536\n"
"s_mov_b32 s87, 196736\n"
"buffer_load_dwordx4 v5, s[48:51], s87 offen nt lds\n"
"s_waitcnt lgkmcnt(6)\n"
"v_mfma_f32_32x32x16_bf16 a[96:111], a[196:199], v[128:131], a[96:111]\n"
"ds_read_b64_tr_b16 a[212:213], v21 offset:4096\n"
"ds_read_b64_tr_b16 a[214:215], v21 offset:4608\n"
"v_add_f32 v8, v8, v208\n"
"v_fma_f32 v211, v211, s88, -v2\n"
"v_exp_f32 v210, v210\n"
"v_add_f32 v9, v9, v209\n"
"v_cvt_pk_bf16_f32 v208, v208, v209\n"
"s_waitcnt lgkmcnt(6)\n"
"v_mfma_f32_32x32x16_bf16 a[80:95], a[200:203], v[132:135], a[80:95]\n"
"ds_read_b64_tr_b16 a[216:217], v20 offset:6144\n"
"ds_read_b64_tr_b16 a[218:219], v20 offset:6656\n"
"v_fma_f32 v212, v212, s88, -v2\n"
"v_exp_f32 v211, v211\n"
"v_add_f32 v10, v10, v210\n"
"v_fma_f32 v213, v213, s88, -v2\n"
"v_exp_f32 v212, v212\n"
"s_add_u32 m0, s60, 131072\n"
"s_add_u32 s87, s72, 0\n"
"buffer_load_dwordx4 v6, s[52:55], s87 offen lds\n"
"s_waitcnt lgkmcnt(6)\n"
"v_mfma_f32_32x32x16_bf16 a[96:111], a[204:207], v[132:135], a[96:111]\n"
"ds_read_b64_tr_b16 a[220:221], v21 offset:6144\n"
"ds_read_b64_tr_b16 a[222:223], v21 offset:6656\n"
"v_add_f32 v11, v11, v211\n"
"v_cvt_pk_bf16_f32 v209, v210, v211\n"
"v_fma_f32 v214, v214, s88, -v2\n"
"v_exp_f32 v213, v213\n"
"v_add_f32 v8, v8, v212\n"
"v_fma_f32 v215, v215, s88, -v2\n"
"s_waitcnt lgkmcnt(6)\n"
"v_mfma_f32_32x32x16_bf16 a[112:127], a[208:211], v[128:131], a[112:127]\n"
"ds_read_b64_tr_b16 a[192:193], v20 offset:8192\n"
"ds_read_b64_tr_b16 a[194:195], v20 offset:8704\n"
"v_exp_f32 v214, v214\n"
"v_add_f32 v9, v9, v213\n"
"v_cvt_pk_bf16_f32 v210, v212, v213\n"
"v_fma_f32 v216, v216, s88, -v2\n"
"v_exp_f32 v215, v215\n"
"s_add_u32 m0, s60, 69632\n"
"s_mov_b32 s87, 213120\n"
"buffer_load_dwordx4 v5, s[48:51], s87 offen nt lds\n"
"s_waitcnt lgkmcnt(6)\n"
"v_mfma_f32_32x32x16_bf16 a[128:143], a[212:215], v[128:131], a[128:143]\n"
"ds_read_b64_tr_b16 a[196:197], v21 offset:8192\n"
"ds_read_b64_tr_b16 a[198:199], v21 offset:8704\n"
"v_add_f32 v10, v10, v214\n"
"v_fma_f32 v217, v217, s88, -v2\n"
"v_exp_f32 v216, v216\n"
"v_add_f32 v11, v11, v215\n"
"v_cvt_pk_bf16_f32 v211, v214, v215\n"
"s_waitcnt lgkmcnt(6)\n"
"v_mfma_f32_32x32x16_bf16 a[112:127], a[216:219], v[132:135], a[112:127]\n"
"ds_read_b64_tr_b16 a[200:201], v20 offset:10240\n"
"ds_read_b64_tr_b16 a[202:203], v20 offset:10752\n"
"v_fma_f32 v218, v218, s88, -v2\n"
"v_exp_f32 v217, v217\n"
"v_add_f32 v8, v8, v216\n"
"v_fma_f32 v219, v219, s88, -v2\n"
"v_exp_f32 v218, v218\n"
"s_add_u32 m0, s60, 135168\n"
"s_add_u32 s87, s72, 28672\n"
"buffer_load_dwordx4 v6, s[52:55], s87 offen lds\n"
"s_waitcnt lgkmcnt(6)\n"
"v_mfma_f32_32x32x16_bf16 a[128:143], a[220:223], v[132:135], a[128:143]\n"
"ds_read_b64_tr_b16 a[204:205], v21 offset:10240\n"
"ds_read_b64_tr_b16 a[206:207], v21 offset:10752\n"
"v_add_f32 v9, v9, v217\n"
"v_cvt_pk_bf16_f32 v212, v216, v217\n"
"v_fma_f32 v220, v220, s88, -v2\n"
"v_exp_f32 v219, v219\n"
"v_add_f32 v10, v10, v218\n"
"v_fma_f32 v221, v221, s88, -v2\n"
"s_waitcnt lgkmcnt(6)\n"
"v_mfma_f32_32x32x16_bf16 a[144:159], a[192:195], v[128:131], a[144:159]\n"
"ds_read_b64_tr_b16 a[208:209], v20 offset:12288\n"
"ds_read_b64_tr_b16 a[210:211], v20 offset:12800\n"
"v_exp_f32 v220, v220\n"
"v_add_f32 v11, v11, v219\n"
"v_cvt_pk_bf16_f32 v213, v218, v219\n"
"v_fma_f32 v222, v222, s88, -v2\n"
"v_exp_f32 v221, v221\n"
"s_add_u32 m0, s60, 73728\n"
"s_mov_b32 s87, 229504\n"
"buffer_load_dwordx4 v5, s[48:51], s87 offen nt lds\n"
"s_waitcnt lgkmcnt(6)\n"
"v_mfma_f32_32x32x16_bf16 a[160:175], a[196:199], v[128:131], a[160:175]\n"
"ds_read_b64_tr_b16 a[212:213], v21 offset:12288\n"
"ds_read_b64_tr_b16 a[214:215], v21 offset:12800\n"
"v_add_f32 v8, v8, v220\n"
"v_fma_f32 v223, v223, s88, -v2\n"
"v_exp_f32 v222, v222\n"
"v_add_f32 v9, v9, v221\n"
"v_cvt_pk_bf16_f32 v214, v220, v221\n"
"s_waitcnt lgkmcnt(6)\n"
"v_mfma_f32_32x32x16_bf16 a[144:159], a[200:203], v[132:135], a[144:159]\n"
"ds_read_b64_tr_b16 a[216:217], v20 offset:14336\n"
"ds_read_b64_tr_b16 a[218:219], v20 offset:14848\n"
"v_exp_f32 v223, v223\n"
"v_add_f32 v10, v10, v222\n"
"v_add_f32 v11, v11, v223\n"
"v_cvt_pk_bf16_f32 v215, v222, v223\n"
"v_fma_f32 v224, v224, s88, -v2\n"
"v_fma_f32 v225, v225, s88, -v2\n"
"s_add_u32 m0, s60, 139264\n"
"s_add_u32 s87, s72, 57344\n"
"buffer_load_dwordx4 v6, s[52:55], s87 offen lds\n"
"s_waitcnt lgkmcnt(6)\n"
"v_mfma_f32_32x32x16_bf16 a[160:175], a[204:207], v[132:135], a[160:175]\n"
"ds_read_b64_tr_b16 a[220:221], v21 offset:14336\n"
"ds_read_b64_tr_b16 a[222:223], v21 offset:14848\n"
"v_exp_f32 v224, v224\n"
"v_fma_f32 v226, v226, s88, -v2\n"
"v_exp_f32 v225, v225\n"
"v_add_f32 v8, v8, v224\n"
"v_fma_f32 v227, v227, s88, -v2\n"
"s_waitcnt lgkmcnt(6)\n"
"v_mfma_f32_32x32x16_bf16 a[176:191], a[208:211], v[128:131], a[176:191]\n"
"ds_read_b64_tr_b16 a[192:193], v20 offset:16384\n"
"ds_read_b64_tr_b16 a[194:195], v20 offset:16896\n"
"v_exp_f32 v226, v226\n"
"v_add_f32 v9, v9, v225\n"
"v_cvt_pk_bf16_f32 v224, v224, v225\n"
"v_fma_f32 v228, v228, s88, -v2\n"
"v_exp_f32 v227, v227\n"
"s_add_u32 m0, s60, 77824\n"
"s_mov_b32 s87, 245888\n"
"buffer_load_dwordx4 v5, s[48:51], s87 offen nt lds\n"
"s_waitcnt lgkmcnt(6)\n"
"v_mfma_f32_32x32x16_bf16 a[224:239], a[212:215], v[128:131], a[224:239]\n"
"ds_read_b64_tr_b16 a[196:197], v21 offset:16384\n"
"ds_read_b64_tr_b16 a[198:199], v21 offset:16896\n"
"v_add_f32 v10, v10, v226\n"
"v_fma_f32 v229, v229, s88, -v2\n"
"v_exp_f32 v228, v228\n"
"v_add_f32 v11, v11, v227\n"
"v_cvt_pk_bf16_f32 v225, v226, v227\n"
"s_waitcnt lgkmcnt(6)\n"
"v_mfma_f32_32x32x16_bf16 a[176:191], a[216:219], v[132:135], a[176:191]\n"
"ds_read_b64_tr_b16 a[200:201], v20 offset:18432\n"
"ds_read_b64_tr_b16 a[202:203], v20 offset:18944\n"
"v_fma_f32 v230, v230, s88, -v2\n"
"v_exp_f32 v229, v229\n"
"v_add_f32 v8, v8, v228\n"
"v_fma_f32 v231, v231, s88, -v2\n"
"v_exp_f32 v230, v230\n"
"v_add_f32 v9, v9, v229\n"
"s_waitcnt lgkmcnt(6)\n"
"v_mfma_f32_32x32x16_bf16 a[224:239], a[220:223], v[132:135], a[224:239]\n"
"ds_read_b64_tr_b16 a[204:205], v21 offset:18432\n"
"ds_read_b64_tr_b16 a[206:207], v21 offset:18944\n"
"v_cvt_pk_bf16_f32 v226, v228, v229\n"
"v_fma_f32 v232, v232, s88, -v2\n"
"v_exp_f32 v231, v231\n"
"v_add_f32 v10, v10, v230\n"
"v_fma_f32 v233, v233, s88, -v2\n"
"s_add_u32 m0, s60, 81920\n"
"s_mov_b32 s87, 164096\n"
"buffer_load_dwordx4 v5, s[48:51], s87 offen nt lds\n"
"s_waitcnt lgkmcnt(6)\n"
"v_mfma_f32_32x32x16_bf16 a[80:95], a[192:195], v[144:147], a[80:95]\n"
"ds_read_b64_tr_b16 a[208:209], v20 offset:20480\n"
"ds_read_b64_tr_b16 a[210:211], v20 offset:20992\n"
"v_exp_f32 v232, v232\n"
"v_add_f32 v11, v11, v231\n"
"v_cvt_pk_bf16_f32 v227, v230, v231\n"
"v_fma_f32 v234, v234, s88, -v2\n"
"v_exp_f32 v233, v233\n"
"s_waitcnt lgkmcnt(6)\n"
"v_mfma_f32_32x32x16_bf16 a[96:111], a[196:199], v[144:147], a[96:111]\n"
"ds_read_b64_tr_b16 a[212:213], v21 offset:20480\n"
"ds_read_b64_tr_b16 a[214:215], v21 offset:20992\n"
"v_add_f32 v8, v8, v232\n"
"v_fma_f32 v235, v235, s88, -v2\n"
"v_exp_f32 v234, v234\n"
"v_add_f32 v9, v9, v233\n"
"v_cvt_pk_bf16_f32 v228, v232, v233\n"
"v_fma_f32 v236, v236, s88, -v2\n"
"s_add_u32 m0, s60, 143360\n"
"s_add_u32 s87, s72, 86016\n"
"buffer_load_dwordx4 v6, s[52:55], s87 offen lds\n"
"s_waitcnt lgkmcnt(6)\n"
"v_mfma_f32_32x32x16_bf16 a[80:95], a[200:203], v[148:151], a[80:95]\n"
"ds_read_b64_tr_b16 a[216:217], v20 offset:22528\n"
"ds_read_b64_tr_b16 a[218:219], v20 offset:23040\n"
"v_exp_f32 v235, v235\n"
"v_add_f32 v10, v10, v234\n"
"v_fma_f32 v237, v237, s88, -v2\n"
"v_exp_f32 v236, v236\n"
"v_add_f32 v11, v11, v235\n"
"s_waitcnt lgkmcnt(6)\n"
"v_mfma_f32_32x32x16_bf16 a[96:111], a[204:207], v[148:151], a[96:111]\n"
"ds_read_b64_tr_b16 a[220:221], v21 offset:22528\n"
"ds_read_b64_tr_b16 a[222:223], v21 offset:23040\n"
"v_cvt_pk_bf16_f32 v229, v234, v235\n"
"v_fma_f32 v238, v238, s88, -v2\n"
"v_exp_f32 v237, v237\n"
"v_add_f32 v8, v8, v236\n"
"v_fma_f32 v239, v239, s88, -v2\n"
"s_add_u32 m0, s60, 86016\n"
"s_mov_b32 s87, 180480\n"
"buffer_load_dwordx4 v5, s[48:51], s87 offen nt lds\n"
"s_waitcnt lgkmcnt(6)\n"
"v_mfma_f32_32x32x16_bf16 a[112:127], a[208:211], v[144:147], a[112:127]\n"
"ds_read_b64_tr_b16 a[192:193], v20 offset:24576\n"
"ds_read_b64_tr_b16 a[194:195], v20 offset:25088\n"
"v_exp_f32 v238, v238\n"
"v_add_f32 v9, v9, v237\n"
"v_cvt_pk_bf16_f32 v230, v236, v237\n"
"v_exp_f32 v239, v239\n"
"v_add_f32 v10, v10, v238\n"
"s_waitcnt lgkmcnt(6)\n"
"v_mfma_f32_32x32x16_bf16 a[128:143], a[212:215], v[144:147], a[128:143]\n"
"ds_read_b64_tr_b16 a[196:197], v21 offset:24576\n"
"ds_read_b64_tr_b16 a[198:199], v21 offset:25088\n"
"v_add_f32 v11, v11, v239\n"
"v_cvt_pk_bf16_f32 v231, v238, v239\n"
"v_fma_f32 v240, v240, s88, -v2\n"
"v_fma_f32 v241, v241, s88, -v2\n"
"v_exp_f32 v240, v240\n"
"v_fma_f32 v242, v242, s88, -v2\n"
"s_add_u32 m0, s60, 147456\n"
"s_add_u32 s87, s72, 114688\n"
"buffer_load_dwordx4 v6, s[52:55], s87 offen lds\n"
"s_waitcnt lgkmcnt(6)\n"
"v_mfma_f32_32x32x16_bf16 a[112:127], a[216:219], v[148:151], a[112:127]\n"
"ds_read_b64_tr_b16 a[200:201], v20 offset:26624\n"
"ds_read_b64_tr_b16 a[202:203], v20 offset:27136\n"
"v_exp_f32 v241, v241\n"
"v_add_f32 v8, v8, v240\n"
"v_fma_f32 v243, v243, s88, -v2\n"
"v_exp_f32 v242, v242\n"
"v_add_f32 v9, v9, v241\n"
"s_waitcnt lgkmcnt(6)\n"
"v_mfma_f32_32x32x16_bf16 a[128:143], a[220:223], v[148:151], a[128:143]\n"
"ds_read_b64_tr_b16 a[204:205], v21 offset:26624\n"
"ds_read_b64_tr_b16 a[206:207], v21 offset:27136\n"
"v_cvt_pk_bf16_f32 v240, v240, v241\n"
"v_fma_f32 v244, v244, s88, -v2\n"
"v_exp_f32 v243, v243\n"
"v_add_f32 v10, v10, v242\n"
"v_fma_f32 v245, v245, s88, -v2\n"
"s_waitcnt lgkmcnt(6)\n"
"v_mfma_f32_32x32x16_bf16 a[144:159], a[192:195], v[144:147], a[144:159]\n"
"ds_read_b64_tr_b16 a[208:209], v20 offset:28672\n"
"ds_read_b64_tr_b16 a[210:211], v20 offset:29184\n"
"v_exp_f32 v244, v244\n"
"v_add_f32 v11, v11, v243\n"
"v_cvt_pk_bf16_f32 v241, v242, v243\n"
"v_fma_f32 v246, v246, s88, -v2\n"
"v_exp_f32 v245, v245\n"
"v_add_f32 v8, v8, v244\n"
"s_add_u32 m0, s60, 90112\n"
"s_mov_b32 s87, 196864\n"
"buffer_load_dwordx4 v5, s[48:51], s87 offen nt lds\n"
"s_waitcnt lgkmcnt(6)\n"
"v_mfma_f32_32x32x16_bf16 a[160:175], a[196:199], v[144:147], a[160:175]\n"
"ds_read_b64_tr_b16 a[212:213], v21 offset:28672\n"
"ds_read_b64_tr_b16 a[214:215], v21 offset:29184\n"
"v_fma_f32 v247, v247, s88, -v2\n"
"v_exp_f32 v246, v246\n"
"v_add_f32 v9, v9, v245\n"
"v_cvt_pk_bf16_f32 v242, v244, v245\n"
"v_fma_f32 v248, v248, s88, -v2\n"
"s_waitcnt lgkmcnt(6)\n"
"v_mfma_f32_32x32x16_bf16 a[144:159], a[200:203], v[148:151], a[144:159]\n"
"ds_read_b64_tr_b16 a[216:217], v20 offset:30720\n"
"ds_read_b64_tr_b16 a[218:219], v20 offset:31232\n"
"v_exp_f32 v247, v247\n"
"v_add_f32 v10, v10, v246\n"
"v_fma_f32 v249, v249, s88, -v2\n"
"v_exp_f32 v248, v248\n"
"v_add_f32 v11, v11, v247\n"
"s_add_u32 m0, s60, 151552\n"
"s_add_u32 s87, s72, 143360\n"
"buffer_load_dwordx4 v6, s[52:55], s87 offen lds\n"
"s_waitcnt lgkmcnt(6)\n"
"v_mfma_f32_32x32x16_bf16 a[160:175], a[204:207], v[148:151], a[160:175]\n"
"ds_read_b64_tr_b16 a[220:221], v21 offset:30720\n"
"ds_read_b64_tr_b16 a[222:223], v21 offset:31232\n"
"v_add_u32 v20, 32768, v7\n"
"v_xor_b32 v21, 64, v20\n"
"v_cvt_pk_bf16_f32 v243, v246, v247\n"
"v_fma_f32 v250, v250, s88, -v2\n"
"v_exp_f32 v249, v249\n"
"v_add_f32 v8, v8, v248\n"
"v_fma_f32 v251, v251, s88, -v2\n"
"s_waitcnt lgkmcnt(6)\n"
"v_mfma_f32_32x32x16_bf16 a[176:191], a[208:211], v[144:147], a[176:191]\n"
"ds_read_b64_tr_b16 a[192:193], v20 offset:0\n"
"ds_read_b64_tr_b16 a[194:195], v20 offset:512\n"
"v_exp_f32 v250, v250\n"
"v_add_f32 v9, v9, v249\n"
"v_cvt_pk_bf16_f32 v244, v248, v249\n"
"v_fma_f32 v252, v252, s88, -v2\n"
"v_exp_f32 v251, v251\n"
"v_add_f32 v10, v10, v250\n"
"s_waitcnt lgkmcnt(6)\n"
"v_mfma_f32_32x32x16_bf16 a[224:239], a[212:215], v[144:147], a[224:239]\n"
"ds_read_b64_tr_b16 a[196:197], v21 offset:0\n"
"ds_read_b64_tr_b16 a[198:199], v21 offset:512\n"
"v_fma_f32 v253, v253, s88, -v2\n"
"v_exp_f32 v252, v252\n"
"v_add_f32 v11, v11, v251\n"
"v_cvt_pk_bf16_f32 v245, v250, v251\n"
"v_fma_f32 v254, v254, s88, -v2\n"
"s_add_u32 m0, s60, 94208\n"
"s_mov_b32 s87, 213248\n"
"buffer_load_dwordx4 v5, s[48:51], s87 offen nt lds\n"
"s_waitcnt lgkmcnt(6)\n"
"v_mfma_f32_32x32x16_bf16 a[176:191], a[216:219], v[148:151], a[176:191]\n"
"ds_read_b64_tr_b16 a[200:201], v20 offset:2048\n"
"ds_read_b64_tr_b16 a[202:203], v20 offset:2560\n"
"v_exp_f32 v253, v253\n"
"v_add_f32 v8, v8, v252\n"
"v_fma_f32 v255, v255, s88, -v2\n"
"v_exp_f32 v254, v254\n"
"v_add_f32 v9, v9, v253\n"
"s_waitcnt lgkmcnt(6)\n"
"v_mfma_f32_32x32x16_bf16 a[224:239], a[220:223], v[148:151], a[224:239]\n"
"ds_read_b64_tr_b16 a[204:205], v21 offset:2048\n"
"ds_read_b64_tr_b16 a[206:207], v21 offset:2560\n"
"v_cvt_pk_bf16_f32 v246, v252, v253\n"
"v_exp_f32 v255, v255\n"
"v_add_f32 v10, v10, v254\n"
"v_add_f32 v11, v11, v255\n"
"v_cvt_pk_bf16_f32 v247, v254, v255\n"
"s_nop 1\n"
"s_add_u32 m0, s60, 155648\n"
"s_add_u32 s87, s72, 172032\n"
"buffer_load_dwordx4 v6, s[52:55], s87 offen lds\n"
"s_waitcnt lgkmcnt(6)\n"
"v_mfma_f32_32x32x16_bf16 a[80:95], a[192:195], v[160:163], a[80:95]\n"
"s_waitcnt vmcnt(14)\n"
"s_barrier\n"
"ds_read_b64_tr_b16 a[208:209], v20 offset:4096\n"
"ds_read_b64_tr_b16 a[210:211], v20 offset:4608\n"
"s_add_u32 m0, s60, 98304\n"
"s_mov_b32 s87, 229632\n"
"buffer_load_dwordx4 v5, s[48:51], s87 offen nt lds\n"
"s_waitcnt lgkmcnt(6)\n"
"v_mfma_f32_32x32x16_bf16 a[96:111], a[196:199], v[160:163], a[96:111]\n"
"ds_read_b64_tr_b16 a[212:213], v21 offset:4096\n"
"ds_read_b64_tr_b16 a[214:215], v21 offset:4608\n"
"v_pk_add_f32 v[8:9], v[8:9], v[10:11]\n"
"s_waitcnt lgkmcnt(6)\n"
"v_mfma_f32_32x32x16_bf16 a[80:95], a[200:203], v[164:167], a[80:95]\n"
"ds_read_b64_tr_b16 a[216:217], v20 offset:6144\n"
"ds_read_b64_tr_b16 a[218:219], v20 offset:6656\n"
"v_add_f32 v24, v8, v9\n"
"s_add_u32 m0, s60, 159744\n"
"s_add_u32 s87, s72, 200704\n"
"buffer_load_dwordx4 v6, s[52:55], s87 offen lds\n"
"s_waitcnt lgkmcnt(6)\n"
"v_mfma_f32_32x32x16_bf16 a[96:111], a[204:207], v[164:167], a[96:111]\n"
"ds_read_b64_tr_b16 a[220:221], v21 offset:6144\n"
"ds_read_b64_tr_b16 a[222:223], v21 offset:6656\n"
"v_mov_b32 v28, v24\n"
"s_nop 1\n"
"v_permlane32_swap_b32 v24, v28\n"
"s_nop 1\n"
"v_add_f32 v24, v24, v28\n"
"s_waitcnt lgkmcnt(6)\n"
"v_mfma_f32_32x32x16_bf16 a[80:95], a[208:211], v[176:179], a[80:95]\n"
"ds_read_b64_tr_b16 a[192:193], v20 offset:8192\n"
"ds_read_b64_tr_b16 a[194:195], v20 offset:8704\n"
"s_add_u32 m0, s60, 102400\n"
"s_mov_b32 s87, 246016\n"
"buffer_load_dwordx4 v5, s[48:51], s87 offen nt lds\n"
"s_waitcnt lgkmcnt(6)\n"
"v_mfma_f32_32x32x16_bf16 a[96:111], a[212:215], v[176:179], a[96:111]\n"
"ds_read_b64_tr_b16 a[196:197], v21 offset:8192\n"
"ds_read_b64_tr_b16 a[198:199], v21 offset:8704\n"
"v_rcp_f32 v2, v24\n"
"s_waitcnt lgkmcnt(6)\n"
"v_mfma_f32_32x32x16_bf16 a[80:95], a[216:219], v[180:183], a[80:95]\n"
"ds_read_b64_tr_b16 a[200:201], v20 offset:10240\n"
"ds_read_b64_tr_b16 a[202:203], v20 offset:10752\n"
"s_nop 0\n"
"s_add_u32 m0, s60, 0\n"
"s_add_u32 s87, s73, 0\n"
"buffer_load_dwordx4 v6, s[52:55], s87 offen lds\n"
"s_waitcnt lgkmcnt(6)\n"
"v_mfma_f32_32x32x16_bf16 a[96:111], a[220:223], v[180:183], a[96:111]\n"
"ds_read_b64_tr_b16 a[204:205], v21 offset:10240\n"
"ds_read_b64_tr_b16 a[206:207], v21 offset:10752\n"
"v_mov_b32 v3, v2\n"
"s_waitcnt lgkmcnt(6)\n"
"v_mfma_f32_32x32x16_bf16 a[80:95], a[192:195], v[192:195], a[80:95]\n"
"ds_read_b64_tr_b16 a[208:209], v20 offset:12288\n"
"ds_read_b64_tr_b16 a[210:211], v20 offset:12800\n"
"s_add_u32 m0, s60, 106496\n"
"s_mov_b32 s87, 164224\n"
"buffer_load_dwordx4 v5, s[48:51], s87 offen nt lds\n"
"s_waitcnt lgkmcnt(6)\n"
"v_mfma_f32_32x32x16_bf16 a[96:111], a[196:199], v[192:195], a[96:111]\n"
"ds_read_b64_tr_b16 a[212:213], v21 offset:12288\n"
"ds_read_b64_tr_b16 a[214:215], v21 offset:12800\n"
"s_waitcnt lgkmcnt(6)\n"
"v_mfma_f32_32x32x16_bf16 a[80:95], a[200:203], v[196:199], a[80:95]\n"
"ds_read_b64_tr_b16 a[216:217], v20 offset:14336\n"
"ds_read_b64_tr_b16 a[218:219], v20 offset:14848\n"
"s_add_u32 m0, s60, 4096\n"
"s_add_u32 s87, s73, 28672\n"
"buffer_load_dwordx4 v6, s[52:55], s87 offen lds\n"
"s_waitcnt lgkmcnt(6)\n"
"v_mfma_f32_32x32x16_bf16 a[96:111], a[204:207], v[196:199], a[96:111]\n"
"ds_read_b64_tr_b16 a[220:221], v21 offset:14336\n"
"ds_read_b64_tr_b16 a[222:223], v21 offset:14848\n"
"s_waitcnt lgkmcnt(6)\n"
"v_mfma_f32_32x32x16_bf16 a[80:95], a[208:211], v[208:211], a[80:95]\n"
"ds_read_b64_tr_b16 a[192:193], v20 offset:16384\n"
"ds_read_b64_tr_b16 a[194:195], v20 offset:16896\n"
"s_waitcnt lgkmcnt(6)\n"
"v_mfma_f32_32x32x16_bf16 a[96:111], a[212:215], v[208:211], a[96:111]\n"
"ds_read_b64_tr_b16 a[196:197], v21 offset:16384\n"
"ds_read_b64_tr_b16 a[198:199], v21 offset:16896\n"
"s_add_u32 m0, s60, 110592\n"
"s_mov_b32 s87, 180608\n"
"buffer_load_dwordx4 v5, s[48:51], s87 offen nt lds\n"
"s_waitcnt lgkmcnt(6)\n"
"v_mfma_f32_32x32x16_bf16 a[80:95], a[216:219], v[212:215], a[80:95]\n"
"ds_read_b64_tr_b16 a[200:201], v20 offset:18432\n"
"ds_read_b64_tr_b16 a[202:203], v20 offset:18944\n"
"s_waitcnt lgkmcnt(6)\n"
"v_mfma_f32_32x32x16_bf16 a[96:111], a[220:223], v[212:215], a[96:111]\n"
"ds_read_b64_tr_b16 a[204:205], v21 offset:18432\n"
"ds_read_b64_tr_b16 a[206:207], v21 offset:18944\n"
"s_waitcnt lgkmcnt(6)\n"
"v_mfma_f32_32x32x16_bf16 a[80:95], a[192:195], v[224:227], a[80:95]\n"
"ds_read_b64_tr_b16 a[208:209], v20 offset:20480\n"
"ds_read_b64_tr_b16 a[210:211], v20 offset:20992\n"
"s_waitcnt lgkmcnt(6)\n"
"v_mfma_f32_32x32x16_bf16 a[96:111], a[196:199], v[224:227], a[96:111]\n"
"ds_read_b64_tr_b16 a[212:213], v21 offset:20480\n"
"ds_read_b64_tr_b16 a[214:215], v21 offset:20992\n"
"s_waitcnt lgkmcnt(6)\n"
"v_mfma_f32_32x32x16_bf16 a[80:95], a[200:203], v[228:231], a[80:95]\n"
"ds_read_b64_tr_b16 a[216:217], v20 offset:22528\n"
"ds_read_b64_tr_b16 a[218:219], v20 offset:23040\n"
"s_waitcnt lgkmcnt(6)\n"
"v_mfma_f32_32x32x16_bf16 a[96:111], a[204:207], v[228:231], a[96:111]\n"
"ds_read_b64_tr_b16 a[220:221], v21 offset:22528\n"
"ds_read_b64_tr_b16 a[222:223], v21 offset:23040\n"
"s_add_u32 m0, s60, 114688\n"
"s_mov_b32 s87, 196992\n"
"buffer_load_dwordx4 v5, s[48:51], s87 offen nt lds\n"
"s_waitcnt lgkmcnt(6)\n"
"v_mfma_f32_32x32x16_bf16 a[80:95], a[208:211], v[240:243], a[80:95]\n"
"ds_read_b64_tr_b16 a[192:193], v20 offset:24576\n"
"ds_read_b64_tr_b16 a[194:195], v20 offset:25088\n"
"s_waitcnt lgkmcnt(6)\n"
"v_mfma_f32_32x32x16_bf16 a[96:111], a[212:215], v[240:243], a[96:111]\n"
"ds_read_b64_tr_b16 a[196:197], v21 offset:24576\n"
"ds_read_b64_tr_b16 a[198:199], v21 offset:25088\n"
"s_add_u32 m0, s60, 8192\n"
"s_add_u32 s87, s73, 57344\n"
"buffer_load_dwordx4 v6, s[52:55], s87 offen lds\n"
"s_waitcnt lgkmcnt(6)\n"
"v_mfma_f32_32x32x16_bf16 a[80:95], a[216:219], v[244:247], a[80:95]\n"
"ds_read_b64_tr_b16 a[200:201], v20 offset:26624\n"
"ds_read_b64_tr_b16 a[202:203], v20 offset:27136\n"
"s_waitcnt lgkmcnt(6)\n"
"v_mfma_f32_32x32x16_bf16 a[96:111], a[220:223], v[244:247], a[96:111]\n"
"ds_read_b64_tr_b16 a[204:205], v21 offset:26624\n"
"ds_read_b64_tr_b16 a[206:207], v21 offset:27136\n"
"s_waitcnt lgkmcnt(6)\n"
"v_mfma_f32_32x32x16_bf16 a[112:127], a[192:195], v[160:163], a[112:127]\n"
"ds_read_b64_tr_b16 a[208:209], v20 offset:28672\n"
"ds_read_b64_tr_b16 a[210:211], v20 offset:29184\n"
"s_add_u32 m0, s60, 12288\n"
"s_add_u32 s87, s73, 86016\n"
"buffer_load_dwordx4 v6, s[52:55], s87 offen lds\n"
"s_waitcnt lgkmcnt(6)\n"
"v_mfma_f32_32x32x16_bf16 a[128:143], a[196:199], v[160:163], a[128:143]\n"
"ds_read_b64_tr_b16 a[212:213], v21 offset:28672\n"
"ds_read_b64_tr_b16 a[214:215], v21 offset:29184\n"
"s_waitcnt lgkmcnt(6)\n"
"v_mfma_f32_32x32x16_bf16 a[112:127], a[200:203], v[164:167], a[112:127]\n"
"ds_read_b64_tr_b16 a[216:217], v20 offset:30720\n"
"ds_read_b64_tr_b16 a[218:219], v20 offset:31232\n"
"s_add_u32 m0, s60, 118784\n"
"s_mov_b32 s87, 213376\n"
"buffer_load_dwordx4 v5, s[48:51], s87 offen nt lds\n"
"s_waitcnt lgkmcnt(6)\n"
"v_mfma_f32_32x32x16_bf16 a[128:143], a[204:207], v[164:167], a[128:143]\n"
"ds_read_b64_tr_b16 a[220:221], v21 offset:30720\n"
"ds_read_b64_tr_b16 a[222:223], v21 offset:31232\n"
"v_add_u32 v20, 65536, v7\n"
"v_xor_b32 v21, 64, v20\n"
"s_waitcnt lgkmcnt(6)\n"
"v_mfma_f32_32x32x16_bf16 a[112:127], a[208:211], v[176:179], a[112:127]\n"
"ds_read_b64_tr_b16 a[192:193], v20 offset:0\n"
"ds_read_b64_tr_b16 a[194:195], v20 offset:512\n"
"v_accvgpr_read_b32 v28, a80\n"
"v_accvgpr_read_b32 v29, a81\n"
"v_accvgpr_read_b32 v30, a82\n"
"v_accvgpr_read_b32 v31, a83\n"
"v_mul_f32 v28, v28, v2\n"
"s_add_u32 m0, s60, 16384\n"
"s_add_u32 s87, s73, 114688\n"
"buffer_load_dwordx4 v6, s[52:55], s87 offen lds\n"
"s_waitcnt lgkmcnt(6)\n"
"v_mfma_f32_32x32x16_bf16 a[128:143], a[212:215], v[176:179], a[128:143]\n"
"ds_read_b64_tr_b16 a[196:197], v21 offset:0\n"
"ds_read_b64_tr_b16 a[198:199], v21 offset:512\n"
"v_mul_f32 v29, v29, v2\n"
"v_mul_f32 v30, v30, v2\n"
"v_mul_f32 v31, v31, v2\n"
"v_cvt_pk_bf16_f32 v80, v28, v29\n"
"v_cvt_pk_bf16_f32 v81, v30, v31\n"
"s_waitcnt lgkmcnt(6)\n"
"v_mfma_f32_32x32x16_bf16 a[112:127], a[216:219], v[180:183], a[112:127]\n"
"ds_read_b64_tr_b16 a[200:201], v20 offset:2048\n"
"ds_read_b64_tr_b16 a[202:203], v20 offset:2560\n"
"v_accvgpr_read_b32 v28, a84\n"
"v_accvgpr_read_b32 v29, a85\n"
"v_accvgpr_read_b32 v30, a86\n"
"v_accvgpr_read_b32 v31, a87\n"
"v_mul_f32 v28, v28, v2\n"
"s_waitcnt lgkmcnt(6)\n"
"v_mfma_f32_32x32x16_bf16 a[128:143], a[220:223], v[180:183], a[128:143]\n"
"ds_read_b64_tr_b16 a[204:205], v21 offset:2048\n"
"ds_read_b64_tr_b16 a[206:207], v21 offset:2560\n"
"v_mul_f32 v29, v29, v2\n"
"v_mul_f32 v30, v30, v2\n"
"v_mul_f32 v31, v31, v2\n"
"v_cvt_pk_bf16_f32 v82, v28, v29\n"
"v_cvt_pk_bf16_f32 v83, v30, v31\n"
"s_add_u32 m0, s60, 122880\n"
"s_mov_b32 s87, 229760\n"
"buffer_load_dwordx4 v5, s[48:51], s87 offen nt lds\n"
"s_waitcnt lgkmcnt(6)\n"
"v_mfma_f32_32x32x16_bf16 a[112:127], a[192:195], v[192:195], a[112:127]\n"
"s_waitcnt vmcnt(12)\n"
"s_barrier\n"
"ds_read_b64_tr_b16 a[208:209], v20 offset:4096\n"
"ds_read_b64_tr_b16 a[210:211], v20 offset:4608\n"
"v_accvgpr_read_b32 v28, a88\n"
"v_accvgpr_read_b32 v29, a89\n"
"v_accvgpr_read_b32 v30, a90\n"
"s_add_u32 m0, s60, 126976\n"
"s_mov_b32 s87, 246144\n"
"buffer_load_dwordx4 v5, s[48:51], s87 offen nt lds\n"
"s_waitcnt lgkmcnt(6)\n"
"v_mfma_f32_32x32x16_bf16 a[128:143], a[196:199], v[192:195], a[128:143]\n"
"ds_read_b64_tr_b16 a[212:213], v21 offset:4096\n"
"ds_read_b64_tr_b16 a[214:215], v21 offset:4608\n"
"v_accvgpr_read_b32 v31, a91\n"
"v_mul_f32 v28, v28, v2\n"
"v_mul_f32 v29, v29, v2\n"
"v_mul_f32 v30, v30, v2\n"
"s_waitcnt lgkmcnt(6)\n"
"v_mfma_f32_32x32x16_bf16 a[112:127], a[200:203], v[196:199], a[112:127]\n"
"ds_read_b64_tr_b16 a[216:217], v20 offset:6144\n"
"ds_read_b64_tr_b16 a[218:219], v20 offset:6656\n"
"v_mul_f32 v31, v31, v2\n"
"v_cvt_pk_bf16_f32 v84, v28, v29\n"
"v_cvt_pk_bf16_f32 v85, v30, v31\n"
"v_accvgpr_read_b32 v28, a92\n"
"s_add_u32 m0, s60, 20480\n"
"s_add_u32 s87, s73, 143360\n"
"buffer_load_dwordx4 v6, s[52:55], s87 offen lds\n"
"s_waitcnt lgkmcnt(6)\n"
"v_mfma_f32_32x32x16_bf16 a[128:143], a[204:207], v[196:199], a[128:143]\n"
"ds_read_b64_tr_b16 a[220:221], v21 offset:6144\n"
"ds_read_b64_tr_b16 a[222:223], v21 offset:6656\n"
"v_accvgpr_read_b32 v29, a93\n"
"v_accvgpr_read_b32 v30, a94\n"
"v_accvgpr_read_b32 v31, a95\n"
"v_mul_f32 v28, v28, v2\n"
"s_waitcnt lgkmcnt(6)\n"
"v_mfma_f32_32x32x16_bf16 a[112:127], a[208:211], v[208:211], a[112:127]\n"
"ds_read_b64_tr_b16 a[192:193], v20 offset:8192\n"
"ds_read_b64_tr_b16 a[194:195], v20 offset:8704\n"
"v_mul_f32 v29, v29, v2\n"
"v_mul_f32 v30, v30, v2\n"
"v_mul_f32 v31, v31, v2\n"
"s_add_u32 m0, s60, 24576\n"
"s_add_u32 s87, s73, 172032\n"
"buffer_load_dwordx4 v6, s[52:55], s87 offen lds\n"
"s_waitcnt lgkmcnt(6)\n"
"v_mfma_f32_32x32x16_bf16 a[128:143], a[212:215], v[208:211], a[128:143]\n"
"ds_read_b64_tr_b16 a[196:197], v21 offset:8192\n"
"ds_read_b64_tr_b16 a[198:199], v21 offset:8704\n"
"v_cvt_pk_bf16_f32 v86, v28, v29\n"
"v_cvt_pk_bf16_f32 v87, v30, v31\n"
"v_accvgpr_read_b32 v28, a96\n"
"v_accvgpr_read_b32 v29, a97\n"
"s_waitcnt lgkmcnt(6)\n"
"v_mfma_f32_32x32x16_bf16 a[112:127], a[216:219], v[212:215], a[112:127]\n"
"ds_read_b64_tr_b16 a[200:201], v20 offset:10240\n"
"ds_read_b64_tr_b16 a[202:203], v20 offset:10752\n"
"v_accvgpr_read_b32 v30, a98\n"
"v_accvgpr_read_b32 v31, a99\n"
"v_mul_f32 v28, v28, v2\n"
"v_mul_f32 v29, v29, v2\n"
"s_add_u32 m0, s60, 28672\n"
"s_add_u32 s87, s73, 200704\n"
"buffer_load_dwordx4 v6, s[52:55], s87 offen lds\n"
"s_waitcnt lgkmcnt(6)\n"
"v_mfma_f32_32x32x16_bf16 a[128:143], a[220:223], v[212:215], a[128:143]\n"
"ds_read_b64_tr_b16 a[204:205], v21 offset:10240\n"
"ds_read_b64_tr_b16 a[206:207], v21 offset:10752\n"
"v_mul_f32 v30, v30, v2\n"
"v_mul_f32 v31, v31, v2\n"
"v_cvt_pk_bf16_f32 v88, v28, v29\n"
"v_cvt_pk_bf16_f32 v89, v30, v31\n"
"s_waitcnt lgkmcnt(6)\n"
"v_mfma_f32_32x32x16_bf16 a[112:127], a[192:195], v[224:227], a[112:127]\n"
"ds_read_b64_tr_b16 a[208:209], v20 offset:12288\n"
"ds_read_b64_tr_b16 a[210:211], v20 offset:12800\n"
"v_accvgpr_read_b32 v28, a100\n"
"v_accvgpr_read_b32 v29, a101\n"
"v_accvgpr_read_b32 v30, a102\n"
"s_add_u32 m0, s60, 32768\n"
"s_add_u32 s87, s74, 0\n"
"buffer_load_dwordx4 v6, s[52:55], s87 offen lds\n"
"s_waitcnt lgkmcnt(6)\n"
"v_mfma_f32_32x32x16_bf16 a[128:143], a[196:199], v[224:227], a[128:143]\n"
"ds_read_b64_tr_b16 a[212:213], v21 offset:12288\n"
"ds_read_b64_tr_b16 a[214:215], v21 offset:12800\n"
"v_accvgpr_read_b32 v31, a103\n"
"v_mul_f32 v28, v28, v2\n"
"v_mul_f32 v29, v29, v2\n"
"v_mul_f32 v30, v30, v2\n"
"s_waitcnt lgkmcnt(6)\n"
"v_mfma_f32_32x32x16_bf16 a[112:127], a[200:203], v[228:231], a[112:127]\n"
"ds_read_b64_tr_b16 a[216:217], v20 offset:14336\n"
"ds_read_b64_tr_b16 a[218:219], v20 offset:14848\n"
"v_mul_f32 v31, v31, v2\n"
"v_cvt_pk_bf16_f32 v90, v28, v29\n"
"v_cvt_pk_bf16_f32 v91, v30, v31\n"
"v_accvgpr_read_b32 v28, a104\n"
"s_add_u32 m0, s60, 36864\n"
"s_add_u32 s87, s74, 28672\n"
"buffer_load_dwordx4 v6, s[52:55], s87 offen lds\n"
"s_waitcnt lgkmcnt(6)\n"
"v_mfma_f32_32x32x16_bf16 a[128:143], a[204:207], v[228:231], a[128:143]\n"
"ds_read_b64_tr_b16 a[220:221], v21 offset:14336\n"
"ds_read_b64_tr_b16 a[222:223], v21 offset:14848\n"
"v_accvgpr_read_b32 v29, a105\n"
"v_accvgpr_read_b32 v30, a106\n"
"v_accvgpr_read_b32 v31, a107\n"
"v_mul_f32 v28, v28, v2\n"
"s_waitcnt lgkmcnt(6)\n"
"v_mfma_f32_32x32x16_bf16 a[112:127], a[208:211], v[240:243], a[112:127]\n"
"ds_read_b64_tr_b16 a[192:193], v20 offset:16384\n"
"ds_read_b64_tr_b16 a[194:195], v20 offset:16896\n"
"v_mul_f32 v29, v29, v2\n"
"v_mul_f32 v30, v30, v2\n"
"v_mul_f32 v31, v31, v2\n"
"s_add_u32 m0, s60, 40960\n"
"s_add_u32 s87, s74, 57344\n"
"buffer_load_dwordx4 v6, s[52:55], s87 offen lds\n"
"s_waitcnt lgkmcnt(6)\n"
"v_mfma_f32_32x32x16_bf16 a[128:143], a[212:215], v[240:243], a[128:143]\n"
"ds_read_b64_tr_b16 a[196:197], v21 offset:16384\n"
"ds_read_b64_tr_b16 a[198:199], v21 offset:16896\n"
"v_cvt_pk_bf16_f32 v92, v28, v29\n"
"v_cvt_pk_bf16_f32 v93, v30, v31\n"
"v_accvgpr_read_b32 v28, a108\n"
"v_accvgpr_read_b32 v29, a109\n"
"s_waitcnt lgkmcnt(6)\n"
"v_mfma_f32_32x32x16_bf16 a[112:127], a[216:219], v[244:247], a[112:127]\n"
"ds_read_b64_tr_b16 a[200:201], v20 offset:18432\n"
"ds_read_b64_tr_b16 a[202:203], v20 offset:18944\n"
"v_accvgpr_read_b32 v30, a110\n"
"v_accvgpr_read_b32 v31, a111\n"
"v_mul_f32 v28, v28, v2\n"
"v_mul_f32 v29, v29, v2\n"
"s_add_u32 m0, s60, 45056\n"
"s_add_u32 s87, s74, 86016\n"
"buffer_load_dwordx4 v6, s[52:55], s87 offen lds\n"
"s_waitcnt lgkmcnt(6)\n"
"v_mfma_f32_32x32x16_bf16 a[128:143], a[220:223], v[244:247], a[128:143]\n"
"ds_read_b64_tr_b16 a[204:205], v21 offset:18432\n"
"ds_read_b64_tr_b16 a[206:207], v21 offset:18944\n"
"v_mul_f32 v30, v30, v2\n"
"v_mul_f32 v31, v31, v2\n"
"v_cvt_pk_bf16_f32 v94, v28, v29\n"
"v_cvt_pk_bf16_f32 v95, v30, v31\n"
"s_waitcnt lgkmcnt(6)\n"
"v_mfma_f32_32x32x16_bf16 a[144:159], a[192:195], v[160:163], a[144:159]\n"
"ds_read_b64_tr_b16 a[208:209], v20 offset:20480\n"
"ds_read_b64_tr_b16 a[210:211], v20 offset:20992\n"
"s_add_u32 m0, s60, 49152\n"
"s_add_u32 s87, s74, 114688\n"
"buffer_load_dwordx4 v6, s[52:55], s87 offen lds\n"
"s_waitcnt lgkmcnt(6)\n"
"v_mfma_f32_32x32x16_bf16 a[160:175], a[196:199], v[160:163], a[160:175]\n"
"ds_read_b64_tr_b16 a[212:213], v21 offset:20480\n"
"ds_read_b64_tr_b16 a[214:215], v21 offset:20992\n"
"s_waitcnt lgkmcnt(6)\n"
"v_mfma_f32_32x32x16_bf16 a[144:159], a[200:203], v[164:167], a[144:159]\n"
"ds_read_b64_tr_b16 a[216:217], v20 offset:22528\n"
"ds_read_b64_tr_b16 a[218:219], v20 offset:23040\n"
"s_add_u32 m0, s60, 53248\n"
"s_add_u32 s87, s74, 143360\n"
"buffer_load_dwordx4 v6, s[52:55], s87 offen lds\n"
"s_waitcnt lgkmcnt(6)\n"
"v_mfma_f32_32x32x16_bf16 a[160:175], a[204:207], v[164:167], a[160:175]\n"
"ds_read_b64_tr_b16 a[220:221], v21 offset:22528\n"
"ds_read_b64_tr_b16 a[222:223], v21 offset:23040\n"
"s_waitcnt lgkmcnt(6)\n"
"v_mfma_f32_32x32x16_bf16 a[144:159], a[208:211], v[176:179], a[144:159]\n"
"ds_read_b64_tr_b16 a[192:193], v20 offset:24576\n"
"ds_read_b64_tr_b16 a[194:195], v20 offset:25088\n"
"v_accvgpr_read_b32 v28, a112\n"
"v_accvgpr_read_b32 v29, a113\n"
"v_accvgpr_read_b32 v30, a114\n"
"s_add_u32 m0, s60, 57344\n"
"s_add_u32 s87, s74, 172032\n"
"buffer_load_dwordx4 v6, s[52:55], s87 offen lds\n"
"s_waitcnt lgkmcnt(6)\n"
"v_mfma_f32_32x32x16_bf16 a[160:175], a[212:215], v[176:179], a[160:175]\n"
"ds_read_b64_tr_b16 a[196:197], v21 offset:24576\n"
"ds_read_b64_tr_b16 a[198:199], v21 offset:25088\n"
"v_accvgpr_read_b32 v31, a115\n"
"v_mul_f32 v28, v28, v2\n"
"v_mul_f32 v29, v29, v2\n"
"s_waitcnt lgkmcnt(6)\n"
"v_mfma_f32_32x32x16_bf16 a[144:159], a[216:219], v[180:183], a[144:159]\n"
"ds_read_b64_tr_b16 a[200:201], v20 offset:26624\n"
"ds_read_b64_tr_b16 a[202:203], v20 offset:27136\n"
"v_mul_f32 v30, v30, v2\n"
"v_mul_f32 v31, v31, v2\n"
"v_cvt_pk_bf16_f32 v96, v28, v29\n"
"v_cvt_pk_bf16_f32 v97, v30, v31\n"
"s_add_u32 m0, s60, 61440\n"
"s_add_u32 s87, s74, 200704\n"
"buffer_load_dwordx4 v6, s[52:55], s87 offen lds\n"
"s_waitcnt lgkmcnt(6)\n"
"v_mfma_f32_32x32x16_bf16 a[160:175], a[220:223], v[180:183], a[160:175]\n"
"ds_read_b64_tr_b16 a[204:205], v21 offset:26624\n"
"ds_read_b64_tr_b16 a[206:207], v21 offset:27136\n"
"v_accvgpr_read_b32 v28, a116\n"
"v_accvgpr_read_b32 v29, a117\n"
"v_accvgpr_read_b32 v30, a118\n"
"s_waitcnt lgkmcnt(6)\n"
"v_mfma_f32_32x32x16_bf16 a[144:159], a[192:195], v[192:195], a[144:159]\n"
"ds_read_b64_tr_b16 a[208:209], v20 offset:28672\n"
"ds_read_b64_tr_b16 a[210:211], v20 offset:29184\n"
"v_accvgpr_read_b32 v31, a119\n"
"v_mul_f32 v28, v28, v2\n"
"v_mul_f32 v29, v29, v2\n"
"s_waitcnt lgkmcnt(6)\n"
"v_mfma_f32_32x32x16_bf16 a[160:175], a[196:199], v[192:195], a[160:175]\n"
"ds_read_b64_tr_b16 a[212:213], v21 offset:28672\n"
"ds_read_b64_tr_b16 a[214:215], v21 offset:29184\n"
"v_mul_f32 v30, v30, v2\n"
"v_mul_f32 v31, v31, v2\n"
"v_cvt_pk_bf16_f32 v98, v28, v29\n"
"v_cvt_pk_bf16_f32 v99, v30, v31\n"
"s_waitcnt lgkmcnt(6)\n"
"v_mfma_f32_32x32x16_bf16 a[144:159], a[200:203], v[196:199], a[144:159]\n"
"ds_read_b64_tr_b16 a[216:217], v20 offset:30720\n"
"ds_read_b64_tr_b16 a[218:219], v20 offset:31232\n"
"v_accvgpr_read_b32 v28, a120\n"
"v_accvgpr_read_b32 v29, a121\n"
"v_accvgpr_read_b32 v30, a122\n"
"s_waitcnt lgkmcnt(6)\n"
"v_mfma_f32_32x32x16_bf16 a[160:175], a[204:207], v[196:199], a[160:175]\n"
"ds_read_b64_tr_b16 a[220:221], v21 offset:30720\n"
"ds_read_b64_tr_b16 a[222:223], v21 offset:31232\n"
"v_add_u32 v20, 98304, v7\n"
"v_xor_b32 v21, 64, v20\n"
"v_accvgpr_read_b32 v31, a123\n"
"v_mul_f32 v28, v28, v2\n"
"v_mul_f32 v29, v29, v2\n"
"s_waitcnt lgkmcnt(6)\n"
"v_mfma_f32_32x32x16_bf16 a[144:159], a[208:211], v[208:211], a[144:159]\n"
"ds_read_b64_tr_b16 a[192:193], v20 offset:0\n"
"ds_read_b64_tr_b16 a[194:195], v20 offset:512\n"
"v_mul_f32 v30, v30, v2\n"
"v_mul_f32 v31, v31, v2\n"
"v_cvt_pk_bf16_f32 v100, v28, v29\n"
"v_cvt_pk_bf16_f32 v101, v30, v31\n"
"s_waitcnt lgkmcnt(6)\n"
"v_mfma_f32_32x32x16_bf16 a[160:175], a[212:215], v[208:211], a[160:175]\n"
"ds_read_b64_tr_b16 a[196:197], v21 offset:0\n"
"ds_read_b64_tr_b16 a[198:199], v21 offset:512\n"
"v_accvgpr_read_b32 v28, a124\n"
"v_accvgpr_read_b32 v29, a125\n"
"v_accvgpr_read_b32 v30, a126\n"
"s_waitcnt lgkmcnt(6)\n"
"v_mfma_f32_32x32x16_bf16 a[144:159], a[216:219], v[212:215], a[144:159]\n"
"ds_read_b64_tr_b16 a[200:201], v20 offset:2048\n"
"ds_read_b64_tr_b16 a[202:203], v20 offset:2560\n"
"v_accvgpr_read_b32 v31, a127\n"
"v_mul_f32 v28, v28, v2\n"
"v_mul_f32 v29, v29, v2\n"
"s_waitcnt lgkmcnt(6)\n"
"v_mfma_f32_32x32x16_bf16 a[160:175], a[220:223], v[212:215], a[160:175]\n"
"ds_read_b64_tr_b16 a[204:205], v21 offset:2048\n"
"ds_read_b64_tr_b16 a[206:207], v21 offset:2560\n"
"v_mul_f32 v30, v30, v2\n"
"v_mul_f32 v31, v31, v2\n"
"v_cvt_pk_bf16_f32 v102, v28, v29\n"
"v_cvt_pk_bf16_f32 v103, v30, v31\n"
"s_waitcnt lgkmcnt(6)\n"
"v_mfma_f32_32x32x16_bf16 a[144:159], a[192:195], v[224:227], a[144:159]\n"
"s_waitcnt vmcnt(11)\n"
"s_barrier\n"
"ds_read_b64_tr_b16 a[208:209], v20 offset:4096\n"
"ds_read_b64_tr_b16 a[210:211], v20 offset:4608\n"
"v_accvgpr_read_b32 v28, a128\n"
"v_accvgpr_read_b32 v29, a129\n"
"v_accvgpr_read_b32 v30, a130\n"
"s_add_u32 m0, s60, 65536\n"
"s_add_u32 s87, s75, 0\n"
"buffer_load_dwordx4 v6, s[52:55], s87 offen lds\n"
"s_waitcnt lgkmcnt(6)\n"
"v_mfma_f32_32x32x16_bf16 a[160:175], a[196:199], v[224:227], a[160:175]\n"
"ds_read_b64_tr_b16 a[212:213], v21 offset:4096\n"
"ds_read_b64_tr_b16 a[214:215], v21 offset:4608\n"
"v_accvgpr_read_b32 v31, a131\n"
"v_mul_f32 v28, v28, v2\n"
"v_mul_f32 v29, v29, v2\n"
"s_waitcnt lgkmcnt(6)\n"
"v_mfma_f32_32x32x16_bf16 a[144:159], a[200:203], v[228:231], a[144:159]\n"
"ds_read_b64_tr_b16 a[216:217], v20 offset:6144\n"
"ds_read_b64_tr_b16 a[218:219], v20 offset:6656\n"
"v_mul_f32 v30, v30, v2\n"
"v_mul_f32 v31, v31, v2\n"
"v_cvt_pk_bf16_f32 v104, v28, v29\n"
"v_cvt_pk_bf16_f32 v105, v30, v31\n"
"s_add_u32 m0, s60, 69632\n"
"s_add_u32 s87, s75, 28672\n"
"buffer_load_dwordx4 v6, s[52:55], s87 offen lds\n"
"s_waitcnt lgkmcnt(6)\n"
"v_mfma_f32_32x32x16_bf16 a[160:175], a[204:207], v[228:231], a[160:175]\n"
"ds_read_b64_tr_b16 a[220:221], v21 offset:6144\n"
"ds_read_b64_tr_b16 a[222:223], v21 offset:6656\n"
"v_accvgpr_read_b32 v28, a132\n"
"v_accvgpr_read_b32 v29, a133\n"
"v_accvgpr_read_b32 v30, a134\n"
"s_waitcnt lgkmcnt(6)\n"
"v_mfma_f32_32x32x16_bf16 a[144:159], a[208:211], v[240:243], a[144:159]\n"
"ds_read_b64_tr_b16 a[192:193], v20 offset:8192\n"
"ds_read_b64_tr_b16 a[194:195], v20 offset:8704\n"
"v_accvgpr_read_b32 v31, a135\n"
"v_mul_f32 v28, v28, v2\n"
"v_mul_f32 v29, v29, v2\n"
"s_add_u32 m0, s60, 73728\n"
"s_add_u32 s87, s75, 57344\n"
"buffer_load_dwordx4 v6, s[52:55], s87 offen lds\n"
"s_waitcnt lgkmcnt(6)\n"
"v_mfma_f32_32x32x16_bf16 a[160:175], a[212:215], v[240:243], a[160:175]\n"
"ds_read_b64_tr_b16 a[196:197], v21 offset:8192\n"
"ds_read_b64_tr_b16 a[198:199], v21 offset:8704\n"
"v_mul_f32 v30, v30, v2\n"
"v_mul_f32 v31, v31, v2\n"
"v_cvt_pk_bf16_f32 v106, v28, v29\n"
"v_cvt_pk_bf16_f32 v107, v30, v31\n"
"s_waitcnt lgkmcnt(6)\n"
"v_mfma_f32_32x32x16_bf16 a[144:159], a[216:219], v[244:247], a[144:159]\n"
"ds_read_b64_tr_b16 a[200:201], v20 offset:10240\n"
"ds_read_b64_tr_b16 a[202:203], v20 offset:10752\n"
"v_accvgpr_read_b32 v28, a136\n"
"v_accvgpr_read_b32 v29, a137\n"
"v_accvgpr_read_b32 v30, a138\n"
"s_add_u32 m0, s60, 77824\n"
"s_add_u32 s87, s75, 86016\n"
"buffer_load_dwordx4 v6, s[52:55], s87 offen lds\n"
"s_waitcnt lgkmcnt(6)\n"
"v_mfma_f32_32x32x16_bf16 a[160:175], a[220:223], v[244:247], a[160:175]\n"
"ds_read_b64_tr_b16 a[204:205], v21 offset:10240\n"
"ds_read_b64_tr_b16 a[206:207], v21 offset:10752\n"
"v_accvgpr_read_b32 v31, a139\n"
"v_mul_f32 v28, v28, v2\n"
"v_mul_f32 v29, v29, v2\n"
"s_waitcnt lgkmcnt(6)\n"
"v_mfma_f32_32x32x16_bf16 a[176:191], a[192:195], v[160:163], a[176:191]\n"
"ds_read_b64_tr_b16 a[208:209], v20 offset:12288\n"
"ds_read_b64_tr_b16 a[210:211], v20 offset:12800\n"
"v_mul_f32 v30, v30, v2\n"
"v_mul_f32 v31, v31, v2\n"
"v_cvt_pk_bf16_f32 v108, v28, v29\n"
"v_cvt_pk_bf16_f32 v109, v30, v31\n"
"s_add_u32 m0, s60, 81920\n"
"s_add_u32 s87, s75, 114688\n"
"buffer_load_dwordx4 v6, s[52:55], s87 offen lds\n"
"s_waitcnt lgkmcnt(6)\n"
"v_mfma_f32_32x32x16_bf16 a[224:239], a[196:199], v[160:163], a[224:239]\n"
"ds_read_b64_tr_b16 a[212:213], v21 offset:12288\n"
"ds_read_b64_tr_b16 a[214:215], v21 offset:12800\n"
"v_accvgpr_read_b32 v28, a140\n"
"v_accvgpr_read_b32 v29, a141\n"
"v_accvgpr_read_b32 v30, a142\n"
"s_waitcnt lgkmcnt(6)\n"
"v_mfma_f32_32x32x16_bf16 a[176:191], a[200:203], v[164:167], a[176:191]\n"
"ds_read_b64_tr_b16 a[216:217], v20 offset:14336\n"
"ds_read_b64_tr_b16 a[218:219], v20 offset:14848\n"
"v_accvgpr_read_b32 v31, a143\n"
"v_mul_f32 v28, v28, v2\n"
"v_mul_f32 v29, v29, v2\n"
"s_add_u32 m0, s60, 86016\n"
"s_add_u32 s87, s75, 143360\n"
"buffer_load_dwordx4 v6, s[52:55], s87 offen lds\n"
"s_waitcnt lgkmcnt(6)\n"
"v_mfma_f32_32x32x16_bf16 a[224:239], a[204:207], v[164:167], a[224:239]\n"
"ds_read_b64_tr_b16 a[220:221], v21 offset:14336\n"
"ds_read_b64_tr_b16 a[222:223], v21 offset:14848\n"
"v_mul_f32 v30, v30, v2\n"
"v_mul_f32 v31, v31, v2\n"
"v_cvt_pk_bf16_f32 v110, v28, v29\n"
"v_cvt_pk_bf16_f32 v111, v30, v31\n"
"s_waitcnt lgkmcnt(6)\n"
"v_mfma_f32_32x32x16_bf16 a[176:191], a[208:211], v[176:179], a[176:191]\n"
"ds_read_b64_tr_b16 a[192:193], v20 offset:16384\n"
"ds_read_b64_tr_b16 a[194:195], v20 offset:16896\n"
"v_accvgpr_read_b32 v28, a144\n"
"v_accvgpr_read_b32 v29, a145\n"
"v_accvgpr_read_b32 v30, a146\n"
"v_accvgpr_read_b32 v31, a147\n"
"s_add_u32 m0, s60, 90112\n"
"s_add_u32 s87, s75, 172032\n"
"buffer_load_dwordx4 v6, s[52:55], s87 offen lds\n"
"s_waitcnt lgkmcnt(6)\n"
"v_mfma_f32_32x32x16_bf16 a[224:239], a[212:215], v[176:179], a[224:239]\n"
"ds_read_b64_tr_b16 a[196:197], v21 offset:16384\n"
"ds_read_b64_tr_b16 a[198:199], v21 offset:16896\n"
"v_mul_f32 v28, v28, v2\n"
"v_mul_f32 v29, v29, v2\n"
"v_mul_f32 v30, v30, v2\n"
"v_mul_f32 v31, v31, v2\n"
"s_waitcnt lgkmcnt(6)\n"
"v_mfma_f32_32x32x16_bf16 a[176:191], a[216:219], v[180:183], a[176:191]\n"
"ds_read_b64_tr_b16 a[200:201], v20 offset:18432\n"
"ds_read_b64_tr_b16 a[202:203], v20 offset:18944\n"
"v_cvt_pk_bf16_f32 v112, v28, v29\n"
"v_cvt_pk_bf16_f32 v113, v30, v31\n"
"v_accvgpr_read_b32 v28, a148\n"
"v_accvgpr_read_b32 v29, a149\n"
"s_add_u32 m0, s60, 94208\n"
"s_add_u32 s87, s75, 200704\n"
"buffer_load_dwordx4 v6, s[52:55], s87 offen lds\n"
"s_waitcnt lgkmcnt(6)\n"
"v_mfma_f32_32x32x16_bf16 a[224:239], a[220:223], v[180:183], a[224:239]\n"
"ds_read_b64_tr_b16 a[204:205], v21 offset:18432\n"
"ds_read_b64_tr_b16 a[206:207], v21 offset:18944\n"
"v_accvgpr_read_b32 v30, a150\n"
"v_accvgpr_read_b32 v31, a151\n"
"v_mul_f32 v28, v28, v2\n"
"v_mul_f32 v29, v29, v2\n"
"s_waitcnt lgkmcnt(6)\n"
"v_mfma_f32_32x32x16_bf16 a[176:191], a[192:195], v[192:195], a[176:191]\n"
"ds_read_b64_tr_b16 a[208:209], v20 offset:20480\n"
"ds_read_b64_tr_b16 a[210:211], v20 offset:20992\n"
"v_mul_f32 v30, v30, v2\n"
"v_mul_f32 v31, v31, v2\n"
"v_cvt_pk_bf16_f32 v114, v28, v29\n"
"v_cvt_pk_bf16_f32 v115, v30, v31\n"
"s_waitcnt lgkmcnt(6)\n"
"v_mfma_f32_32x32x16_bf16 a[224:239], a[196:199], v[192:195], a[224:239]\n"
"ds_read_b64_tr_b16 a[212:213], v21 offset:20480\n"
"ds_read_b64_tr_b16 a[214:215], v21 offset:20992\n"
"v_accvgpr_read_b32 v28, a152\n"
"v_accvgpr_read_b32 v29, a153\n"
"v_accvgpr_read_b32 v30, a154\n"
"v_accvgpr_read_b32 v31, a155\n"
"s_waitcnt lgkmcnt(6)\n"
"v_mfma_f32_32x32x16_bf16 a[176:191], a[200:203], v[196:199], a[176:191]\n"
"ds_read_b64_tr_b16 a[216:217], v20 offset:22528\n"
"ds_read_b64_tr_b16 a[218:219], v20 offset:23040\n"
"v_mul_f32 v28, v28, v2\n"
"v_mul_f32 v29, v29, v2\n"
"v_mul_f32 v30, v30, v2\n"
"v_mul_f32 v31, v31, v2\n"
"s_waitcnt lgkmcnt(6)\n"
"v_mfma_f32_32x32x16_bf16 a[224:239], a[204:207], v[196:199], a[224:239]\n"
"ds_read_b64_tr_b16 a[220:221], v21 offset:22528\n"
"ds_read_b64_tr_b16 a[222:223], v21 offset:23040\n"
"v_cvt_pk_bf16_f32 v116, v28, v29\n"
"v_cvt_pk_bf16_f32 v117, v30, v31\n"
"v_accvgpr_read_b32 v28, a156\n"
"v_accvgpr_read_b32 v29, a157\n"
"s_waitcnt lgkmcnt(6)\n"
"v_mfma_f32_32x32x16_bf16 a[176:191], a[208:211], v[208:211], a[176:191]\n"
"ds_read_b64_tr_b16 a[192:193], v20 offset:24576\n"
"ds_read_b64_tr_b16 a[194:195], v20 offset:25088\n"
"v_accvgpr_read_b32 v30, a158\n"
"v_accvgpr_read_b32 v31, a159\n"
"v_mul_f32 v28, v28, v2\n"
"v_mul_f32 v29, v29, v2\n"
"s_waitcnt lgkmcnt(6)\n"
"v_mfma_f32_32x32x16_bf16 a[224:239], a[212:215], v[208:211], a[224:239]\n"
"ds_read_b64_tr_b16 a[196:197], v21 offset:24576\n"
"ds_read_b64_tr_b16 a[198:199], v21 offset:25088\n"
"v_mul_f32 v30, v30, v2\n"
"v_mul_f32 v31, v31, v2\n"
"v_cvt_pk_bf16_f32 v118, v28, v29\n"
"v_cvt_pk_bf16_f32 v119, v30, v31\n"
"s_waitcnt lgkmcnt(6)\n"
"v_mfma_f32_32x32x16_bf16 a[176:191], a[216:219], v[212:215], a[176:191]\n"
"ds_read_b64_tr_b16 a[200:201], v20 offset:26624\n"
"ds_read_b64_tr_b16 a[202:203], v20 offset:27136\n"
"v_accvgpr_read_b32 v28, a160\n"
"v_accvgpr_read_b32 v29, a161\n"
"v_accvgpr_read_b32 v30, a162\n"
"v_accvgpr_read_b32 v31, a163\n"
"s_waitcnt lgkmcnt(6)\n"
"v_mfma_f32_32x32x16_bf16 a[224:239], a[220:223], v[212:215], a[224:239]\n"
"ds_read_b64_tr_b16 a[204:205], v21 offset:26624\n"
"ds_read_b64_tr_b16 a[206:207], v21 offset:27136\n"
"v_mul_f32 v28, v28, v2\n"
"v_mul_f32 v29, v29, v2\n"
"v_mul_f32 v30, v30, v2\n"
"v_mul_f32 v31, v31, v2\n"
"s_waitcnt lgkmcnt(6)\n"
"v_mfma_f32_32x32x16_bf16 a[176:191], a[192:195], v[224:227], a[176:191]\n"
"ds_read_b64_tr_b16 a[208:209], v20 offset:28672\n"
"ds_read_b64_tr_b16 a[210:211], v20 offset:29184\n"
"v_cvt_pk_bf16_f32 v120, v28, v29\n"
"v_cvt_pk_bf16_f32 v121, v30, v31\n"
"v_accvgpr_read_b32 v28, a164\n"
"v_accvgpr_read_b32 v29, a165\n"
"s_waitcnt lgkmcnt(6)\n"
"v_mfma_f32_32x32x16_bf16 a[224:239], a[196:199], v[224:227], a[224:239]\n"
"ds_read_b64_tr_b16 a[212:213], v21 offset:28672\n"
"ds_read_b64_tr_b16 a[214:215], v21 offset:29184\n"
"v_accvgpr_read_b32 v30, a166\n"
"v_accvgpr_read_b32 v31, a167\n"
"v_mul_f32 v28, v28, v2\n"
"v_mul_f32 v29, v29, v2\n"
"s_waitcnt lgkmcnt(6)\n"
"v_mfma_f32_32x32x16_bf16 a[176:191], a[200:203], v[228:231], a[176:191]\n"
"ds_read_b64_tr_b16 a[216:217], v20 offset:30720\n"
"ds_read_b64_tr_b16 a[218:219], v20 offset:31232\n"
"v_mul_f32 v30, v30, v2\n"
"v_mul_f32 v31, v31, v2\n"
"v_cvt_pk_bf16_f32 v122, v28, v29\n"
"v_cvt_pk_bf16_f32 v123, v30, v31\n"
"s_waitcnt lgkmcnt(6)\n"
"v_mfma_f32_32x32x16_bf16 a[224:239], a[204:207], v[228:231], a[224:239]\n"
"ds_read_b64_tr_b16 a[220:221], v21 offset:30720\n"
"ds_read_b64_tr_b16 a[222:223], v21 offset:31232\n"
"v_add_u32 v20, 131072, v7\n"
"v_xor_b32 v21, 64, v20\n"
"v_accvgpr_read_b32 v28, a168\n"
"v_accvgpr_read_b32 v29, a169\n"
"v_accvgpr_read_b32 v30, a170\n"
"v_accvgpr_read_b32 v31, a171\n"
"s_waitcnt lgkmcnt(6)\n"
"v_mfma_f32_32x32x16_bf16 a[176:191], a[208:211], v[240:243], a[176:191]\n"
"ds_read_b64_tr_b16 a[192:193], v20 offset:0\n"
"ds_read_b64_tr_b16 a[194:195], v20 offset:512\n"
"v_mul_f32 v28, v28, v2\n"
"v_mul_f32 v29, v29, v2\n"
"v_mul_f32 v30, v30, v2\n"
"v_mul_f32 v31, v31, v2\n"
"s_waitcnt lgkmcnt(6)\n"
"v_mfma_f32_32x32x16_bf16 a[224:239], a[212:215], v[240:243], a[224:239]\n"
"ds_read_b64_tr_b16 a[196:197], v21 offset:0\n"
"ds_read_b64_tr_b16 a[198:199], v21 offset:512\n"
"v_cvt_pk_bf16_f32 v124, v28, v29\n"
"v_cvt_pk_bf16_f32 v125, v30, v31\n"
"v_accvgpr_read_b32 v28, a172\n"
"v_accvgpr_read_b32 v29, a173\n"
"s_waitcnt lgkmcnt(6)\n"
"v_mfma_f32_32x32x16_bf16 a[176:191], a[216:219], v[244:247], a[176:191]\n"
"ds_read_b64_tr_b16 a[200:201], v20 offset:2048\n"
"ds_read_b64_tr_b16 a[202:203], v20 offset:2560\n"
"v_accvgpr_read_b32 v30, a174\n"
"v_accvgpr_read_b32 v31, a175\n"
"v_mul_f32 v28, v28, v2\n"
"v_mul_f32 v29, v29, v2\n"
"s_waitcnt lgkmcnt(6)\n"
"v_mfma_f32_32x32x16_bf16 a[224:239], a[220:223], v[244:247], a[224:239]\n"
"ds_read_b64_tr_b16 a[204:205], v21 offset:2048\n"
"ds_read_b64_tr_b16 a[206:207], v21 offset:2560\n"
"v_mul_f32 v30, v30, v2\n"
"v_mul_f32 v31, v31, v2\n"
"v_cvt_pk_bf16_f32 v126, v28, v29\n"
"v_cvt_pk_bf16_f32 v127, v30, v31\n"
"s_waitcnt lgkmcnt(6)\n"
"v_mfma_f32_32x32x16_bf16 v[32:47], a[192:195], v[80:83], 0\n"
"s_barrier\n"
"ds_read_b64_tr_b16 a[208:209], v20 offset:4096\n"
"ds_read_b64_tr_b16 a[210:211], v20 offset:4608\n"
"s_add_u32 m0, s60, 98304\n"
"s_add_u32 s87, s76, 0\n"
"buffer_load_dwordx4 v6, s[52:55], s87 offen lds\n"
"s_waitcnt lgkmcnt(6)\n"
"v_mfma_f32_32x32x16_bf16 v[48:63], a[196:199], v[80:83], 0\n"
"ds_read_b64_tr_b16 a[212:213], v21 offset:4096\n"
"ds_read_b64_tr_b16 a[214:215], v21 offset:4608\n"
"s_waitcnt lgkmcnt(6)\n"
"v_mfma_f32_32x32x16_bf16 v[32:47], a[200:203], v[84:87], v[32:47]\n"
"ds_read_b64_tr_b16 a[216:217], v20 offset:6144\n"
"ds_read_b64_tr_b16 a[218:219], v20 offset:6656\n"
"s_add_u32 m0, s60, 102400\n"
"s_add_u32 s87, s76, 28672\n"
"buffer_load_dwordx4 v6, s[52:55], s87 offen lds\n"
"s_waitcnt lgkmcnt(6)\n"
"v_mfma_f32_32x32x16_bf16 v[48:63], a[204:207], v[84:87], v[48:63]\n"
"ds_read_b64_tr_b16 a[220:221], v21 offset:6144\n"
"ds_read_b64_tr_b16 a[222:223], v21 offset:6656\n"
"s_waitcnt lgkmcnt(6)\n"
"v_mfma_f32_32x32x16_bf16 v[32:47], a[208:211], v[88:91], v[32:47]\n"
"ds_read_b64_tr_b16 a[192:193], v20 offset:8192\n"
"ds_read_b64_tr_b16 a[194:195], v20 offset:8704\n"
"v_accvgpr_read_b32 v28, a176\n"
"v_accvgpr_read_b32 v29, a177\n"
"v_accvgpr_read_b32 v30, a178\n"
"v_accvgpr_read_b32 v31, a179\n"
"s_add_u32 m0, s60, 106496\n"
"s_add_u32 s87, s76, 57344\n"
"buffer_load_dwordx4 v6, s[52:55], s87 offen lds\n"
"s_waitcnt lgkmcnt(6)\n"
"v_mfma_f32_32x32x16_bf16 v[48:63], a[212:215], v[88:91], v[48:63]\n"
"ds_read_b64_tr_b16 a[196:197], v21 offset:8192\n"
"ds_read_b64_tr_b16 a[198:199], v21 offset:8704\n"
"v_mul_f32 v28, v28, v2\n"
"v_mul_f32 v29, v29, v2\n"
"v_mul_f32 v30, v30, v2\n"
"v_mul_f32 v31, v31, v2\n"
"s_waitcnt lgkmcnt(6)\n"
"v_mfma_f32_32x32x16_bf16 v[32:47], a[216:219], v[92:95], v[32:47]\n"
"ds_read_b64_tr_b16 a[200:201], v20 offset:10240\n"
"ds_read_b64_tr_b16 a[202:203], v20 offset:10752\n"
"v_cvt_pk_bf16_f32 v128, v28, v29\n"
"v_cvt_pk_bf16_f32 v129, v30, v31\n"
"v_accvgpr_read_b32 v28, a180\n"
"v_accvgpr_read_b32 v29, a181\n"
"s_add_u32 m0, s60, 110592\n"
"s_add_u32 s87, s76, 86016\n"
"buffer_load_dwordx4 v6, s[52:55], s87 offen lds\n"
"s_waitcnt lgkmcnt(6)\n"
"v_mfma_f32_32x32x16_bf16 v[48:63], a[220:223], v[92:95], v[48:63]\n"
"ds_read_b64_tr_b16 a[204:205], v21 offset:10240\n"
"ds_read_b64_tr_b16 a[206:207], v21 offset:10752\n"
"v_accvgpr_read_b32 v30, a182\n"
"v_accvgpr_read_b32 v31, a183\n"
"v_mul_f32 v28, v28, v2\n"
"v_mul_f32 v29, v29, v2\n"
"s_waitcnt lgkmcnt(6)\n"
"v_mfma_f32_32x32x16_bf16 v[32:47], a[192:195], v[96:99], v[32:47]\n"
"ds_read_b64_tr_b16 a[208:209], v20 offset:12288\n"
"ds_read_b64_tr_b16 a[210:211], v20 offset:12800\n"
"v_mul_f32 v30, v30, v2\n"
"v_mul_f32 v31, v31, v2\n"
"v_cvt_pk_bf16_f32 v130, v28, v29\n"
"v_cvt_pk_bf16_f32 v131, v30, v31\n"
"s_add_u32 m0, s60, 114688\n"
"s_add_u32 s87, s76, 114688\n"
"buffer_load_dwordx4 v6, s[52:55], s87 offen lds\n"
"s_waitcnt lgkmcnt(6)\n"
"v_mfma_f32_32x32x16_bf16 v[48:63], a[196:199], v[96:99], v[48:63]\n"
"ds_read_b64_tr_b16 a[212:213], v21 offset:12288\n"
"ds_read_b64_tr_b16 a[214:215], v21 offset:12800\n"
"v_accvgpr_read_b32 v28, a184\n"
"v_accvgpr_read_b32 v29, a185\n"
"v_accvgpr_read_b32 v30, a186\n"
"v_accvgpr_read_b32 v31, a187\n"
"s_waitcnt lgkmcnt(6)\n"
"v_mfma_f32_32x32x16_bf16 v[32:47], a[200:203], v[100:103], v[32:47]\n"
"ds_read_b64_tr_b16 a[216:217], v20 offset:14336\n"
"ds_read_b64_tr_b16 a[218:219], v20 offset:14848\n"
"v_mul_f32 v28, v28, v2\n"
"v_mul_f32 v29, v29, v2\n"
"v_mul_f32 v30, v30, v2\n"
"v_mul_f32 v31, v31, v2\n"
"s_add_u32 m0, s60, 118784\n"
"s_add_u32 s87, s76, 143360\n"
"buffer_load_dwordx4 v6, s[52:55], s87 offen lds\n"
"s_waitcnt lgkmcnt(6)\n"
"v_mfma_f32_32x32x16_bf16 v[48:63], a[204:207], v[100:103], v[48:63]\n"
"ds_read_b64_tr_b16 a[220:221], v21 offset:14336\n"
"ds_read_b64_tr_b16 a[222:223], v21 offset:14848\n"
"v_cvt_pk_bf16_f32 v132, v28, v29\n"
"v_cvt_pk_bf16_f32 v133, v30, v31\n"
"v_accvgpr_read_b32 v28, a188\n"
"v_accvgpr_read_b32 v29, a189\n"
"s_waitcnt lgkmcnt(6)\n"
"v_mfma_f32_32x32x16_bf16 v[32:47], a[208:211], v[104:107], v[32:47]\n"
"ds_read_b64_tr_b16 a[192:193], v20 offset:16384\n"
"ds_read_b64_tr_b16 a[194:195], v20 offset:16896\n"
"v_accvgpr_read_b32 v30, a190\n"
"v_accvgpr_read_b32 v31, a191\n"
"v_mul_f32 v28, v28, v2\n"
"v_mul_f32 v29, v29, v2\n"
"s_add_u32 m0, s60, 122880\n"
"s_add_u32 s87, s76, 172032\n"
"buffer_load_dwordx4 v6, s[52:55], s87 offen lds\n"
"s_waitcnt lgkmcnt(6)\n"
"v_mfma_f32_32x32x16_bf16 v[48:63], a[212:215], v[104:107], v[48:63]\n"
"ds_read_b64_tr_b16 a[196:197], v21 offset:16384\n"
"ds_read_b64_tr_b16 a[198:199], v21 offset:16896\n"
"v_mul_f32 v30, v30, v2\n"
"v_mul_f32 v31, v31, v2\n"
"v_cvt_pk_bf16_f32 v134, v28, v29\n"
"v_cvt_pk_bf16_f32 v135, v30, v31\n"
"s_waitcnt lgkmcnt(6)\n"
"v_mfma_f32_32x32x16_bf16 v[32:47], a[216:219], v[108:111], v[32:47]\n"
"ds_read_b64_tr_b16 a[200:201], v20 offset:18432\n"
"ds_read_b64_tr_b16 a[202:203], v20 offset:18944\n"
"v_accvgpr_read_b32 v28, a224\n"
"v_accvgpr_read_b32 v29, a225\n"
"v_accvgpr_read_b32 v30, a226\n"
"v_accvgpr_read_b32 v31, a227\n"
"s_add_u32 m0, s60, 126976\n"
"s_add_u32 s87, s76, 200704\n"
"buffer_load_dwordx4 v6, s[52:55], s87 offen lds\n"
"s_waitcnt lgkmcnt(6)\n"
"v_mfma_f32_32x32x16_bf16 v[48:63], a[220:223], v[108:111], v[48:63]\n"
"ds_read_b64_tr_b16 a[204:205], v21 offset:18432\n"
"ds_read_b64_tr_b16 a[206:207], v21 offset:18944\n"
"v_mul_f32 v28, v28, v2\n"
"v_mul_f32 v29, v29, v2\n"
"v_mul_f32 v30, v30, v2\n"
"v_mul_f32 v31, v31, v2\n"
"s_waitcnt lgkmcnt(6)\n"
"v_mfma_f32_32x32x16_bf16 v[32:47], a[192:195], v[112:115], v[32:47]\n"
"ds_read_b64_tr_b16 a[208:209], v20 offset:20480\n"
"ds_read_b64_tr_b16 a[210:211], v20 offset:20992\n"
"v_cvt_pk_bf16_f32 v136, v28, v29\n"
"v_cvt_pk_bf16_f32 v137, v30, v31\n"
"v_accvgpr_read_b32 v28, a228\n"
"v_accvgpr_read_b32 v29, a229\n"
"s_waitcnt lgkmcnt(6)\n"
"v_mfma_f32_32x32x16_bf16 v[48:63], a[196:199], v[112:115], v[48:63]\n"
"ds_read_b64_tr_b16 a[212:213], v21 offset:20480\n"
"ds_read_b64_tr_b16 a[214:215], v21 offset:20992\n"
"v_accvgpr_read_b32 v30, a230\n"
"v_accvgpr_read_b32 v31, a231\n"
"v_mul_f32 v28, v28, v2\n"
"v_mul_f32 v29, v29, v2\n"
"s_waitcnt lgkmcnt(6)\n"
"v_mfma_f32_32x32x16_bf16 v[32:47], a[200:203], v[116:119], v[32:47]\n"
"ds_read_b64_tr_b16 a[216:217], v20 offset:22528\n"
"ds_read_b64_tr_b16 a[218:219], v20 offset:23040\n"
"v_mul_f32 v30, v30, v2\n"
"v_mul_f32 v31, v31, v2\n"
"v_cvt_pk_bf16_f32 v138, v28, v29\n"
"v_cvt_pk_bf16_f32 v139, v30, v31\n"
"s_waitcnt lgkmcnt(6)\n"
"v_mfma_f32_32x32x16_bf16 v[48:63], a[204:207], v[116:119], v[48:63]\n"
"ds_read_b64_tr_b16 a[220:221], v21 offset:22528\n"
"ds_read_b64_tr_b16 a[222:223], v21 offset:23040\n"
"v_accvgpr_read_b32 v28, a232\n"
"v_accvgpr_read_b32 v29, a233\n"
"v_accvgpr_read_b32 v30, a234\n"
"v_accvgpr_read_b32 v31, a235\n"
"s_waitcnt lgkmcnt(6)\n"
"v_mfma_f32_32x32x16_bf16 v[32:47], a[208:211], v[120:123], v[32:47]\n"
"ds_read_b64_tr_b16 a[192:193], v20 offset:24576\n"
"ds_read_b64_tr_b16 a[194:195], v20 offset:25088\n"
"v_mul_f32 v28, v28, v2\n"
"v_mul_f32 v29, v29, v2\n"
"v_mul_f32 v30, v30, v2\n"
"v_mul_f32 v31, v31, v2\n"
"s_waitcnt lgkmcnt(6)\n"
"v_mfma_f32_32x32x16_bf16 v[48:63], a[212:215], v[120:123], v[48:63]\n"
"ds_read_b64_tr_b16 a[196:197], v21 offset:24576\n"
"ds_read_b64_tr_b16 a[198:199], v21 offset:25088\n"
"v_cvt_pk_bf16_f32 v140, v28, v29\n"
"v_cvt_pk_bf16_f32 v141, v30, v31\n"
"v_accvgpr_read_b32 v28, a236\n"
"v_accvgpr_read_b32 v29, a237\n"
"s_waitcnt lgkmcnt(6)\n"
"v_mfma_f32_32x32x16_bf16 v[32:47], a[216:219], v[124:127], v[32:47]\n"
"ds_read_b64_tr_b16 a[200:201], v20 offset:26624\n"
"ds_read_b64_tr_b16 a[202:203], v20 offset:27136\n"
"v_accvgpr_read_b32 v30, a238\n"
"v_accvgpr_read_b32 v31, a239\n"
"v_mul_f32 v28, v28, v2\n"
"v_mul_f32 v29, v29, v2\n"
"s_waitcnt lgkmcnt(6)\n"
"v_mfma_f32_32x32x16_bf16 v[48:63], a[220:223], v[124:127], v[48:63]\n"
"ds_read_b64_tr_b16 a[204:205], v21 offset:26624\n"
"ds_read_b64_tr_b16 a[206:207], v21 offset:27136\n"
"v_mul_f32 v30, v30, v2\n"
"v_mul_f32 v31, v31, v2\n"
"v_cvt_pk_bf16_f32 v142, v28, v29\n"
"v_cvt_pk_bf16_f32 v143, v30, v31\n"
"s_nop 1\n"
"s_waitcnt lgkmcnt(6)\n"
"v_mfma_f32_32x32x16_bf16 v[32:47], a[192:195], v[128:131], v[32:47]\n"
"ds_read_b64_tr_b16 a[208:209], v20 offset:28672\n"
"ds_read_b64_tr_b16 a[210:211], v20 offset:29184\n"
"s_waitcnt lgkmcnt(6)\n"
"v_mfma_f32_32x32x16_bf16 v[48:63], a[196:199], v[128:131], v[48:63]\n"
"ds_read_b64_tr_b16 a[212:213], v21 offset:28672\n"
"ds_read_b64_tr_b16 a[214:215], v21 offset:29184\n"
"s_waitcnt lgkmcnt(6)\n"
"v_mfma_f32_32x32x16_bf16 v[32:47], a[200:203], v[132:135], v[32:47]\n"
"ds_read_b64_tr_b16 a[216:217], v20 offset:30720\n"
"ds_read_b64_tr_b16 a[218:219], v20 offset:31232\n"
"s_waitcnt lgkmcnt(6)\n"
"v_mfma_f32_32x32x16_bf16 v[48:63], a[204:207], v[132:135], v[48:63]\n"
"ds_read_b64_tr_b16 a[220:221], v21 offset:30720\n"
"ds_read_b64_tr_b16 a[222:223], v21 offset:31232\n"
"v_mov_b32 v20, v7\n"
"v_xor_b32 v21, 64, v20\n"
"s_waitcnt lgkmcnt(6)\n"
"v_mfma_f32_32x32x16_bf16 v[32:47], a[208:211], v[136:139], v[32:47]\n"
"ds_read_b64_tr_b16 a[192:193], v20 offset:0\n"
"ds_read_b64_tr_b16 a[194:195], v20 offset:512\n"
"s_waitcnt lgkmcnt(6)\n"
"v_mfma_f32_32x32x16_bf16 v[48:63], a[212:215], v[136:139], v[48:63]\n"
"ds_read_b64_tr_b16 a[196:197], v21 offset:0\n"
"ds_read_b64_tr_b16 a[198:199], v21 offset:512\n"
"s_waitcnt lgkmcnt(6)\n"
"v_mfma_f32_32x32x16_bf16 v[32:47], a[216:219], v[140:143], v[32:47]\n"
"ds_read_b64_tr_b16 a[200:201], v20 offset:2048\n"
"ds_read_b64_tr_b16 a[202:203], v20 offset:2560\n"
"s_waitcnt lgkmcnt(6)\n"
"v_mfma_f32_32x32x16_bf16 v[48:63], a[220:223], v[140:143], v[48:63]\n"
"ds_read_b64_tr_b16 a[204:205], v21 offset:2048\n"
"ds_read_b64_tr_b16 a[206:207], v21 offset:2560\n"
"s_waitcnt lgkmcnt(6)\n"
"v_mfma_f32_32x32x16_bf16 v[160:175], a[192:195], v[80:83], 0\n"
"s_waitcnt vmcnt(23)\n"
"s_barrier\n"
"ds_read_b64_tr_b16 a[208:209], v20 offset:4096\n"
"ds_read_b64_tr_b16 a[210:211], v20 offset:4608\n"
"s_add_u32 m0, s60, 131072\n"
"s_add_u32 s87, s77, 0\n"
"buffer_load_dwordx4 v6, s[52:55], s87 offen lds\n"
"s_waitcnt lgkmcnt(6)\n"
"v_mfma_f32_32x32x16_bf16 v[176:191], a[196:199], v[80:83], 0\n"
"ds_read_b64_tr_b16 a[212:213], v21 offset:4096\n"
"ds_read_b64_tr_b16 a[214:215], v21 offset:4608\n"
"s_waitcnt lgkmcnt(6)\n"
"v_mfma_f32_32x32x16_bf16 v[160:175], a[200:203], v[84:87], v[160:175]\n"
"ds_read_b64_tr_b16 a[216:217], v20 offset:6144\n"
"ds_read_b64_tr_b16 a[218:219], v20 offset:6656\n"
"s_add_u32 m0, s60, 135168\n"
"s_add_u32 s87, s77, 28672\n"
"buffer_load_dwordx4 v6, s[52:55], s87 offen lds\n"
"s_waitcnt lgkmcnt(6)\n"
"v_mfma_f32_32x32x16_bf16 v[176:191], a[204:207], v[84:87], v[176:191]\n"
"ds_read_b64_tr_b16 a[220:221], v21 offset:6144\n"
"ds_read_b64_tr_b16 a[222:223], v21 offset:6656\n"
"s_waitcnt lgkmcnt(6)\n"
"v_mfma_f32_32x32x16_bf16 v[160:175], a[208:211], v[88:91], v[160:175]\n"
"ds_read_b64_tr_b16 a[192:193], v20 offset:8192\n"
"ds_read_b64_tr_b16 a[194:195], v20 offset:8704\n"
"v_cvt_pk_bf16_f32 v192, v32, v33\n"
"s_add_u32 m0, s60, 139264\n"
"s_add_u32 s87, s77, 57344\n"
"buffer_load_dwordx4 v6, s[52:55], s87 offen lds\n"
"s_waitcnt lgkmcnt(6)\n"
"v_mfma_f32_32x32x16_bf16 v[176:191], a[212:215], v[88:91], v[176:191]\n"
"ds_read_b64_tr_b16 a[196:197], v21 offset:8192\n"
"ds_read_b64_tr_b16 a[198:199], v21 offset:8704\n"
"v_cvt_pk_bf16_f32 v193, v34, v35\n"
"s_waitcnt lgkmcnt(6)\n"
"v_mfma_f32_32x32x16_bf16 v[160:175], a[216:219], v[92:95], v[160:175]\n"
"ds_read_b64_tr_b16 a[200:201], v20 offset:10240\n"
"ds_read_b64_tr_b16 a[202:203], v20 offset:10752\n"
"v_cvt_pk_bf16_f32 v194, v36, v37\n"
"s_add_u32 m0, s60, 143360\n"
"s_add_u32 s87, s77, 86016\n"
"buffer_load_dwordx4 v6, s[52:55], s87 offen lds\n"
"s_waitcnt lgkmcnt(6)\n"
"v_mfma_f32_32x32x16_bf16 v[176:191], a[220:223], v[92:95], v[176:191]\n"
"ds_read_b64_tr_b16 a[204:205], v21 offset:10240\n"
"ds_read_b64_tr_b16 a[206:207], v21 offset:10752\n"
"v_cvt_pk_bf16_f32 v195, v38, v39\n"
"s_waitcnt lgkmcnt(6)\n"
"v_mfma_f32_32x32x16_bf16 v[160:175], a[192:195], v[96:99], v[160:175]\n"
"ds_read_b64_tr_b16 a[208:209], v20 offset:12288\n"
"ds_read_b64_tr_b16 a[210:211], v20 offset:12800\n"
"s_add_u32 s71, s62, 0\n"
"s_add_u32 m0, s60, 147456\n"
"s_add_u32 s87, s77, 114688\n"
"buffer_load_dwordx4 v6, s[52:55], s87 offen lds\n"
"s_waitcnt lgkmcnt(6)\n"
"v_mfma_f32_32x32x16_bf16 v[176:191], a[196:199], v[96:99], v[176:191]\n"
"ds_read_b64_tr_b16 a[212:213], v21 offset:12288\n"
"ds_read_b64_tr_b16 a[214:215], v21 offset:12800\n"
"buffer_store_dwordx4 v[192:195], v12, s[56:59], s71 offen sc1\n"
"s_waitcnt lgkmcnt(6)\n"
"v_mfma_f32_32x32x16_bf16 v[160:175], a[200:203], v[100:103], v[160:175]\n"
"ds_read_b64_tr_b16 a[216:217], v20 offset:14336\n"
"ds_read_b64_tr_b16 a[218:219], v20 offset:14848\n"
"v_cvt_pk_bf16_f32 v196, v40, v41\n"
"s_add_u32 m0, s60, 151552\n"
"s_add_u32 s87, s77, 143360\n"
"buffer_load_dwordx4 v6, s[52:55], s87 offen lds\n"
"s_waitcnt lgkmcnt(6)\n"
"v_mfma_f32_32x32x16_bf16 v[176:191], a[204:207], v[100:103], v[176:191]\n"
"ds_read_b64_tr_b16 a[220:221], v21 offset:14336\n"
"ds_read_b64_tr_b16 a[222:223], v21 offset:14848\n"
"v_cvt_pk_bf16_f32 v197, v42, v43\n"
"s_waitcnt lgkmcnt(6)\n"
"v_mfma_f32_32x32x16_bf16 v[160:175], a[208:211], v[104:107], v[160:175]\n"
"ds_read_b64_tr_b16 a[192:193], v20 offset:16384\n"
"ds_read_b64_tr_b16 a[194:195], v20 offset:16896\n"
"v_cvt_pk_bf16_f32 v198, v44, v45\n"
"s_add_u32 m0, s60, 155648\n"
"s_add_u32 s87, s77, 172032\n"
"buffer_load_dwordx4 v6, s[52:55], s87 offen lds\n"
"s_waitcnt lgkmcnt(6)\n"
"v_mfma_f32_32x32x16_bf16 v[176:191], a[212:215], v[104:107], v[176:191]\n"
"ds_read_b64_tr_b16 a[196:197], v21 offset:16384\n"
"ds_read_b64_tr_b16 a[198:199], v21 offset:16896\n"
"v_cvt_pk_bf16_f32 v199, v46, v47\n"
"s_waitcnt lgkmcnt(6)\n"
"v_mfma_f32_32x32x16_bf16 v[160:175], a[216:219], v[108:111], v[160:175]\n"
"ds_read_b64_tr_b16 a[200:201], v20 offset:18432\n"
"ds_read_b64_tr_b16 a[202:203], v20 offset:18944\n"
"s_add_u32 s71, s62, 4096\n"
"s_add_u32 m0, s60, 159744\n"
"s_add_u32 s87, s77, 200704\n"
"buffer_load_dwordx4 v6, s[52:55], s87 offen lds\n"
"s_waitcnt lgkmcnt(6)\n"
"v_mfma_f32_32x32x16_bf16 v[176:191], a[220:223], v[108:111], v[176:191]\n"
"ds_read_b64_tr_b16 a[204:205], v21 offset:18432\n"
"ds_read_b64_tr_b16 a[206:207], v21 offset:18944\n"
"buffer_store_dwordx4 v[196:199], v12, s[56:59], s71 offen sc1\n"
"s_waitcnt lgkmcnt(6)\n"
"v_mfma_f32_32x32x16_bf16 v[160:175], a[192:195], v[112:115], v[160:175]\n"
"ds_read_b64_tr_b16 a[208:209], v20 offset:20480\n"
"ds_read_b64_tr_b16 a[210:211], v20 offset:20992\n"
"v_cvt_pk_bf16_f32 v200, v48, v49\n"
"s_waitcnt lgkmcnt(6)\n"
"v_mfma_f32_32x32x16_bf16 v[176:191], a[196:199], v[112:115], v[176:191]\n"
"ds_read_b64_tr_b16 a[212:213], v21 offset:20480\n"
"ds_read_b64_tr_b16 a[214:215], v21 offset:20992\n"
"v_cvt_pk_bf16_f32 v201, v50, v51\n"
"s_waitcnt lgkmcnt(6)\n"
"v_mfma_f32_32x32x16_bf16 v[160:175], a[200:203], v[116:119], v[160:175]\n"
"ds_read_b64_tr_b16 a[216:217], v20 offset:22528\n"
"ds_read_b64_tr_b16 a[218:219], v20 offset:23040\n"
"v_cvt_pk_bf16_f32 v202, v52, v53\n"
"s_waitcnt lgkmcnt(6)\n"
"v_mfma_f32_32x32x16_bf16 v[176:191], a[204:207], v[116:119], v[176:191]\n"
"ds_read_b64_tr_b16 a[220:221], v21 offset:22528\n"
"ds_read_b64_tr_b16 a[222:223], v21 offset:23040\n"
"v_cvt_pk_bf16_f32 v203, v54, v55\n"
"s_waitcnt lgkmcnt(6)\n"
"v_mfma_f32_32x32x16_bf16 v[160:175], a[208:211], v[120:123], v[160:175]\n"
"ds_read_b64_tr_b16 a[192:193], v20 offset:24576\n"
"ds_read_b64_tr_b16 a[194:195], v20 offset:25088\n"
"s_add_u32 s71, s62, 8192\n"
"s_waitcnt lgkmcnt(6)\n"
"v_mfma_f32_32x32x16_bf16 v[176:191], a[212:215], v[120:123], v[176:191]\n"
"ds_read_b64_tr_b16 a[196:197], v21 offset:24576\n"
"ds_read_b64_tr_b16 a[198:199], v21 offset:25088\n"
"buffer_store_dwordx4 v[200:203], v12, s[56:59], s71 offen sc1\n"
"s_waitcnt lgkmcnt(6)\n"
"v_mfma_f32_32x32x16_bf16 v[160:175], a[216:219], v[124:127], v[160:175]\n"
"ds_read_b64_tr_b16 a[200:201], v20 offset:26624\n"
"ds_read_b64_tr_b16 a[202:203], v20 offset:27136\n"
"v_cvt_pk_bf16_f32 v204, v56, v57\n"
"s_waitcnt lgkmcnt(6)\n"
"v_mfma_f32_32x32x16_bf16 v[176:191], a[220:223], v[124:127], v[176:191]\n"
"ds_read_b64_tr_b16 a[204:205], v21 offset:26624\n"
"ds_read_b64_tr_b16 a[206:207], v21 offset:27136\n"
"v_cvt_pk_bf16_f32 v205, v58, v59\n"
"s_waitcnt lgkmcnt(6)\n"
"v_mfma_f32_32x32x16_bf16 v[160:175], a[192:195], v[128:131], v[160:175]\n"
"ds_read_b64_tr_b16 a[208:209], v20 offset:28672\n"
"ds_read_b64_tr_b16 a[210:211], v20 offset:29184\n"
"v_cvt_pk_bf16_f32 v206, v60, v61\n"
"s_waitcnt lgkmcnt(6)\n"
"v_mfma_f32_32x32x16_bf16 v[176:191], a[196:199], v[128:131], v[176:191]\n"
"ds_read_b64_tr_b16 a[212:213], v21 offset:28672\n"
"ds_read_b64_tr_b16 a[214:215], v21 offset:29184\n"
"v_cvt_pk_bf16_f32 v207, v62, v63\n"
"s_waitcnt lgkmcnt(6)\n"
"v_mfma_f32_32x32x16_bf16 v[160:175], a[200:203], v[132:135], v[160:175]\n"
"ds_read_b64_tr_b16 a[216:217], v20 offset:30720\n"
"ds_read_b64_tr_b16 a[218:219], v20 offset:31232\n"
"s_add_u32 s71, s62, 12288\n"
"s_waitcnt lgkmcnt(6)\n"
"v_mfma_f32_32x32x16_bf16 v[176:191], a[204:207], v[132:135], v[176:191]\n"
"ds_read_b64_tr_b16 a[220:221], v21 offset:30720\n"
"ds_read_b64_tr_b16 a[222:223], v21 offset:31232\n"
"v_add_u32 v20, 32768, v7\n"
"v_xor_b32 v21, 64, v20\n"
"buffer_store_dwordx4 v[204:207], v12, s[56:59], s71 offen sc1\n"
"s_waitcnt lgkmcnt(6)\n"
"v_mfma_f32_32x32x16_bf16 v[160:175], a[208:211], v[136:139], v[160:175]\n"
"ds_read_b64_tr_b16 a[192:193], v20 offset:0\n"
"ds_read_b64_tr_b16 a[194:195], v20 offset:512\n"
"s_waitcnt lgkmcnt(6)\n"
"v_mfma_f32_32x32x16_bf16 v[176:191], a[212:215], v[136:139], v[176:191]\n"
"ds_read_b64_tr_b16 a[196:197], v21 offset:0\n"
"ds_read_b64_tr_b16 a[198:199], v21 offset:512\n"
"s_waitcnt lgkmcnt(6)\n"
"v_mfma_f32_32x32x16_bf16 v[160:175], a[216:219], v[140:143], v[160:175]\n"
"ds_read_b64_tr_b16 a[200:201], v20 offset:2048\n"
"ds_read_b64_tr_b16 a[202:203], v20 offset:2560\n"
"s_waitcnt lgkmcnt(6)\n"
"v_mfma_f32_32x32x16_bf16 v[176:191], a[220:223], v[140:143], v[176:191]\n"
"ds_read_b64_tr_b16 a[204:205], v21 offset:2048\n"
"ds_read_b64_tr_b16 a[206:207], v21 offset:2560\n"
"s_waitcnt lgkmcnt(6)\n"
"v_mfma_f32_32x32x16_bf16 v[32:47], a[192:195], v[80:83], 0\n"
"s_waitcnt vmcnt(27)\n"
"s_barrier\n"
"ds_read_b64_tr_b16 a[208:209], v20 offset:4096\n"
"ds_read_b64_tr_b16 a[210:211], v20 offset:4608\n"
"s_add_u32 m0, s60, 0\n"
"s_add_u32 s87, s78, 0\n"
"buffer_load_dwordx4 v6, s[52:55], s87 offen lds\n"
"s_waitcnt lgkmcnt(6)\n"
"v_mfma_f32_32x32x16_bf16 v[48:63], a[196:199], v[80:83], 0\n"
"ds_read_b64_tr_b16 a[212:213], v21 offset:4096\n"
"ds_read_b64_tr_b16 a[214:215], v21 offset:4608\n"
"s_waitcnt lgkmcnt(6)\n"
"v_mfma_f32_32x32x16_bf16 v[32:47], a[200:203], v[84:87], v[32:47]\n"
"ds_read_b64_tr_b16 a[216:217], v20 offset:6144\n"
"ds_read_b64_tr_b16 a[218:219], v20 offset:6656\n"
"s_add_u32 m0, s60, 4096\n"
"s_add_u32 s87, s78, 28672\n"
"buffer_load_dwordx4 v6, s[52:55], s87 offen lds\n"
"s_waitcnt lgkmcnt(6)\n"
"v_mfma_f32_32x32x16_bf16 v[48:63], a[204:207], v[84:87], v[48:63]\n"
"ds_read_b64_tr_b16 a[220:221], v21 offset:6144\n"
"ds_read_b64_tr_b16 a[222:223], v21 offset:6656\n"
"s_waitcnt lgkmcnt(6)\n"
"v_mfma_f32_32x32x16_bf16 v[32:47], a[208:211], v[88:91], v[32:47]\n"
"ds_read_b64_tr_b16 a[192:193], v20 offset:8192\n"
"ds_read_b64_tr_b16 a[194:195], v20 offset:8704\n"
"v_cvt_pk_bf16_f32 v208, v160, v161\n"
"s_add_u32 m0, s60, 8192\n"
"s_add_u32 s87, s78, 57344\n"
"buffer_load_dwordx4 v6, s[52:55], s87 offen lds\n"
"s_waitcnt lgkmcnt(6)\n"
"v_mfma_f32_32x32x16_bf16 v[48:63], a[212:215], v[88:91], v[48:63]\n"
"ds_read_b64_tr_b16 a[196:197], v21 offset:8192\n"
"ds_read_b64_tr_b16 a[198:199], v21 offset:8704\n"
"v_cvt_pk_bf16_f32 v209, v162, v163\n"
"s_waitcnt lgkmcnt(6)\n"
"v_mfma_f32_32x32x16_bf16 v[32:47], a[216:219], v[92:95], v[32:47]\n"
"ds_read_b64_tr_b16 a[200:201], v20 offset:10240\n"
"ds_read_b64_tr_b16 a[202:203], v20 offset:10752\n"
"v_cvt_pk_bf16_f32 v210, v164, v165\n"
"s_add_u32 m0, s60, 12288\n"
"s_add_u32 s87, s78, 86016\n"
"buffer_load_dwordx4 v6, s[52:55], s87 offen lds\n"
"s_waitcnt lgkmcnt(6)\n"
"v_mfma_f32_32x32x16_bf16 v[48:63], a[220:223], v[92:95], v[48:63]\n"
"ds_read_b64_tr_b16 a[204:205], v21 offset:10240\n"
"ds_read_b64_tr_b16 a[206:207], v21 offset:10752\n"
"v_cvt_pk_bf16_f32 v211, v166, v167\n"
"s_waitcnt lgkmcnt(6)\n"
"v_mfma_f32_32x32x16_bf16 v[32:47], a[192:195], v[96:99], v[32:47]\n"
"ds_read_b64_tr_b16 a[208:209], v20 offset:12288\n"
"ds_read_b64_tr_b16 a[210:211], v20 offset:12800\n"
"s_add_u32 s71, s62, 16384\n"
"s_add_u32 m0, s60, 16384\n"
"s_add_u32 s87, s78, 114688\n"
"buffer_load_dwordx4 v6, s[52:55], s87 offen lds\n"
"s_waitcnt lgkmcnt(6)\n"
"v_mfma_f32_32x32x16_bf16 v[48:63], a[196:199], v[96:99], v[48:63]\n"
"ds_read_b64_tr_b16 a[212:213], v21 offset:12288\n"
"ds_read_b64_tr_b16 a[214:215], v21 offset:12800\n"
"buffer_store_dwordx4 v[208:211], v12, s[56:59], s71 offen sc1\n"
"s_waitcnt lgkmcnt(6)\n"
"v_mfma_f32_32x32x16_bf16 v[32:47], a[200:203], v[100:103], v[32:47]\n"
"ds_read_b64_tr_b16 a[216:217], v20 offset:14336\n"
"ds_read_b64_tr_b16 a[218:219], v20 offset:14848\n"
"v_cvt_pk_bf16_f32 v212, v168, v169\n"
"s_add_u32 m0, s60, 20480\n"
"s_add_u32 s87, s78, 143360\n"
"buffer_load_dwordx4 v6, s[52:55], s87 offen lds\n"
"s_waitcnt lgkmcnt(6)\n"
"v_mfma_f32_32x32x16_bf16 v[48:63], a[204:207], v[100:103], v[48:63]\n"
"ds_read_b64_tr_b16 a[220:221], v21 offset:14336\n"
"ds_read_b64_tr_b16 a[222:223], v21 offset:14848\n"
"v_cvt_pk_bf16_f32 v213, v170, v171\n"
"s_waitcnt lgkmcnt(6)\n"
"v_mfma_f32_32x32x16_bf16 v[32:47], a[208:211], v[104:107], v[32:47]\n"
"ds_read_b64_tr_b16 a[192:193], v20 offset:16384\n"
"ds_read_b64_tr_b16 a[194:195], v20 offset:16896\n"
"v_cvt_pk_bf16_f32 v214, v172, v173\n"
"s_add_u32 m0, s60, 24576\n"
"s_add_u32 s87, s78, 172032\n"
"buffer_load_dwordx4 v6, s[52:55], s87 offen lds\n"
"s_waitcnt lgkmcnt(6)\n"
"v_mfma_f32_32x32x16_bf16 v[48:63], a[212:215], v[104:107], v[48:63]\n"
"ds_read_b64_tr_b16 a[196:197], v21 offset:16384\n"
"ds_read_b64_tr_b16 a[198:199], v21 offset:16896\n"
"v_cvt_pk_bf16_f32 v215, v174, v175\n"
"s_waitcnt lgkmcnt(6)\n"
"v_mfma_f32_32x32x16_bf16 v[32:47], a[216:219], v[108:111], v[32:47]\n"
"ds_read_b64_tr_b16 a[200:201], v20 offset:18432\n"
"ds_read_b64_tr_b16 a[202:203], v20 offset:18944\n"
"s_add_u32 s71, s62, 20480\n"
"s_add_u32 m0, s60, 28672\n"
"s_add_u32 s87, s78, 200704\n"
"buffer_load_dwordx4 v6, s[52:55], s87 offen lds\n"
"s_waitcnt lgkmcnt(6)\n"
"v_mfma_f32_32x32x16_bf16 v[48:63], a[220:223], v[108:111], v[48:63]\n"
"ds_read_b64_tr_b16 a[204:205], v21 offset:18432\n"
"ds_read_b64_tr_b16 a[206:207], v21 offset:18944\n"
"buffer_store_dwordx4 v[212:215], v12, s[56:59], s71 offen sc1\n"
"s_waitcnt lgkmcnt(6)\n"
"v_mfma_f32_32x32x16_bf16 v[32:47], a[192:195], v[112:115], v[32:47]\n"
"ds_read_b64_tr_b16 a[208:209], v20 offset:20480\n"
"ds_read_b64_tr_b16 a[210:211], v20 offset:20992\n"
"v_cvt_pk_bf16_f32 v216, v176, v177\n"
"s_waitcnt lgkmcnt(6)\n"
"v_mfma_f32_32x32x16_bf16 v[48:63], a[196:199], v[112:115], v[48:63]\n"
"ds_read_b64_tr_b16 a[212:213], v21 offset:20480\n"
"ds_read_b64_tr_b16 a[214:215], v21 offset:20992\n"
"v_cvt_pk_bf16_f32 v217, v178, v179\n"
"s_waitcnt lgkmcnt(6)\n"
"v_mfma_f32_32x32x16_bf16 v[32:47], a[200:203], v[116:119], v[32:47]\n"
"ds_read_b64_tr_b16 a[216:217], v20 offset:22528\n"
"ds_read_b64_tr_b16 a[218:219], v20 offset:23040\n"
"v_cvt_pk_bf16_f32 v218, v180, v181\n"
"s_waitcnt lgkmcnt(6)\n"
"v_mfma_f32_32x32x16_bf16 v[48:63], a[204:207], v[116:119], v[48:63]\n"
"ds_read_b64_tr_b16 a[220:221], v21 offset:22528\n"
"ds_read_b64_tr_b16 a[222:223], v21 offset:23040\n"
"v_cvt_pk_bf16_f32 v219, v182, v183\n"
"s_waitcnt lgkmcnt(6)\n"
"v_mfma_f32_32x32x16_bf16 v[32:47], a[208:211], v[120:123], v[32:47]\n"
"ds_read_b64_tr_b16 a[192:193], v20 offset:24576\n"
"ds_read_b64_tr_b16 a[194:195], v20 offset:25088\n"
"s_add_u32 s71, s62, 24576\n"
"s_waitcnt lgkmcnt(6)\n"
"v_mfma_f32_32x32x16_bf16 v[48:63], a[212:215], v[120:123], v[48:63]\n"
"ds_read_b64_tr_b16 a[196:197], v21 offset:24576\n"
"ds_read_b64_tr_b16 a[198:199], v21 offset:25088\n"
"buffer_store_dwordx4 v[216:219], v12, s[56:59], s71 offen sc1\n"
"s_waitcnt lgkmcnt(6)\n"
"v_mfma_f32_32x32x16_bf16 v[32:47], a[216:219], v[124:127], v[32:47]\n"
"ds_read_b64_tr_b16 a[200:201], v20 offset:26624\n"
"ds_read_b64_tr_b16 a[202:203], v20 offset:27136\n"
"v_cvt_pk_bf16_f32 v220, v184, v185\n"
"s_waitcnt lgkmcnt(6)\n"
"v_mfma_f32_32x32x16_bf16 v[48:63], a[220:223], v[124:127], v[48:63]\n"
"ds_read_b64_tr_b16 a[204:205], v21 offset:26624\n"
"ds_read_b64_tr_b16 a[206:207], v21 offset:27136\n"
"v_cvt_pk_bf16_f32 v221, v186, v187\n"
"s_waitcnt lgkmcnt(6)\n"
"v_mfma_f32_32x32x16_bf16 v[32:47], a[192:195], v[128:131], v[32:47]\n"
"ds_read_b64_tr_b16 a[208:209], v20 offset:28672\n"
"ds_read_b64_tr_b16 a[210:211], v20 offset:29184\n"
"v_cvt_pk_bf16_f32 v222, v188, v189\n"
"s_waitcnt lgkmcnt(6)\n"
"v_mfma_f32_32x32x16_bf16 v[48:63], a[196:199], v[128:131], v[48:63]\n"
"ds_read_b64_tr_b16 a[212:213], v21 offset:28672\n"
"ds_read_b64_tr_b16 a[214:215], v21 offset:29184\n"
"v_cvt_pk_bf16_f32 v223, v190, v191\n"
"s_waitcnt lgkmcnt(6)\n"
"v_mfma_f32_32x32x16_bf16 v[32:47], a[200:203], v[132:135], v[32:47]\n"
"ds_read_b64_tr_b16 a[216:217], v20 offset:30720\n"
"ds_read_b64_tr_b16 a[218:219], v20 offset:31232\n"
"s_add_u32 s71, s62, 28672\n"
"s_waitcnt lgkmcnt(6)\n"
"v_mfma_f32_32x32x16_bf16 v[48:63], a[204:207], v[132:135], v[48:63]\n"
"ds_read_b64_tr_b16 a[220:221], v21 offset:30720\n"
"ds_read_b64_tr_b16 a[222:223], v21 offset:31232\n"
"v_add_u32 v20, 65536, v7\n"
"v_xor_b32 v21, 64, v20\n"
"buffer_store_dwordx4 v[220:223], v12, s[56:59], s71 offen sc1\n"
"s_waitcnt lgkmcnt(6)\n"
"v_mfma_f32_32x32x16_bf16 v[32:47], a[208:211], v[136:139], v[32:47]\n"
"ds_read_b64_tr_b16 a[192:193], v20 offset:0\n"
"ds_read_b64_tr_b16 a[194:195], v20 offset:512\n"
"s_waitcnt lgkmcnt(6)\n"
"v_mfma_f32_32x32x16_bf16 v[48:63], a[212:215], v[136:139], v[48:63]\n"
"ds_read_b64_tr_b16 a[196:197], v21 offset:0\n"
"ds_read_b64_tr_b16 a[198:199], v21 offset:512\n"
"s_waitcnt lgkmcnt(6)\n"
"v_mfma_f32_32x32x16_bf16 v[32:47], a[216:219], v[140:143], v[32:47]\n"
"ds_read_b64_tr_b16 a[200:201], v20 offset:2048\n"
"ds_read_b64_tr_b16 a[202:203], v20 offset:2560\n"
"s_waitcnt lgkmcnt(6)\n"
"v_mfma_f32_32x32x16_bf16 v[48:63], a[220:223], v[140:143], v[48:63]\n"
"ds_read_b64_tr_b16 a[204:205], v21 offset:2048\n"
"ds_read_b64_tr_b16 a[206:207], v21 offset:2560\n"
"s_waitcnt lgkmcnt(6)\n"
"v_mfma_f32_32x32x16_bf16 v[160:175], a[192:195], v[80:83], 0\n"
"s_waitcnt vmcnt(31)\n"
"s_barrier\n"
"ds_read_b64_tr_b16 a[208:209], v20 offset:4096\n"
"ds_read_b64_tr_b16 a[210:211], v20 offset:4608\n"
"s_waitcnt lgkmcnt(6)\n"
"v_mfma_f32_32x32x16_bf16 v[176:191], a[196:199], v[80:83], 0\n"
"ds_read_b64_tr_b16 a[212:213], v21 offset:4096\n"
"ds_read_b64_tr_b16 a[214:215], v21 offset:4608\n"
"s_waitcnt lgkmcnt(6)\n"
"v_mfma_f32_32x32x16_bf16 v[160:175], a[200:203], v[84:87], v[160:175]\n"
"ds_read_b64_tr_b16 a[216:217], v20 offset:6144\n"
"ds_read_b64_tr_b16 a[218:219], v20 offset:6656\n"
"s_waitcnt lgkmcnt(6)\n"
"v_mfma_f32_32x32x16_bf16 v[176:191], a[204:207], v[84:87], v[176:191]\n"
"ds_read_b64_tr_b16 a[220:221], v21 offset:6144\n"
"ds_read_b64_tr_b16 a[222:223], v21 offset:6656\n"
"s_waitcnt lgkmcnt(6)\n"
"v_mfma_f32_32x32x16_bf16 v[160:175], a[208:211], v[88:91], v[160:175]\n"
"ds_read_b64_tr_b16 a[192:193], v20 offset:8192\n"
"ds_read_b64_tr_b16 a[194:195], v20 offset:8704\n"
"v_cvt_pk_bf16_f32 v192, v32, v33\n"
"s_waitcnt lgkmcnt(6)\n"
"v_mfma_f32_32x32x16_bf16 v[176:191], a[212:215], v[88:91], v[176:191]\n"
"ds_read_b64_tr_b16 a[196:197], v21 offset:8192\n"
"ds_read_b64_tr_b16 a[198:199], v21 offset:8704\n"
"v_cvt_pk_bf16_f32 v193, v34, v35\n"
"s_waitcnt lgkmcnt(6)\n"
"v_mfma_f32_32x32x16_bf16 v[160:175], a[216:219], v[92:95], v[160:175]\n"
"ds_read_b64_tr_b16 a[200:201], v20 offset:10240\n"
"ds_read_b64_tr_b16 a[202:203], v20 offset:10752\n"
"v_cvt_pk_bf16_f32 v194, v36, v37\n"
"s_waitcnt lgkmcnt(6)\n"
"v_mfma_f32_32x32x16_bf16 v[176:191], a[220:223], v[92:95], v[176:191]\n"
"ds_read_b64_tr_b16 a[204:205], v21 offset:10240\n"
"ds_read_b64_tr_b16 a[206:207], v21 offset:10752\n"
"v_cvt_pk_bf16_f32 v195, v38, v39\n"
"s_waitcnt lgkmcnt(6)\n"
"v_mfma_f32_32x32x16_bf16 v[160:175], a[192:195], v[96:99], v[160:175]\n"
"ds_read_b64_tr_b16 a[208:209], v20 offset:12288\n"
"ds_read_b64_tr_b16 a[210:211], v20 offset:12800\n"
"s_add_u32 s71, s62, 32768\n"
"s_waitcnt lgkmcnt(6)\n"
"v_mfma_f32_32x32x16_bf16 v[176:191], a[196:199], v[96:99], v[176:191]\n"
"ds_read_b64_tr_b16 a[212:213], v21 offset:12288\n"
"ds_read_b64_tr_b16 a[214:215], v21 offset:12800\n"
"buffer_store_dwordx4 v[192:195], v12, s[56:59], s71 offen sc1\n"
"s_waitcnt lgkmcnt(6)\n"
"v_mfma_f32_32x32x16_bf16 v[160:175], a[200:203], v[100:103], v[160:175]\n"
"ds_read_b64_tr_b16 a[216:217], v20 offset:14336\n"
"ds_read_b64_tr_b16 a[218:219], v20 offset:14848\n"
"v_cvt_pk_bf16_f32 v196, v40, v41\n"
"s_waitcnt lgkmcnt(6)\n"
"v_mfma_f32_32x32x16_bf16 v[176:191], a[204:207], v[100:103], v[176:191]\n"
"ds_read_b64_tr_b16 a[220:221], v21 offset:14336\n"
"ds_read_b64_tr_b16 a[222:223], v21 offset:14848\n"
"v_cvt_pk_bf16_f32 v197, v42, v43\n"
"s_waitcnt lgkmcnt(6)\n"
"v_mfma_f32_32x32x16_bf16 v[160:175], a[208:211], v[104:107], v[160:175]\n"
"ds_read_b64_tr_b16 a[192:193], v20 offset:16384\n"
"ds_read_b64_tr_b16 a[194:195], v20 offset:16896\n"
"v_cvt_pk_bf16_f32 v198, v44, v45\n"
"s_waitcnt lgkmcnt(6)\n"
"v_mfma_f32_32x32x16_bf16 v[176:191], a[212:215], v[104:107], v[176:191]\n"
"ds_read_b64_tr_b16 a[196:197], v21 offset:16384\n"
"ds_read_b64_tr_b16 a[198:199], v21 offset:16896\n"
"v_cvt_pk_bf16_f32 v199, v46, v47\n"
"s_waitcnt lgkmcnt(6)\n"
"v_mfma_f32_32x32x16_bf16 v[160:175], a[216:219], v[108:111], v[160:175]\n"
"ds_read_b64_tr_b16 a[200:201], v20 offset:18432\n"
"ds_read_b64_tr_b16 a[202:203], v20 offset:18944\n"
"s_add_u32 s71, s62, 36864\n"
"s_waitcnt lgkmcnt(6)\n"
"v_mfma_f32_32x32x16_bf16 v[176:191], a[220:223], v[108:111], v[176:191]\n"
"ds_read_b64_tr_b16 a[204:205], v21 offset:18432\n"
"ds_read_b64_tr_b16 a[206:207], v21 offset:18944\n"
"buffer_store_dwordx4 v[196:199], v12, s[56:59], s71 offen sc1\n"
"s_waitcnt lgkmcnt(6)\n"
"v_mfma_f32_32x32x16_bf16 v[160:175], a[192:195], v[112:115], v[160:175]\n"
"ds_read_b64_tr_b16 a[208:209], v20 offset:20480\n"
"ds_read_b64_tr_b16 a[210:211], v20 offset:20992\n"
"v_cvt_pk_bf16_f32 v200, v48, v49\n"
"s_waitcnt lgkmcnt(6)\n"
"v_mfma_f32_32x32x16_bf16 v[176:191], a[196:199], v[112:115], v[176:191]\n"
"ds_read_b64_tr_b16 a[212:213], v21 offset:20480\n"
"ds_read_b64_tr_b16 a[214:215], v21 offset:20992\n"
"v_cvt_pk_bf16_f32 v201, v50, v51\n"
"s_waitcnt lgkmcnt(6)\n"
"v_mfma_f32_32x32x16_bf16 v[160:175], a[200:203], v[116:119], v[160:175]\n"
"ds_read_b64_tr_b16 a[216:217], v20 offset:22528\n"
"ds_read_b64_tr_b16 a[218:219], v20 offset:23040\n"
"v_cvt_pk_bf16_f32 v202, v52, v53\n"
"s_waitcnt lgkmcnt(6)\n"
"v_mfma_f32_32x32x16_bf16 v[176:191], a[204:207], v[116:119], v[176:191]\n"
"ds_read_b64_tr_b16 a[220:221], v21 offset:22528\n"
"ds_read_b64_tr_b16 a[222:223], v21 offset:23040\n"
"v_cvt_pk_bf16_f32 v203, v54, v55\n"
"s_waitcnt lgkmcnt(6)\n"
"v_mfma_f32_32x32x16_bf16 v[160:175], a[208:211], v[120:123], v[160:175]\n"
"ds_read_b64_tr_b16 a[192:193], v20 offset:24576\n"
"ds_read_b64_tr_b16 a[194:195], v20 offset:25088\n"
"s_add_u32 s71, s62, 40960\n"
"s_waitcnt lgkmcnt(6)\n"
"v_mfma_f32_32x32x16_bf16 v[176:191], a[212:215], v[120:123], v[176:191]\n"
"ds_read_b64_tr_b16 a[196:197], v21 offset:24576\n"
"ds_read_b64_tr_b16 a[198:199], v21 offset:25088\n"
"buffer_store_dwordx4 v[200:203], v12, s[56:59], s71 offen sc1\n"
"s_waitcnt lgkmcnt(6)\n"
"v_mfma_f32_32x32x16_bf16 v[160:175], a[216:219], v[124:127], v[160:175]\n"
"ds_read_b64_tr_b16 a[200:201], v20 offset:26624\n"
"ds_read_b64_tr_b16 a[202:203], v20 offset:27136\n"
"v_cvt_pk_bf16_f32 v204, v56, v57\n"
"s_waitcnt lgkmcnt(6)\n"
"v_mfma_f32_32x32x16_bf16 v[176:191], a[220:223], v[124:127], v[176:191]\n"
"ds_read_b64_tr_b16 a[204:205], v21 offset:26624\n"
"ds_read_b64_tr_b16 a[206:207], v21 offset:27136\n"
"v_cvt_pk_bf16_f32 v205, v58, v59\n"
"s_waitcnt lgkmcnt(6)\n"
"v_mfma_f32_32x32x16_bf16 v[160:175], a[192:195], v[128:131], v[160:175]\n"
"ds_read_b64_tr_b16 a[208:209], v20 offset:28672\n"
"ds_read_b64_tr_b16 a[210:211], v20 offset:29184\n"
"v_cvt_pk_bf16_f32 v206, v60, v61\n"
"s_waitcnt lgkmcnt(6)\n"
"v_mfma_f32_32x32x16_bf16 v[176:191], a[196:199], v[128:131], v[176:191]\n"
"ds_read_b64_tr_b16 a[212:213], v21 offset:28672\n"
"ds_read_b64_tr_b16 a[214:215], v21 offset:29184\n"
"v_cvt_pk_bf16_f32 v207, v62, v63\n"
"s_waitcnt lgkmcnt(6)\n"
"v_mfma_f32_32x32x16_bf16 v[160:175], a[200:203], v[132:135], v[160:175]\n"
"ds_read_b64_tr_b16 a[216:217], v20 offset:30720\n"
"ds_read_b64_tr_b16 a[218:219], v20 offset:31232\n"
"s_add_u32 s71, s62, 45056\n"
"s_waitcnt lgkmcnt(6)\n"
"v_mfma_f32_32x32x16_bf16 v[176:191], a[204:207], v[132:135], v[176:191]\n"
"ds_read_b64_tr_b16 a[220:221], v21 offset:30720\n"
"ds_read_b64_tr_b16 a[222:223], v21 offset:31232\n"
"v_add_u32 v20, 98304, v7\n"
"v_xor_b32 v21, 64, v20\n"
"buffer_store_dwordx4 v[204:207], v12, s[56:59], s71 offen sc1\n"
"s_waitcnt lgkmcnt(6)\n"
"v_mfma_f32_32x32x16_bf16 v[160:175], a[208:211], v[136:139], v[160:175]\n"
"ds_read_b64_tr_b16 a[192:193], v20 offset:0\n"
"ds_read_b64_tr_b16 a[194:195], v20 offset:512\n"
"s_waitcnt lgkmcnt(6)\n"
"v_mfma_f32_32x32x16_bf16 v[176:191], a[212:215], v[136:139], v[176:191]\n"
"ds_read_b64_tr_b16 a[196:197], v21 offset:0\n"
"ds_read_b64_tr_b16 a[198:199], v21 offset:512\n"
"s_waitcnt lgkmcnt(6)\n"
"v_mfma_f32_32x32x16_bf16 v[160:175], a[216:219], v[140:143], v[160:175]\n"
"ds_read_b64_tr_b16 a[200:201], v20 offset:2048\n"
"ds_read_b64_tr_b16 a[202:203], v20 offset:2560\n"
"s_waitcnt lgkmcnt(6)\n"
"v_mfma_f32_32x32x16_bf16 v[176:191], a[220:223], v[140:143], v[176:191]\n"
"ds_read_b64_tr_b16 a[204:205], v21 offset:2048\n"
"ds_read_b64_tr_b16 a[206:207], v21 offset:2560\n"
"s_waitcnt lgkmcnt(6)\n"
"v_mfma_f32_32x32x16_bf16 v[32:47], a[192:195], v[80:83], 0\n"
"s_waitcnt vmcnt(27)\n"
"s_barrier\n"
"ds_read_b64_tr_b16 a[208:209], v20 offset:4096\n"
"ds_read_b64_tr_b16 a[210:211], v20 offset:4608\n"
"s_waitcnt lgkmcnt(6)\n"
"v_mfma_f32_32x32x16_bf16 v[48:63], a[196:199], v[80:83], 0\n"
"ds_read_b64_tr_b16 a[212:213], v21 offset:4096\n"
"ds_read_b64_tr_b16 a[214:215], v21 offset:4608\n"
"s_waitcnt lgkmcnt(6)\n"
"v_mfma_f32_32x32x16_bf16 v[32:47], a[200:203], v[84:87], v[32:47]\n"
"ds_read_b64_tr_b16 a[216:217], v20 offset:6144\n"
"ds_read_b64_tr_b16 a[218:219], v20 offset:6656\n"
"s_waitcnt lgkmcnt(6)\n"
"v_mfma_f32_32x32x16_bf16 v[48:63], a[204:207], v[84:87], v[48:63]\n"
"ds_read_b64_tr_b16 a[220:221], v21 offset:6144\n"
"ds_read_b64_tr_b16 a[222:223], v21 offset:6656\n"
"s_waitcnt lgkmcnt(6)\n"
"v_mfma_f32_32x32x16_bf16 v[32:47], a[208:211], v[88:91], v[32:47]\n"
"ds_read_b64_tr_b16 a[192:193], v20 offset:8192\n"
"ds_read_b64_tr_b16 a[194:195], v20 offset:8704\n"
"v_cvt_pk_bf16_f32 v208, v160, v161\n"
"s_waitcnt lgkmcnt(6)\n"
"v_mfma_f32_32x32x16_bf16 v[48:63], a[212:215], v[88:91], v[48:63]\n"
"ds_read_b64_tr_b16 a[196:197], v21 offset:8192\n"
"ds_read_b64_tr_b16 a[198:199], v21 offset:8704\n"
"v_cvt_pk_bf16_f32 v209, v162, v163\n"
"s_waitcnt lgkmcnt(6)\n"
"v_mfma_f32_32x32x16_bf16 v[32:47], a[216:219], v[92:95], v[32:47]\n"
"ds_read_b64_tr_b16 a[200:201], v20 offset:10240\n"
"ds_read_b64_tr_b16 a[202:203], v20 offset:10752\n"
"v_cvt_pk_bf16_f32 v210, v164, v165\n"
"s_waitcnt lgkmcnt(6)\n"
"v_mfma_f32_32x32x16_bf16 v[48:63], a[220:223], v[92:95], v[48:63]\n"
"ds_read_b64_tr_b16 a[204:205], v21 offset:10240\n"
"ds_read_b64_tr_b16 a[206:207], v21 offset:10752\n"
"v_cvt_pk_bf16_f32 v211, v166, v167\n"
"s_waitcnt lgkmcnt(6)\n"
"v_mfma_f32_32x32x16_bf16 v[32:47], a[192:195], v[96:99], v[32:47]\n"
"ds_read_b64_tr_b16 a[208:209], v20 offset:12288\n"
"ds_read_b64_tr_b16 a[210:211], v20 offset:12800\n"
"s_add_u32 s71, s62, 49152\n"
"s_waitcnt lgkmcnt(6)\n"
"v_mfma_f32_32x32x16_bf16 v[48:63], a[196:199], v[96:99], v[48:63]\n"
"ds_read_b64_tr_b16 a[212:213], v21 offset:12288\n"
"ds_read_b64_tr_b16 a[214:215], v21 offset:12800\n"
"buffer_store_dwordx4 v[208:211], v12, s[56:59], s71 offen sc1\n"
"s_waitcnt lgkmcnt(6)\n"
"v_mfma_f32_32x32x16_bf16 v[32:47], a[200:203], v[100:103], v[32:47]\n"
"ds_read_b64_tr_b16 a[216:217], v20 offset:14336\n"
"ds_read_b64_tr_b16 a[218:219], v20 offset:14848\n"
"v_cvt_pk_bf16_f32 v212, v168, v169\n"
"s_waitcnt lgkmcnt(6)\n"
"v_mfma_f32_32x32x16_bf16 v[48:63], a[204:207], v[100:103], v[48:63]\n"
"ds_read_b64_tr_b16 a[220:221], v21 offset:14336\n"
"ds_read_b64_tr_b16 a[222:223], v21 offset:14848\n"
"v_cvt_pk_bf16_f32 v213, v170, v171\n"
"s_waitcnt lgkmcnt(6)\n"
"v_mfma_f32_32x32x16_bf16 v[32:47], a[208:211], v[104:107], v[32:47]\n"
"ds_read_b64_tr_b16 a[192:193], v20 offset:16384\n"
"ds_read_b64_tr_b16 a[194:195], v20 offset:16896\n"
"v_cvt_pk_bf16_f32 v214, v172, v173\n"
"s_waitcnt lgkmcnt(6)\n"
"v_mfma_f32_32x32x16_bf16 v[48:63], a[212:215], v[104:107], v[48:63]\n"
"ds_read_b64_tr_b16 a[196:197], v21 offset:16384\n"
"ds_read_b64_tr_b16 a[198:199], v21 offset:16896\n"
"v_cvt_pk_bf16_f32 v215, v174, v175\n"
"s_waitcnt lgkmcnt(6)\n"
"v_mfma_f32_32x32x16_bf16 v[32:47], a[216:219], v[108:111], v[32:47]\n"
"ds_read_b64_tr_b16 a[200:201], v20 offset:18432\n"
"ds_read_b64_tr_b16 a[202:203], v20 offset:18944\n"
"s_add_u32 s71, s62, 53248\n"
"s_waitcnt lgkmcnt(6)\n"
"v_mfma_f32_32x32x16_bf16 v[48:63], a[220:223], v[108:111], v[48:63]\n"
"ds_read_b64_tr_b16 a[204:205], v21 offset:18432\n"
"ds_read_b64_tr_b16 a[206:207], v21 offset:18944\n"
"buffer_store_dwordx4 v[212:215], v12, s[56:59], s71 offen sc1\n"
"s_waitcnt lgkmcnt(6)\n"
"v_mfma_f32_32x32x16_bf16 v[32:47], a[192:195], v[112:115], v[32:47]\n"
"ds_read_b64_tr_b16 a[208:209], v20 offset:20480\n"
"ds_read_b64_tr_b16 a[210:211], v20 offset:20992\n"
"v_cvt_pk_bf16_f32 v216, v176, v177\n"
"s_waitcnt lgkmcnt(6)\n"
"v_mfma_f32_32x32x16_bf16 v[48:63], a[196:199], v[112:115], v[48:63]\n"
"ds_read_b64_tr_b16 a[212:213], v21 offset:20480\n"
"ds_read_b64_tr_b16 a[214:215], v21 offset:20992\n"
"v_cvt_pk_bf16_f32 v217, v178, v179\n"
"s_waitcnt lgkmcnt(6)\n"
"v_mfma_f32_32x32x16_bf16 v[32:47], a[200:203], v[116:119], v[32:47]\n"
"ds_read_b64_tr_b16 a[216:217], v20 offset:22528\n"
"ds_read_b64_tr_b16 a[218:219], v20 offset:23040\n"
"v_cvt_pk_bf16_f32 v218, v180, v181\n"
"s_waitcnt lgkmcnt(6)\n"
"v_mfma_f32_32x32x16_bf16 v[48:63], a[204:207], v[116:119], v[48:63]\n"
"ds_read_b64_tr_b16 a[220:221], v21 offset:22528\n"
"ds_read_b64_tr_b16 a[222:223], v21 offset:23040\n"
"v_cvt_pk_bf16_f32 v219, v182, v183\n"
"s_waitcnt lgkmcnt(6)\n"
"v_mfma_f32_32x32x16_bf16 v[32:47], a[208:211], v[120:123], v[32:47]\n"
"ds_read_b64_tr_b16 a[192:193], v20 offset:24576\n"
"ds_read_b64_tr_b16 a[194:195], v20 offset:25088\n"
"s_add_u32 s71, s62, 57344\n"
"s_waitcnt lgkmcnt(6)\n"
"v_mfma_f32_32x32x16_bf16 v[48:63], a[212:215], v[120:123], v[48:63]\n"
"ds_read_b64_tr_b16 a[196:197], v21 offset:24576\n"
"ds_read_b64_tr_b16 a[198:199], v21 offset:25088\n"
"buffer_store_dwordx4 v[216:219], v12, s[56:59], s71 offen sc1\n"
"s_waitcnt lgkmcnt(6)\n"
"v_mfma_f32_32x32x16_bf16 v[32:47], a[216:219], v[124:127], v[32:47]\n"
"ds_read_b64_tr_b16 a[200:201], v20 offset:26624\n"
"ds_read_b64_tr_b16 a[202:203], v20 offset:27136\n"
"v_cvt_pk_bf16_f32 v220, v184, v185\n"
"s_waitcnt lgkmcnt(6)\n"
"v_mfma_f32_32x32x16_bf16 v[48:63], a[220:223], v[124:127], v[48:63]\n"
"ds_read_b64_tr_b16 a[204:205], v21 offset:26624\n"
"ds_read_b64_tr_b16 a[206:207], v21 offset:27136\n"
"v_cvt_pk_bf16_f32 v221, v186, v187\n"
"s_waitcnt lgkmcnt(6)\n"
"v_mfma_f32_32x32x16_bf16 v[32:47], a[192:195], v[128:131], v[32:47]\n"
"ds_read_b64_tr_b16 a[208:209], v20 offset:28672\n"
"ds_read_b64_tr_b16 a[210:211], v20 offset:29184\n"
"v_cvt_pk_bf16_f32 v222, v188, v189\n"
"s_waitcnt lgkmcnt(6)\n"
"v_mfma_f32_32x32x16_bf16 v[48:63], a[196:199], v[128:131], v[48:63]\n"
"ds_read_b64_tr_b16 a[212:213], v21 offset:28672\n"
"ds_read_b64_tr_b16 a[214:215], v21 offset:29184\n"
"v_cvt_pk_bf16_f32 v223, v190, v191\n"
"s_waitcnt lgkmcnt(6)\n"
"v_mfma_f32_32x32x16_bf16 v[32:47], a[200:203], v[132:135], v[32:47]\n"
"ds_read_b64_tr_b16 a[216:217], v20 offset:30720\n"
"ds_read_b64_tr_b16 a[218:219], v20 offset:31232\n"
"s_add_u32 s71, s62, 61440\n"
"s_waitcnt lgkmcnt(6)\n"
"v_mfma_f32_32x32x16_bf16 v[48:63], a[204:207], v[132:135], v[48:63]\n"
"ds_read_b64_tr_b16 a[220:221], v21 offset:30720\n"
"ds_read_b64_tr_b16 a[222:223], v21 offset:31232\n"
"v_add_u32 v20, 131072, v7\n"
"v_xor_b32 v21, 64, v20\n"
"buffer_store_dwordx4 v[220:223], v12, s[56:59], s71 offen sc1\n"
"s_waitcnt lgkmcnt(6)\n"
"v_mfma_f32_32x32x16_bf16 v[32:47], a[208:211], v[136:139], v[32:47]\n"
"ds_read_b64_tr_b16 a[192:193], v20 offset:0\n"
"ds_read_b64_tr_b16 a[194:195], v20 offset:512\n"
"s_waitcnt lgkmcnt(6)\n"
"v_mfma_f32_32x32x16_bf16 v[48:63], a[212:215], v[136:139], v[48:63]\n"
"ds_read_b64_tr_b16 a[196:197], v21 offset:0\n"
"ds_read_b64_tr_b16 a[198:199], v21 offset:512\n"
"s_waitcnt lgkmcnt(6)\n"
"v_mfma_f32_32x32x16_bf16 v[32:47], a[216:219], v[140:143], v[32:47]\n"
"ds_read_b64_tr_b16 a[200:201], v20 offset:2048\n"
"ds_read_b64_tr_b16 a[202:203], v20 offset:2560\n"
"s_waitcnt lgkmcnt(6)\n"
"v_mfma_f32_32x32x16_bf16 v[48:63], a[220:223], v[140:143], v[48:63]\n"
"ds_read_b64_tr_b16 a[204:205], v21 offset:2048\n"
"ds_read_b64_tr_b16 a[206:207], v21 offset:2560\n"
"s_waitcnt lgkmcnt(6)\n"
"v_mfma_f32_32x32x16_bf16 v[160:175], a[192:195], v[80:83], 0\n"
"s_waitcnt vmcnt(19)\n"
"s_barrier\n"
"ds_read_b64_tr_b16 a[208:209], v20 offset:4096\n"
"ds_read_b64_tr_b16 a[210:211], v20 offset:4608\n"
"s_waitcnt lgkmcnt(6)\n"
"v_mfma_f32_32x32x16_bf16 v[176:191], a[196:199], v[80:83], 0\n"
"ds_read_b64_tr_b16 a[212:213], v21 offset:4096\n"
"ds_read_b64_tr_b16 a[214:215], v21 offset:4608\n"
"s_waitcnt lgkmcnt(6)\n"
"v_mfma_f32_32x32x16_bf16 v[160:175], a[200:203], v[84:87], v[160:175]\n"
"ds_read_b64_tr_b16 a[216:217], v20 offset:6144\n"
"ds_read_b64_tr_b16 a[218:219], v20 offset:6656\n"
"s_waitcnt lgkmcnt(6)\n"
"v_mfma_f32_32x32x16_bf16 v[176:191], a[204:207], v[84:87], v[176:191]\n"
"ds_read_b64_tr_b16 a[220:221], v21 offset:6144\n"
"ds_read_b64_tr_b16 a[222:223], v21 offset:6656\n"
"s_waitcnt lgkmcnt(6)\n"
"v_mfma_f32_32x32x16_bf16 v[160:175], a[208:211], v[88:91], v[160:175]\n"
"ds_read_b64_tr_b16 a[192:193], v20 offset:8192\n"
"ds_read_b64_tr_b16 a[194:195], v20 offset:8704\n"
"v_cvt_pk_bf16_f32 v192, v32, v33\n"
"s_waitcnt lgkmcnt(6)\n"
"v_mfma_f32_32x32x16_bf16 v[176:191], a[212:215], v[88:91], v[176:191]\n"
"ds_read_b64_tr_b16 a[196:197], v21 offset:8192\n"
"ds_read_b64_tr_b16 a[198:199], v21 offset:8704\n"
"v_cvt_pk_bf16_f32 v193, v34, v35\n"
"s_waitcnt lgkmcnt(6)\n"
"v_mfma_f32_32x32x16_bf16 v[160:175], a[216:219], v[92:95], v[160:175]\n"
"ds_read_b64_tr_b16 a[200:201], v20 offset:10240\n"
"ds_read_b64_tr_b16 a[202:203], v20 offset:10752\n"
"v_cvt_pk_bf16_f32 v194, v36, v37\n"
"s_waitcnt lgkmcnt(6)\n"
"v_mfma_f32_32x32x16_bf16 v[176:191], a[220:223], v[92:95], v[176:191]\n"
"ds_read_b64_tr_b16 a[204:205], v21 offset:10240\n"
"ds_read_b64_tr_b16 a[206:207], v21 offset:10752\n"
"v_cvt_pk_bf16_f32 v195, v38, v39\n"
"s_waitcnt lgkmcnt(6)\n"
"v_mfma_f32_32x32x16_bf16 v[160:175], a[192:195], v[96:99], v[160:175]\n"
"ds_read_b64_tr_b16 a[208:209], v20 offset:12288\n"
"ds_read_b64_tr_b16 a[210:211], v20 offset:12800\n"
"s_add_u32 s71, s62, 65536\n"
"s_waitcnt lgkmcnt(6)\n"
"v_mfma_f32_32x32x16_bf16 v[176:191], a[196:199], v[96:99], v[176:191]\n"
"ds_read_b64_tr_b16 a[212:213], v21 offset:12288\n"
"ds_read_b64_tr_b16 a[214:215], v21 offset:12800\n"
"buffer_store_dwordx4 v[192:195], v12, s[56:59], s71 offen sc1\n"
"s_waitcnt lgkmcnt(6)\n"
"v_mfma_f32_32x32x16_bf16 v[160:175], a[200:203], v[100:103], v[160:175]\n"
"ds_read_b64_tr_b16 a[216:217], v20 offset:14336\n"
"ds_read_b64_tr_b16 a[218:219], v20 offset:14848\n"
"v_cvt_pk_bf16_f32 v196, v40, v41\n"
"s_waitcnt lgkmcnt(6)\n"
"v_mfma_f32_32x32x16_bf16 v[176:191], a[204:207], v[100:103], v[176:191]\n"
"ds_read_b64_tr_b16 a[220:221], v21 offset:14336\n"
"ds_read_b64_tr_b16 a[222:223], v21 offset:14848\n"
"v_cvt_pk_bf16_f32 v197, v42, v43\n"
"s_waitcnt lgkmcnt(6)\n"
"v_mfma_f32_32x32x16_bf16 v[160:175], a[208:211], v[104:107], v[160:175]\n"
"ds_read_b64_tr_b16 a[192:193], v20 offset:16384\n"
"ds_read_b64_tr_b16 a[194:195], v20 offset:16896\n"
"v_cvt_pk_bf16_f32 v198, v44, v45\n"
"s_waitcnt lgkmcnt(6)\n"
"v_mfma_f32_32x32x16_bf16 v[176:191], a[212:215], v[104:107], v[176:191]\n"
"ds_read_b64_tr_b16 a[196:197], v21 offset:16384\n"
"ds_read_b64_tr_b16 a[198:199], v21 offset:16896\n"
"v_cvt_pk_bf16_f32 v199, v46, v47\n"
"s_waitcnt lgkmcnt(6)\n"
"v_mfma_f32_32x32x16_bf16 v[160:175], a[216:219], v[108:111], v[160:175]\n"
"ds_read_b64_tr_b16 a[200:201], v20 offset:18432\n"
"ds_read_b64_tr_b16 a[202:203], v20 offset:18944\n"
"s_add_u32 s71, s62, 69632\n"
"s_waitcnt lgkmcnt(6)\n"
"v_mfma_f32_32x32x16_bf16 v[176:191], a[220:223], v[108:111], v[176:191]\n"
"ds_read_b64_tr_b16 a[204:205], v21 offset:18432\n"
"ds_read_b64_tr_b16 a[206:207], v21 offset:18944\n"
"buffer_store_dwordx4 v[196:199], v12, s[56:59], s71 offen sc1\n"
"s_waitcnt lgkmcnt(6)\n"
"v_mfma_f32_32x32x16_bf16 v[160:175], a[192:195], v[112:115], v[160:175]\n"
"ds_read_b64_tr_b16 a[208:209], v20 offset:20480\n"
"ds_read_b64_tr_b16 a[210:211], v20 offset:20992\n"
"v_cvt_pk_bf16_f32 v200, v48, v49\n"
"s_waitcnt lgkmcnt(6)\n"
"v_mfma_f32_32x32x16_bf16 v[176:191], a[196:199], v[112:115], v[176:191]\n"
"ds_read_b64_tr_b16 a[212:213], v21 offset:20480\n"
"ds_read_b64_tr_b16 a[214:215], v21 offset:20992\n"
"v_cvt_pk_bf16_f32 v201, v50, v51\n"
"s_waitcnt lgkmcnt(6)\n"
"v_mfma_f32_32x32x16_bf16 v[160:175], a[200:203], v[116:119], v[160:175]\n"
"ds_read_b64_tr_b16 a[216:217], v20 offset:22528\n"
"ds_read_b64_tr_b16 a[218:219], v20 offset:23040\n"
"v_cvt_pk_bf16_f32 v202, v52, v53\n"
"s_waitcnt lgkmcnt(6)\n"
"v_mfma_f32_32x32x16_bf16 v[176:191], a[204:207], v[116:119], v[176:191]\n"
"ds_read_b64_tr_b16 a[220:221], v21 offset:22528\n"
"ds_read_b64_tr_b16 a[222:223], v21 offset:23040\n"
"v_cvt_pk_bf16_f32 v203, v54, v55\n"
"s_waitcnt lgkmcnt(6)\n"
"v_mfma_f32_32x32x16_bf16 v[160:175], a[208:211], v[120:123], v[160:175]\n"
"ds_read_b64_tr_b16 a[192:193], v20 offset:24576\n"
"ds_read_b64_tr_b16 a[194:195], v20 offset:25088\n"
"s_add_u32 s71, s62, 73728\n"
"s_waitcnt lgkmcnt(6)\n"
"v_mfma_f32_32x32x16_bf16 v[176:191], a[212:215], v[120:123], v[176:191]\n"
"ds_read_b64_tr_b16 a[196:197], v21 offset:24576\n"
"ds_read_b64_tr_b16 a[198:199], v21 offset:25088\n"
"buffer_store_dwordx4 v[200:203], v12, s[56:59], s71 offen sc1\n"
"s_waitcnt lgkmcnt(6)\n"
"v_mfma_f32_32x32x16_bf16 v[160:175], a[216:219], v[124:127], v[160:175]\n"
"ds_read_b64_tr_b16 a[200:201], v20 offset:26624\n"
"ds_read_b64_tr_b16 a[202:203], v20 offset:27136\n"
"v_cvt_pk_bf16_f32 v204, v56, v57\n"
"s_waitcnt lgkmcnt(6)\n"
"v_mfma_f32_32x32x16_bf16 v[176:191], a[220:223], v[124:127], v[176:191]\n"
"ds_read_b64_tr_b16 a[204:205], v21 offset:26624\n"
"ds_read_b64_tr_b16 a[206:207], v21 offset:27136\n"
"v_cvt_pk_bf16_f32 v205, v58, v59\n"
"s_waitcnt lgkmcnt(6)\n"
"v_mfma_f32_32x32x16_bf16 v[160:175], a[192:195], v[128:131], v[160:175]\n"
"ds_read_b64_tr_b16 a[208:209], v20 offset:28672\n"
"ds_read_b64_tr_b16 a[210:211], v20 offset:29184\n"
"v_cvt_pk_bf16_f32 v206, v60, v61\n"
"s_waitcnt lgkmcnt(6)\n"
"v_mfma_f32_32x32x16_bf16 v[176:191], a[196:199], v[128:131], v[176:191]\n"
"ds_read_b64_tr_b16 a[212:213], v21 offset:28672\n"
"ds_read_b64_tr_b16 a[214:215], v21 offset:29184\n"
"v_cvt_pk_bf16_f32 v207, v62, v63\n"
"s_waitcnt lgkmcnt(6)\n"
"v_mfma_f32_32x32x16_bf16 v[160:175], a[200:203], v[132:135], v[160:175]\n"
"ds_read_b64_tr_b16 a[216:217], v20 offset:30720\n"
"ds_read_b64_tr_b16 a[218:219], v20 offset:31232\n"
"s_add_u32 s71, s62, 77824\n"
"s_waitcnt lgkmcnt(6)\n"
"v_mfma_f32_32x32x16_bf16 v[176:191], a[204:207], v[132:135], v[176:191]\n"
"ds_read_b64_tr_b16 a[220:221], v21 offset:30720\n"
"ds_read_b64_tr_b16 a[222:223], v21 offset:31232\n"
"v_mov_b32 v20, v7\n"
"v_xor_b32 v21, 64, v20\n"
"buffer_store_dwordx4 v[204:207], v12, s[56:59], s71 offen sc1\n"
"s_waitcnt lgkmcnt(6)\n"
"v_mfma_f32_32x32x16_bf16 v[160:175], a[208:211], v[136:139], v[160:175]\n"
"ds_read_b64_tr_b16 a[192:193], v20 offset:0\n"
"ds_read_b64_tr_b16 a[194:195], v20 offset:512\n"
"s_waitcnt lgkmcnt(6)\n"
"v_mfma_f32_32x32x16_bf16 v[176:191], a[212:215], v[136:139], v[176:191]\n"
"ds_read_b64_tr_b16 a[196:197], v21 offset:0\n"
"ds_read_b64_tr_b16 a[198:199], v21 offset:512\n"
"s_waitcnt lgkmcnt(6)\n"
"v_mfma_f32_32x32x16_bf16 v[160:175], a[216:219], v[140:143], v[160:175]\n"
"ds_read_b64_tr_b16 a[200:201], v20 offset:2048\n"
"ds_read_b64_tr_b16 a[202:203], v20 offset:2560\n"
"s_waitcnt lgkmcnt(6)\n"
"v_mfma_f32_32x32x16_bf16 v[176:191], a[220:223], v[140:143], v[176:191]\n"
"ds_read_b64_tr_b16 a[204:205], v21 offset:2048\n"
"ds_read_b64_tr_b16 a[206:207], v21 offset:2560\n"
"s_waitcnt lgkmcnt(6)\n"
"v_mfma_f32_32x32x16_bf16 v[32:47], a[192:195], v[80:83], 0\n"
"s_waitcnt vmcnt(15)\n"
"s_barrier\n"
"ds_read_b64_tr_b16 a[208:209], v20 offset:4096\n"
"ds_read_b64_tr_b16 a[210:211], v20 offset:4608\n"
"s_waitcnt lgkmcnt(6)\n"
"v_mfma_f32_32x32x16_bf16 v[48:63], a[196:199], v[80:83], 0\n"
"ds_read_b64_tr_b16 a[212:213], v21 offset:4096\n"
"ds_read_b64_tr_b16 a[214:215], v21 offset:4608\n"
"s_waitcnt lgkmcnt(6)\n"
"v_mfma_f32_32x32x16_bf16 v[32:47], a[200:203], v[84:87], v[32:47]\n"
"ds_read_b64_tr_b16 a[216:217], v20 offset:6144\n"
"ds_read_b64_tr_b16 a[218:219], v20 offset:6656\n"
"s_waitcnt lgkmcnt(6)\n"
"v_mfma_f32_32x32x16_bf16 v[48:63], a[204:207], v[84:87], v[48:63]\n"
"ds_read_b64_tr_b16 a[220:221], v21 offset:6144\n"
"ds_read_b64_tr_b16 a[222:223], v21 offset:6656\n"
"s_waitcnt lgkmcnt(6)\n"
"v_mfma_f32_32x32x16_bf16 v[32:47], a[208:211], v[88:91], v[32:47]\n"
"ds_read_b64_tr_b16 a[192:193], v20 offset:8192\n"
"ds_read_b64_tr_b16 a[194:195], v20 offset:8704\n"
"v_cvt_pk_bf16_f32 v208, v160, v161\n"
"s_waitcnt lgkmcnt(6)\n"
"v_mfma_f32_32x32x16_bf16 v[48:63], a[212:215], v[88:91], v[48:63]\n"
"ds_read_b64_tr_b16 a[196:197], v21 offset:8192\n"
"ds_read_b64_tr_b16 a[198:199], v21 offset:8704\n"
"v_cvt_pk_bf16_f32 v209, v162, v163\n"
"s_waitcnt lgkmcnt(6)\n"
"v_mfma_f32_32x32x16_bf16 v[32:47], a[216:219], v[92:95], v[32:47]\n"
"ds_read_b64_tr_b16 a[200:201], v20 offset:10240\n"
"ds_read_b64_tr_b16 a[202:203], v20 offset:10752\n"
"v_cvt_pk_bf16_f32 v210, v164, v165\n"
"s_waitcnt lgkmcnt(6)\n"
"v_mfma_f32_32x32x16_bf16 v[48:63], a[220:223], v[92:95], v[48:63]\n"
"ds_read_b64_tr_b16 a[204:205], v21 offset:10240\n"
"ds_read_b64_tr_b16 a[206:207], v21 offset:10752\n"
"v_cvt_pk_bf16_f32 v211, v166, v167\n"
"s_waitcnt lgkmcnt(6)\n"
"v_mfma_f32_32x32x16_bf16 v[32:47], a[192:195], v[96:99], v[32:47]\n"
"ds_read_b64_tr_b16 a[208:209], v20 offset:12288\n"
"ds_read_b64_tr_b16 a[210:211], v20 offset:12800\n"
"s_add_u32 s71, s62, 81920\n"
"s_waitcnt lgkmcnt(6)\n"
"v_mfma_f32_32x32x16_bf16 v[48:63], a[196:199], v[96:99], v[48:63]\n"
"ds_read_b64_tr_b16 a[212:213], v21 offset:12288\n"
"ds_read_b64_tr_b16 a[214:215], v21 offset:12800\n"
"buffer_store_dwordx4 v[208:211], v12, s[56:59], s71 offen sc1\n"
"s_waitcnt lgkmcnt(6)\n"
"v_mfma_f32_32x32x16_bf16 v[32:47], a[200:203], v[100:103], v[32:47]\n"
"ds_read_b64_tr_b16 a[216:217], v20 offset:14336\n"
"ds_read_b64_tr_b16 a[218:219], v20 offset:14848\n"
"v_cvt_pk_bf16_f32 v212, v168, v169\n"
"s_waitcnt lgkmcnt(6)\n"
"v_mfma_f32_32x32x16_bf16 v[48:63], a[204:207], v[100:103], v[48:63]\n"
"ds_read_b64_tr_b16 a[220:221], v21 offset:14336\n"
"ds_read_b64_tr_b16 a[222:223], v21 offset:14848\n"
"v_cvt_pk_bf16_f32 v213, v170, v171\n"
"s_waitcnt lgkmcnt(6)\n"
"v_mfma_f32_32x32x16_bf16 v[32:47], a[208:211], v[104:107], v[32:47]\n"
"ds_read_b64_tr_b16 a[192:193], v20 offset:16384\n"
"ds_read_b64_tr_b16 a[194:195], v20 offset:16896\n"
"v_cvt_pk_bf16_f32 v214, v172, v173\n"
"s_waitcnt lgkmcnt(6)\n"
"v_mfma_f32_32x32x16_bf16 v[48:63], a[212:215], v[104:107], v[48:63]\n"
"ds_read_b64_tr_b16 a[196:197], v21 offset:16384\n"
"ds_read_b64_tr_b16 a[198:199], v21 offset:16896\n"
"v_cvt_pk_bf16_f32 v215, v174, v175\n"
"s_waitcnt lgkmcnt(6)\n"
"v_mfma_f32_32x32x16_bf16 v[32:47], a[216:219], v[108:111], v[32:47]\n"
"ds_read_b64_tr_b16 a[200:201], v20 offset:18432\n"
"ds_read_b64_tr_b16 a[202:203], v20 offset:18944\n"
"s_add_u32 s71, s62, 86016\n"
"s_waitcnt lgkmcnt(6)\n"
"v_mfma_f32_32x32x16_bf16 v[48:63], a[220:223], v[108:111], v[48:63]\n"
"ds_read_b64_tr_b16 a[204:205], v21 offset:18432\n"
"ds_read_b64_tr_b16 a[206:207], v21 offset:18944\n"
"buffer_store_dwordx4 v[212:215], v12, s[56:59], s71 offen sc1\n"
"s_waitcnt lgkmcnt(6)\n"
"v_mfma_f32_32x32x16_bf16 v[32:47], a[192:195], v[112:115], v[32:47]\n"
"ds_read_b64_tr_b16 a[208:209], v20 offset:20480\n"
"ds_read_b64_tr_b16 a[210:211], v20 offset:20992\n"
"v_cvt_pk_bf16_f32 v216, v176, v177\n"
"s_waitcnt lgkmcnt(6)\n"
"v_mfma_f32_32x32x16_bf16 v[48:63], a[196:199], v[112:115], v[48:63]\n"
"ds_read_b64_tr_b16 a[212:213], v21 offset:20480\n"
"ds_read_b64_tr_b16 a[214:215], v21 offset:20992\n"
"v_cvt_pk_bf16_f32 v217, v178, v179\n"
"s_waitcnt lgkmcnt(6)\n"
"v_mfma_f32_32x32x16_bf16 v[32:47], a[200:203], v[116:119], v[32:47]\n"
"ds_read_b64_tr_b16 a[216:217], v20 offset:22528\n"
"ds_read_b64_tr_b16 a[218:219], v20 offset:23040\n"
"v_cvt_pk_bf16_f32 v218, v180, v181\n"
"s_waitcnt lgkmcnt(6)\n"
"v_mfma_f32_32x32x16_bf16 v[48:63], a[204:207], v[116:119], v[48:63]\n"
"ds_read_b64_tr_b16 a[220:221], v21 offset:22528\n"
"ds_read_b64_tr_b16 a[222:223], v21 offset:23040\n"
"v_cvt_pk_bf16_f32 v219, v182, v183\n"
"s_waitcnt lgkmcnt(6)\n"
"v_mfma_f32_32x32x16_bf16 v[32:47], a[208:211], v[120:123], v[32:47]\n"
"ds_read_b64_tr_b16 a[192:193], v20 offset:24576\n"
"ds_read_b64_tr_b16 a[194:195], v20 offset:25088\n"
"s_add_u32 s71, s62, 90112\n"
"s_waitcnt lgkmcnt(6)\n"
"v_mfma_f32_32x32x16_bf16 v[48:63], a[212:215], v[120:123], v[48:63]\n"
"ds_read_b64_tr_b16 a[196:197], v21 offset:24576\n"
"ds_read_b64_tr_b16 a[198:199], v21 offset:25088\n"
"buffer_store_dwordx4 v[216:219], v12, s[56:59], s71 offen sc1\n"
"s_waitcnt lgkmcnt(6)\n"
"v_mfma_f32_32x32x16_bf16 v[32:47], a[216:219], v[124:127], v[32:47]\n"
"ds_read_b64_tr_b16 a[200:201], v20 offset:26624\n"
"ds_read_b64_tr_b16 a[202:203], v20 offset:27136\n"
"v_cvt_pk_bf16_f32 v220, v184, v185\n"
"s_waitcnt lgkmcnt(6)\n"
"v_mfma_f32_32x32x16_bf16 v[48:63], a[220:223], v[124:127], v[48:63]\n"
"ds_read_b64_tr_b16 a[204:205], v21 offset:26624\n"
"ds_read_b64_tr_b16 a[206:207], v21 offset:27136\n"
"v_cvt_pk_bf16_f32 v221, v186, v187\n"
"s_waitcnt lgkmcnt(6)\n"
"v_mfma_f32_32x32x16_bf16 v[32:47], a[192:195], v[128:131], v[32:47]\n"
"ds_read_b64_tr_b16 a[208:209], v20 offset:28672\n"
"ds_read_b64_tr_b16 a[210:211], v20 offset:29184\n"
"v_cvt_pk_bf16_f32 v222, v188, v189\n"
"s_waitcnt lgkmcnt(6)\n"
"v_mfma_f32_32x32x16_bf16 v[48:63], a[196:199], v[128:131], v[48:63]\n"
"ds_read_b64_tr_b16 a[212:213], v21 offset:28672\n"
"ds_read_b64_tr_b16 a[214:215], v21 offset:29184\n"
"v_cvt_pk_bf16_f32 v223, v190, v191\n"
"s_waitcnt lgkmcnt(6)\n"
"v_mfma_f32_32x32x16_bf16 v[32:47], a[200:203], v[132:135], v[32:47]\n"
"ds_read_b64_tr_b16 a[216:217], v20 offset:30720\n"
"ds_read_b64_tr_b16 a[218:219], v20 offset:31232\n"
"s_add_u32 s71, s62, 94208\n"
"s_waitcnt lgkmcnt(6)\n"
"v_mfma_f32_32x32x16_bf16 v[48:63], a[204:207], v[132:135], v[48:63]\n"
"ds_read_b64_tr_b16 a[220:221], v21 offset:30720\n"
"ds_read_b64_tr_b16 a[222:223], v21 offset:31232\n"
"buffer_store_dwordx4 v[220:223], v12, s[56:59], s71 offen sc1\n"
"s_waitcnt lgkmcnt(6)\n"
"v_mfma_f32_32x32x16_bf16 v[32:47], a[208:211], v[136:139], v[32:47]\n"
"s_waitcnt lgkmcnt(4)\n"
"v_mfma_f32_32x32x16_bf16 v[48:63], a[212:215], v[136:139], v[48:63]\n"
"s_waitcnt lgkmcnt(2)\n"
"v_mfma_f32_32x32x16_bf16 v[32:47], a[216:219], v[140:143], v[32:47]\n"
"s_waitcnt lgkmcnt(0)\n"
"v_mfma_f32_32x32x16_bf16 v[48:63], a[220:223], v[140:143], v[48:63]\n"
"s_nop 7\n"
"s_nop 3\n"
"v_cvt_pk_bf16_f32 v192, v32, v33\n"
"v_cvt_pk_bf16_f32 v193, v34, v35\n"
"v_cvt_pk_bf16_f32 v194, v36, v37\n"
"v_cvt_pk_bf16_f32 v195, v38, v39\n"
"s_add_u32 s71, s62, 98304\n"
"buffer_store_dwordx4 v[192:195], v12, s[56:59], s71 offen sc1\n"
"v_cvt_pk_bf16_f32 v196, v40, v41\n"
"v_cvt_pk_bf16_f32 v197, v42, v43\n"
"v_cvt_pk_bf16_f32 v198, v44, v45\n"
"v_cvt_pk_bf16_f32 v199, v46, v47\n"
"s_add_u32 s71, s62, 102400\n"
"buffer_store_dwordx4 v[196:199], v12, s[56:59], s71 offen sc1\n"
"v_cvt_pk_bf16_f32 v200, v48, v49\n"
"v_cvt_pk_bf16_f32 v201, v50, v51\n"
"v_cvt_pk_bf16_f32 v202, v52, v53\n"
"v_cvt_pk_bf16_f32 v203, v54, v55\n"
"s_add_u32 s71, s62, 106496\n"
"buffer_store_dwordx4 v[200:203], v12, s[56:59], s71 offen sc1\n"
"v_cvt_pk_bf16_f32 v204, v56, v57\n"
"v_cvt_pk_bf16_f32 v205, v58, v59\n"
"v_cvt_pk_bf16_f32 v206, v60, v61\n"
"v_cvt_pk_bf16_f32 v207, v62, v63\n"
"s_add_u32 s71, s62, 110592\n"
"buffer_store_dwordx4 v[204:207], v12, s[56:59], s71 offen sc1\n"
"s_nop 1\n"
        : "+{v2}"(vx), "+{v3}"(vwq), "+{v4}"(vk), "+{v5}"(vv), "+{v6}"(vwo), "+{v7}"(trb), "+{v8}"(kb),
          "+{v9}"(xw0), "+{v10}"(xw1), "+{v11}"(xb), "+{v12}"(slab_v), "+{v13}"(pfo), "+{v14}"(toff)
        : "{s[36:39]}"(rx), "{s[40:43]}"(rwq), "{s[44:47]}"(rk), "{s[48:51]}"(rv), "{s[52:55]}"(rwo), "{s[56:59]}"(rsl),
          "{s60}"(m0base), "{s61}"(sx), "{s62}"(slab_s),
          "{s64}"(kx[0]), "{s65}"(kx[1]), "{s66}"(kx[2]), "{s67}"(kx[3]), "{s68}"(kx[4]), "{s69}"(kx[5]), "{s70}"(kx[6]),
          "{s72}"(no[0]), "{s73}"(no[1]), "{s74}"(no[2]), "{s75}"(no[3]), "{s76}"(no[4]), "{s77}"(no[5]), "{s78}"(no[6])
        :
          "memory", "vcc", "scc", "v20", "v21", "v22", "v23", "v24", "v25", "v26", "v27", "v28", "v29", "v30", "v31", "v32",
          "v33", "v34", "v35", "v36", "v37", "v38", "v39", "v40", "v41", "v42", "v43", "v44", "v45", "v46", "v47", "v48",
          "v49", "v50", "v51", "v52", "v53", "v54", "v55", "v56", "v57", "v58", "v59", "v60", "v61", "v62", "v63", "v64",
          "v65", "v66", "v67", "v68", "v69", "v70", "v71", "v72", "v73", "v74", "v75", "v76", "v77", "v78", "v79", "v80",
          "v81", "v82", "v83", "v84", "v85", "v86", "v87", "v88", "v89", "v90", "v91", "v92", "v93", "v94", "v95", "v96",
          "v97", "v98", "v99", "v100", "v101", "v102", "v103", "v104", "v105", "v106", "v107", "v108", "v109", "v110", "v111", "v112",
          "v113", "v114", "v115", "v116", "v117", "v118", "v119", "v120", "v121", "v122", "v123", "v124", "v125", "v126", "v127", "v128",
          "v129", "v130", "v131", "v132", "v133", "v134", "v135", "v136", "v137", "v138", "v139", "v140", "v141", "v142", "v143", "v144",
          "v145", "v146", "v147", "v148", "v149", "v150", "v151", "v152", "v153", "v154", "v155", "v156", "v157", "v158", "v159", "v160",
          "v161", "v162", "v163", "v164", "v165", "v166", "v167", "v168", "v169", "v170", "v171", "v172", "v173", "v174", "v175", "v176",
          "v177", "v178", "v179", "v180", "v181", "v182", "v183", "v184", "v185", "v186", "v187", "v188", "v189", "v190", "v191", "v192",
          "v193", "v194", "v195", "v196", "v197", "v198", "v199", "v200", "v201", "v202", "v203", "v204", "v205", "v206", "v207", "v208",
          "v209", "v210", "v211", "v212", "v213", "v214", "v215", "v216", "v217", "v218", "v219", "v220", "v221", "v222", "v223", "v224",
          "v225", "v226", "v227", "v228", "v229", "v230", "v231", "v232", "v233", "v234", "v235", "v236", "v237", "v238", "v239", "v240",
          "v241", "v242", "v243", "v244", "v245", "v246", "v247", "v248", "v249", "v250", "v251", "v252", "v253", "v254", "v255", "a0",
          "a1", "a2", "a3", "a4", "a5", "a6", "a7", "a8", "a9", "a10", "a11", "a12", "a13", "a14", "a15", "a16",
          "a17", "a18", "a19", "a20", "a21", "a22", "a23", "a24", "a25", "a26", "a27", "a28", "a29", "a30", "a31", "a32",
          "a33", "a34", "a35", "a36", "a37", "a38", "a39", "a40", "a41", "a42", "a43", "a44", "a45", "a46", "a47", "a48",
          "a49", "a50", "a51", "a52", "a53", "a54", "a55", "a56", "a57", "a58", "a59", "a60", "a61", "a62", "a63", "a64",
          "a65", "a66", "a67", "a68", "a69", "a70", "a71", "a72", "a73", "a74", "a75", "a76", "a77", "a78", "a79", "a80",
          "a81", "a82", "a83", "a84", "a85", "a86", "a87", "a88", "a89", "a90", "a91", "a92", "a93", "a94", "a95", "a96",
          "a97", "a98", "a99", "a100", "a101", "a102", "a103", "a104", "a105", "a106", "a107", "a108", "a109", "a110", "a111", "a112",
          "a113", "a114", "a115", "a116", "a117", "a118", "a119", "a120", "a121", "a122", "a123", "a124", "a125", "a126", "a127", "a128",
          "a129", "a130", "a131", "a132", "a133", "a134", "a135", "a136", "a137", "a138", "a139", "a140", "a141", "a142", "a143", "a144",
          "a145", "a146", "a147", "a148", "a149", "a150", "a151", "a152", "a153", "a154", "a155", "a156", "a157", "a158", "a159", "a160",
          "a161", "a162", "a163", "a164", "a165", "a166", "a167", "a168", "a169", "a170", "a171", "a172", "a173", "a174", "a175", "a176",
          "a177", "a178", "a179", "a180", "a181", "a182", "a183", "a184", "a185", "a186", "a187", "a188", "a189", "a190", "a191", "a192",
          "a193", "a194", "a195", "a196", "a197", "a198", "a199", "a200", "a201", "a202", "a203", "a204", "a205", "a206", "a207", "a208",
          "a209", "a210", "a211", "a212", "a213", "a214", "a215", "a216", "a217", "a218", "a219", "a220", "a221", "a222", "a223", "a224",
          "a225", "a226", "a227", "a228", "a229", "a230", "a231", "a232", "a233", "a234", "a235", "a236", "a237", "a238", "a239", "a240",
          "a241", "a242", "a243", "a244", "a245", "a246", "a247", "a248", "a249", "a250", "a251", "a252", "a253", "a254", "a255", "s71",
          "s79", "s80", "s81", "s82", "s83", "s84", "s85", "s86", "s87", "s88", "s89", "s90", "s91", "s92", "s93", "s94",
          "s95", "s96", "s97", "s98", "s99");
    }

    const rsrc_t rslab = __builtin_amdgcn_make_buffer_rsrc((void*)slabp, 0, NH * SLAB_BYTES, 0x00020000);
    typedef GAS unsigned long long gu64;
    gu64* epoch_p = (gu64*)(ws + grp * 256u);
    gu64* flags = (gu64*)(ws + 4096u + grp * (16u * 128u));
    const unsigned long long ep = __hip_atomic_load(epoch_p, __ATOMIC_RELAXED, __HIP_MEMORY_SCOPE_AGENT);
    const unsigned long long tag = (ep + 1ull) * 0x9E3779B97F4A7C15ull + 0x632BE59BD9B4E019ull;
    asm volatile("s_waitcnt vmcnt(0)\n\ts_barrier" ::: "memory");
    STAMP(37);
    if (w == 0) {
        if (lane == 0) __hip_atomic_store(flags + hh * 16u, tag, __ATOMIC_RELAXED, __HIP_MEMORY_SCOPE_AGENT);
        for (unsigned spins = 0; spins < (1u << 22); ++spins) {
            const unsigned long long f = __hip_atomic_load(flags + (lane & 15u) * 16u, __ATOMIC_RELAXED, __HIP_MEMORY_SCOPE_AGENT);
            if (__all(f == tag)) break;
            __builtin_amdgcn_s_sleep(1);
        }
        if (hh == 0 && lane == 0) __hip_atomic_store(epoch_p, ep + 1ull, __ATOMIC_RELAXED, __HIP_MEMORY_SCOPE_AGENT);
    }
    asm volatile("s_barrier" ::: "memory");
    STAMP(38);

    const unsigned rd_v = ((lane >> 4) * 4u + (hh >> 2)) * 1024u + (hh & 3u) * 256u + (lane & 15u) * 16u;
    char* outp = out + ((size_t)(bb * SQ + 8u * hh + ((lane & 15u) >> 1)) * (2 * DM) + br * DM + (lane >> 4) * 16u + (lane & 1u) * 8u) * 2;
    u32x4 v[2][16];
#pragma unroll
    for (int uu = 0; uu < 2; ++uu) {
        const unsigned u = w + 4u * uu;
        const unsigned uo = u < 7u ? u * 16384u : 0x7ff00000u;
#pragma unroll
        for (int s = 0; s < 16; ++s)
            v[uu][s] = __builtin_amdgcn_raw_buffer_load_b128(rslab, rd_v, s * SLAB_BYTES + uo, 16);
    }
#pragma unroll
    for (int uu = 0; uu < 2; ++uu) {
        const unsigned u = w + 4u * uu;
        float sum[8];
#pragma unroll
        for (int e = 0; e < 8; ++e) sum[e] = 0.f;
#pragma unroll
        for (int s = 0; s < 16; ++s)
#pragma unroll
            for (int e = 0; e < 4; ++e) { sum[2 * e] += bf_lo(v[uu][s][e]); sum[2 * e + 1] += bf_hi(v[uu][s][e]); }
        u32x4 o;
#pragma unroll
        for (int e = 0; e < 4; ++e) o[e] = cvtpk(sum[2 * e], sum[2 * e + 1]);
        if (u < 7u) *(u32x4*)(outp + ((u + bb) % 7u) * 128u) = o;
    }
    asm volatile(".Lpf_end:");
}

extern "C" void kernel_launch(void* const* d_in, const int* in_sizes, int n_in,
                              void* d_out, int out_size, void* d_ws, size_t ws_size,
                              hipStream_t stream) {
    (void)in_sizes; (void)n_in; (void)out_size; (void)ws_size;
    fused_attn_block<<<256, 256, 0, stream>>>(
        (const char*)d_in[0], (const char*)d_in[1], (const char*)d_in[2], (const char*)d_in[3], (const char*)d_in[4],
        (const char*)d_in[5], (const char*)d_in[6], (const char*)d_in[7], (const char*)d_in[8], (const char*)d_in[9],
        (char*)d_out, (char*)d_ws);
}
```
